```python
import torch

def _softmax(x, axis=-1):
    return torch.softmax(x, dim=axis)
def _gelu(x):
    return torch.nn.functional.gelu(x, approximate="tanh")
def _silu(x):
    return torch.nn.functional.silu(x)
def _sigmoid(x):
    return torch.sigmoid(x)
def _layernorm(x, axis=-1, eps=1e-5):
    m = x.mean(dim=axis, keepdim=True)
    v = x.var(dim=axis, keepdim=True, unbiased=False)
    return (x - m) / torch.sqrt(v + eps)
def _scatter_add(tgt, idx, src, axis=0):
    return tgt.clone().index_add_(axis, idx, src)
def _segment_sum(src, seg, n):
    out = torch.zeros((n,) + src.shape[1:], dtype=src.dtype)
    return out.index_add_(0, seg, src)
def _pad(x, pw):
    flat = tuple(v for pair in reversed(pw) for v in pair)
    return torch.nn.functional.pad(x, flat)

kernel_name = 'interleave_layers__gqa_block_cached__dense_activation_silu__decode'

def setup_inputs():

    _w = (lambda a: (lambda t: t.to(torch.bfloat16) if t.is_floating_point() else t)(torch.as_tensor(a)))
    def _A_setup():
        B, Sq, D = 2, 1, 640
        Hq, Hkv, Sk, Dh = 32, 32, 8192, 128
        assert Hq % Hkv == 0, (Hq, Hkv)
        return {
            "x": _w((lambda *s: torch.randn(*s, dtype=torch.bfloat16) * 0.1)(B, Sq, D)),
            "k_cache": _w((lambda *s: torch.randn(*s, dtype=torch.bfloat16) * 1.6)(B, Hkv, Sk, Dh)),
            "v_cache": _w((lambda *s: torch.randn(*s, dtype=torch.bfloat16) * 3.2)(B, Hkv, Sk, Dh)),
            "wq": _w((lambda *s: torch.randn(*s, dtype=torch.bfloat16) * 1.6)(D, Hq * Dh) * (D ** -0.5)),
            "wo": _w((lambda *s: torch.randn(*s, dtype=torch.bfloat16) * 3.2)(Hq * Dh, D) * ((Hq * Dh) ** -0.5)),
        }
    _A_in = _A_setup()

    def _B_setup():
        B, S, D, F = 2, 1, 640, 1728
        return {
            "x": _w((lambda *s: torch.randn(*s, dtype=torch.bfloat16) * 0.1)(B, S, D)),
            "w1": _w((lambda *s: torch.randn(*s, dtype=torch.bfloat16) * 3.2)(D, F) * (D ** -0.5)),
            "w2": _w((lambda *s: torch.randn(*s, dtype=torch.bfloat16) * 0.8)(F, D) * (F ** -0.5)),
        }
    _B_in = _B_setup()

    return {
        "A_x": _A_in["x"],
        "A_k_cache": _A_in["k_cache"],
        "A_v_cache": _A_in["v_cache"],
        "A_wq": _A_in["wq"],
        "A_wo": _A_in["wo"],
        "B_x": _B_in["x"],
        "B_w1": _B_in["w1"],
        "B_w2": _B_in["w2"],
    }

def reference(A_x, A_k_cache, A_v_cache, A_wq, A_wo, B_x, B_w1, B_w2):
    def _A_ref(x, k_cache, v_cache, wq, wo):
        B, Sq, D = x.shape
        Hq, Dh = 32, 128
        Hkv = k_cache.shape[1]
        rep = Hq // Hkv
        q = (lambda x, *d: x.permute(*d))((x @ wq).reshape(B, Sq, Hq, Dh), 0, 2, 1, 3)
        k = (lambda x, n, axis: torch.repeat_interleave(x, n, dim=axis))(k_cache, rep, axis=1)
        v = (lambda x, n, axis: torch.repeat_interleave(x, n, dim=axis))(v_cache, rep, axis=1)
        scores = torch.einsum("bhqd,bhkd->bhqk", q, k) * (Dh ** -0.5)
        p = _softmax(scores, axis=-1)
        o = torch.einsum("bhqk,bhkd->bhqd", p, v)
        return (lambda x, *d: x.permute(*d))(o, 0, 2, 1, 3).reshape(B, Sq, Hq * Dh) @ wo

    def _B_ref(x, w1, w2):
        h = torch.einsum("bsd,df->bsf", x, w1)
        h = _silu(h)
        out = torch.einsum("bsf,fd->bsd", h, w2)
        return out

    x = A_x
    x = x + _A_ref(_layernorm(x, axis=-1), A_k_cache, A_v_cache, A_wq, A_wo)
    x = x + _B_ref(_layernorm(x, axis=-1), B_w1, B_w2)
    x = x + _A_ref(_layernorm(x, axis=-1), A_k_cache, A_v_cache, A_wq, A_wo)
    x = x + _B_ref(_layernorm(x, axis=-1), B_w1, B_w2)
    return x
```

```cpp
#include <hip/hip_runtime.h>
#include <hip/hip_bf16.h>
#include <stdint.h>

#define MIMIC 1

constexpr int B = 2, D = 640, H = 32, SK = 8192, DH = 128, F = 1728;
constexpr int NSPLIT = 8;
typedef unsigned short u16;
typedef unsigned u32x4 __attribute__((ext_vector_type(4)));

__device__ __forceinline__ float bf2f(u16 v) { return __uint_as_float((unsigned)v << 16); }
__device__ __forceinline__ u16 f2bf(float f) { __hip_bfloat16 h = __float2bfloat16(f); return *(u16*)&h; }
__device__ __forceinline__ float rb(float f) { return MIMIC ? bf2f(f2bf(f)) : f; }

__device__ float block_sum(float v, float* red) {
  for (int o = 32; o > 0; o >>= 1) v += __shfl_xor(v, o);
  __syncthreads();
  if ((threadIdx.x & 63) == 0) red[threadIdx.x >> 6] = v;
  __syncthreads();
  float s = 0; for (int i = 0; i < (int)blockDim.x / 64; i++) s += red[i];
  return s;
}
__device__ void ln_to_shared(const float* x, float* xs, float* red) {
  float s = 0; for (int i = threadIdx.x; i < D; i += blockDim.x) s += x[i];
  float mean = block_sum(s, red) / D;
  float s2 = 0; for (int i = threadIdx.x; i < D; i += blockDim.x) { float d = x[i] - mean; s2 += d * d; }
  float var = block_sum(s2, red) / D;
  float m = rb(mean), v = rb(var);
  float den = rb(sqrtf(rb(v + 1e-5f)));
  for (int i = threadIdx.x; i < D; i += blockDim.x) xs[i] = rb(rb(x[i] - m) / den);
  __syncthreads();
}
__global__ void k_init(const u16* x, float* xf) { int i = blockIdx.x * blockDim.x + threadIdx.x; if (i < B * D) xf[i] = bf2f(x[i]); }
template <int ACT> __global__ void k_ln_gemv(const float* x, const u16* w, float* out, int N) {
  __shared__ float xs[D]; __shared__ float red[16]; __shared__ float part[4][64];
  int b = blockIdx.y; ln_to_shared(x + b * D, xs, red);
  int c = blockIdx.x * 64 + (threadIdx.x & 63), kq = threadIdx.x >> 6;
  float acc = 0; for (int k = kq * (D / 4); k < (kq + 1) * (D / 4); k++) acc += xs[k] * bf2f(w[(size_t)k * N + c]);
  part[kq][threadIdx.x & 63] = acc; __syncthreads();
  if (kq == 0) { float s = part[0][threadIdx.x] + part[1][threadIdx.x] + part[2][threadIdx.x] + part[3][threadIdx.x];
    s = rb(s); if (ACT) s = rb(s / (1.f + __expf(-s))); out[b * N + c] = s; }
}
__global__ void k_gemv_res(const float* in, const u16* w, float* x, u16* out, int K) {
  __shared__ float part[16][64];
  int b = blockIdx.y, c = blockIdx.x * 64 + (threadIdx.x & 63), kq = threadIdx.x >> 6;
  float acc = 0; for (int k = kq * (K / 16); k < (kq + 1) * (K / 16); k++) acc += in[b * K + k] * bf2f(w[(size_t)k * D + c]);
  part[kq][threadIdx.x & 63] = acc; __syncthreads();
  if (kq == 0) { float s = 0; for (int i = 0; i < 16; i++) s += part[i][threadIdx.x];
    s = rb(x[b * D + c] + rb(s)); x[b * D + c] = s; if (out) out[b * D + c] = f2bf(s); }
}
__global__ void __launch_bounds__(256) k_attn(const float* q, const u16* kc, const u16* vc, float* part) {
  __shared__ float mrg[4][4][132];
  int s = blockIdx.x, h = blockIdx.y, b = blockIdx.z, lane = threadIdx.x & 63, w = threadIdx.x >> 6, g = lane >> 4, c = lane & 15;
  constexpr int KP = SK / NSPLIT;
  size_t base = ((size_t)(b * H + h) * SK + s * KP) * DH;
  float qf[8]; for (int j = 0; j < 8; j++) qf[j] = q[b * H * DH + h * DH + 8 * c + j];
  float m = -INFINITY, l = 0, acc[8] = {0};
  for (int k0 = w * 16; k0 < KP; k0 += 64) {
    u32x4 kk[4], vv[4];
    for (int t = 0; t < 4; t++) { size_t o = base + (size_t)(k0 + 4 * t + g) * DH + 8 * c;
      kk[t] = __builtin_nontemporal_load((const u32x4*)(kc + o)); vv[t] = __builtin_nontemporal_load((const u32x4*)(vc + o)); }
    float sc[4];
    for (int t = 0; t < 4; t++) { float d = 0;
      for (int j = 0; j < 4; j++) { d += qf[2 * j] * __uint_as_float(kk[t][j] << 16); d += qf[2 * j + 1] * __uint_as_float(kk[t][j] & 0xffff0000u); }
      for (int o = 1; o < 16; o <<= 1) d += __shfl_xor(d, o);
      sc[t] = rb(rb(d) * rb(0.08838834764831845f)); if (!MIMIC) sc[t] = d * 0.08838834764831845f; }
    float mn = fmaxf(fmaxf(fmaxf(sc[0], sc[1]), fmaxf(sc[2], sc[3])), m);
    float al = __expf(m - mn); l *= al; for (int j = 0; j < 8; j++) acc[j] *= al; m = mn;
    for (int t = 0; t < 4; t++) { float p = __expf(sc[t] - m); l += p;
      for (int j = 0; j < 4; j++) { acc[2 * j] += p * __uint_as_float(vv[t][j] << 16); acc[2 * j + 1] += p * __uint_as_float(vv[t][j] & 0xffff0000u); } }
  }
  for (int j = 0; j < 8; j++) mrg[w][g][4 + 8 * c + j] = acc[j];
  if (c == 0) { mrg[w][g][0] = m; mrg[w][g][1] = l; }
  __syncthreads();
  if (threadIdx.x < 128) { float M = -INFINITY; for (int i = 0; i < 16; i++) M = fmaxf(M, mrg[i >> 2][i & 3][0]);
    float L = 0, o = 0; for (int i = 0; i < 16; i++) { float e = __expf(mrg[i >> 2][i & 3][0] - M); L += e * mrg[i >> 2][i & 3][1]; o += e * mrg[i >> 2][i & 3][4 + threadIdx.x]; }
    float* p = part + ((size_t)(b * H + h) * NSPLIT + s) * 132; p[4 + threadIdx.x] = o; if (threadIdx.x == 0) { p[0] = M; p[1] = L; } }
}
__global__ void k_comb(const float* part, float* o) {
  const float* p = part + (size_t)blockIdx.x * NSPLIT * 132; float M = -INFINITY;
  for (int i = 0; i < NSPLIT; i++) M = fmaxf(M, p[i * 132]);
  float L = 0, a = 0; for (int i = 0; i < NSPLIT; i++) { float e = __expf(p[i * 132] - M); L += e * p[i * 132 + 1]; a += e * p[i * 132 + 4 + threadIdx.x]; }
  o[blockIdx.x * DH + threadIdx.x] = rb(a / L);
}

extern "C" void kernel_launch(void* const* d_in, const int* in_sizes, int n_in, void* d_out, int out_size, void* d_ws, size_t ws_size, hipStream_t stream) {
  const u16* x = (const u16*)d_in[0]; const u16* kc = (const u16*)d_in[1]; const u16* vc = (const u16*)d_in[2];
  const u16* wq = (const u16*)d_in[3]; const u16* wo = (const u16*)d_in[4]; const u16* w1 = (const u16*)d_in[6]; const u16* w2 = (const u16*)d_in[7];
  float* ws = (float*)d_ws; float* xf = ws; float* q = ws + 2048; float* o = q + 8192; float* hh = o + 8192; float* part = hh + 4096;
  k_init<<<5, 256, 0, stream>>>(x, xf);
  for (int L = 0; L < 2; L++) {
    k_ln_gemv<0><<<dim3(H * DH / 64, B), 256, 0, stream>>>(xf, wq, q, H * DH);
    k_attn<<<dim3(NSPLIT, H, B), 256, 0, stream>>>(q, kc, vc, part);
    k_comb<<<B * H, 128, 0, stream>>>(part, o);
    k_gemv_res<<<dim3(D / 64, B), 1024, 0, stream>>>(o, wo, xf, nullptr, H * DH);
    k_ln_gemv<1><<<dim3(F / 64, B), 256, 0, stream>>>(xf, w1, hh, F);
    k_gemv_res<<<dim3(D / 64, B), 1024, 0, stream>>>(hh, w2, xf, L == 1 ? (u16*)d_out : nullptr, F);
  }
}
```

```cpp
#include <hip/hip_runtime.h>

__global__ void __launch_bounds__(256, 1) fused_decode_kernel(const void* x, const void* kc, const void* vc, const void* wq, const void* wo,
                                                              const void* w1, const void* w2, void* out, void* ws) {
  __shared__ __attribute__((aligned(16))) char lds[163840];
  const unsigned wave = __builtin_amdgcn_readfirstlane(threadIdx.x >> 6);
  asm volatile(
"s_memrealtime s[94:95]\n"
"s_load_dwordx16 s[16:31], %0, 0\n"
"s_load_dwordx2 s[8:9], %0, 64\n"
"s_mov_b32 s14, %1\n"
"s_mov_b32 s15, %2\n"
"v_mbcnt_lo_u32_b32 v47, -1, 0\n"
"v_mbcnt_hi_u32_b32 v47, -1, v47\n"
"s_cmp_lt_u32 s14, 32\n"
"s_cbranch_scc1 .Lk_chain\n"
"s_sub_u32 s88, s14, 32\n"
"s_and_b32 s12, s88, 31\n"
"s_lshr_b32 s13, s88, 5\n"
"s_lshl_b32 s89, s13, 2\n"
"s_add_u32 s89, s89, s15\n"
"s_mul_i32 s90, s89, 73\n"
"s_max_u32 s91, s89, 24\n"
"s_sub_u32 s91, s91, 24\n"
"s_add_u32 s90, s90, s91\n"
"s_lshl_b32 s90, s90, 10\n"
"s_lshl_b32 s91, s12, 21\n"
"s_add_u32 s90, s90, s91\n"
"s_mov_b32 s92, 74752\n"
"s_mov_b32 s84, 0x555555ff\n"
"s_mov_b32 s85, 0x55555555\n"
"s_cmp_ge_u32 s89, 24\n"
"s_cmov_b32 s92, 75776\n"
"s_cmov_b32 s84, 0x5555ffff\n"
"v_lshlrev_b32 v39, 4, v47\n"
"v_lshlrev_b32 v28, 5, v47\n"
"s_mov_b32 s56, 0\n"
"s_mov_b32 s57, 4096\n"
"s_mov_b32 s58, 8192\n"
"s_waitcnt lgkmcnt(0)\n"
"s_add_u32 s40, s20, s90\n"
"s_addc_u32 s41, s21, 0\n"
"s_and_b32 s41, s41, 0xffff\n"
"s_mov_b32 s42, s92\n"
"s_mov_b32 s43, 0x20000\n"
"buffer_load_dwordx4 a[0:3], v39, s[40:43], s56 offen offset:0 nt\n"
"buffer_load_dwordx4 a[4:7], v39, s[40:43], s56 offen offset:1024 nt\n"
"buffer_load_dword v29, v28, s[40:43], s56 offen offset:0 sc1\n"
"buffer_load_dwordx4 a[8:11], v39, s[40:43], s56 offen offset:2048 nt\n"
"buffer_load_dwordx4 a[12:15], v39, s[40:43], s56 offen offset:3072 nt\n"
"buffer_load_dword v29, v28, s[40:43], s56 offen offset:2048 sc1\n"
"buffer_load_dwordx4 a[16:19], v39, s[40:43], s57 offen offset:0 nt\n"
"buffer_load_dwordx4 a[20:23], v39, s[40:43], s57 offen offset:1024 nt\n"
"buffer_load_dword v29, v28, s[40:43], s57 offen offset:0 sc1\n"
"buffer_load_dwordx4 a[24:27], v39, s[40:43], s57 offen offset:2048 nt\n"
"buffer_load_dwordx4 a[28:31], v39, s[40:43], s57 offen offset:3072 nt\n"
"buffer_load_dword v29, v28, s[40:43], s57 offen offset:2048 sc1\n"
"s_mov_b32 s59, 12288\n"
"s_mov_b32 s60, 16384\n"
"s_mov_b32 s61, 20480\n"
"s_mov_b32 s62, 24576\n"
"s_mov_b32 s63, 28672\n"
"s_mov_b32 s64, 32768\n"
"s_mov_b32 s65, 36864\n"
"s_mov_b32 s66, 40960\n"
"s_mov_b32 s67, 45056\n"
"s_mov_b32 s68, 49152\n"
"s_mov_b32 s69, 53248\n"
"s_mov_b32 s70, 57344\n"
"s_mov_b32 s71, 61440\n"
"s_mov_b32 s72, 65536\n"
"s_mov_b32 s73, 69632\n"
"s_mov_b32 s74, 73728\n"
"s_mov_b32 s52, s8\n"
"s_and_b32 s53, s9, 0xffff\n"
"s_mov_b32 s54, 2097152\n"
"s_mov_b32 s55, 0x20000\n"
"buffer_load_dwordx2 v[40:41], off, s[52:55], 0 sc1\n"
"s_mul_i32 s95, s12, 7\n"
"s_add_u32 s95, s95, s13\n"
"s_mul_i32 s95, s95, 640\n"
"s_add_u32 s95, s95, 40960\n"
"s_mul_i32 s88, s15, 143360\n"
"s_add_u32 s88, s88, s95\n"
"v_mov_b32 v16, -1\n"
"v_mov_b32 v17, -1\n"
"v_mov_b32 v18, -1\n"
"v_mov_b32 v19, -1\n"
"s_mov_b32 exec_hi, 0xff\n"
"buffer_store_dwordx4 v[16:19], v39, s[52:55], s88 offen sc1\n"
"s_mov_b32 exec_hi, -1\n"
"s_getpc_b64 s[44:45]\n"
".Lk_pc_1:\n"
"s_and_b32 s45, s45, 0xffff\n"
"s_mov_b32 s46, .Lk_chain-.Lk_pc_1\n"
"s_mov_b32 s47, 0x20000\n"
"s_lshl_b32 s88, s15, 13\n"
"v_lshlrev_b32 v43, 3, v39\n"
"buffer_load_dword v43, v43, s[44:47], s88 offen\n"
"s_add_u32 s36, s18, s90\n"
"s_addc_u32 s37, s19, 0\n"
"s_and_b32 s37, s37, 0xffff\n"
"s_mov_b32 s38, s92\n"
"s_mov_b32 s39, 0x20000\n"
"s_add_u32 s90, s90, 67108864\n"
"s_add_u32 s48, s20, s90\n"
"s_addc_u32 s49, s21, 0\n"
"s_and_b32 s49, s49, 0xffff\n"
"s_mov_b32 s50, s92\n"
"s_mov_b32 s51, 0x20000\n"
"s_mov_b32 s35, s90\n"
"s_mov_b32 s75, s92\n"
"s_mov_b32 s20, s94\n"
"s_mov_b32 s96, 131072\n"
"s_mul_i32 s88, s15, 38944\n"
"s_add_u32 s88, s88, 8096\n"
"v_add_u32 v46, s88, v39\n"
"v_lshrrev_b32 v44, 4, v47\n"
"v_bfe_u32 v45, v47, 1, 3\n"
"v_and_b32 v38, 16, v39\n"
"v_lshl_or_b32 v38, v44, 5, v38\n"
"v_lshl_or_b32 v38, v45, 8, v38\n"
"v_and_b32 v42, 3, v47\n"
"v_xor_b32 v33, 1, v42\n"
"v_lshl_or_b32 v33, v33, 2, v42\n"
"v_mul_u32_u24 v33, 0x1111, v33\n"
"v_and_b32 v45, 1, v45\n"
"v_mul_u32_u24 v45, 0xaaaa, v45\n"
"v_add_u32 v45, 0x4444, v45\n"
"v_lshl_or_b32 v33, v45, 16, v33\n"
"s_waitcnt vmcnt(12)\n"
"buffer_load_dwordx4 a[32:35], v39, s[40:43], s58 offen offset:0 nt\n"
"buffer_load_dwordx4 a[36:39], v39, s[40:43], s58 offen offset:1024 nt\n"
"buffer_load_dword v29, v28, s[40:43], s58 offen offset:0 sc1\n"
"s_waitcnt vmcnt(12)\n"
"buffer_load_dwordx4 a[40:43], v39, s[40:43], s58 offen offset:2048 nt\n"
"buffer_load_dwordx4 a[44:47], v39, s[40:43], s58 offen offset:3072 nt\n"
"buffer_load_dword v29, v28, s[40:43], s58 offen offset:2048 sc1\n"
"s_memrealtime s[92:93]\n"
"s_waitcnt vmcnt(12)\n"
"s_waitcnt lgkmcnt(0)\n"
"s_sub_u32 s93, s92, s20\n"
"s_cmp_ge_u32 s93, 250\n"
"s_cbranch_scc1 .Lk_tok_2\n"
".Lk_tlp_3:\n"
"s_memrealtime s[92:93]\n"
"s_waitcnt lgkmcnt(0)\n"
"s_sub_u32 s93, s92, s20\n"
"s_cmp_ge_u32 s93, 250\n"
"s_cbranch_scc0 .Lk_tlp_3\n"
".Lk_tok_2:\n"
"buffer_load_dwordx4 a[48:51], v39, s[40:43], s59 offen offset:0 nt\n"
"buffer_load_dwordx4 a[52:55], v39, s[40:43], s59 offen offset:1024 nt\n"
"buffer_load_dword v29, v28, s[40:43], s59 offen offset:0 sc1\n"
"s_waitcnt vmcnt(12)\n"
"buffer_load_dwordx4 a[56:59], v39, s[40:43], s59 offen offset:2048 nt\n"
"buffer_load_dwordx4 a[60:63], v39, s[40:43], s59 offen offset:3072 nt\n"
"buffer_load_dword v29, v28, s[40:43], s59 offen offset:2048 sc1\n"
"s_memrealtime s[92:93]\n"
"s_waitcnt vmcnt(9)\n"
"s_waitcnt lgkmcnt(0)\n"
"s_sub_u32 s93, s92, s20\n"
"s_cmp_ge_u32 s93, 310\n"
"s_cbranch_scc1 .Lk_tok_4\n"
".Lk_tlp_5:\n"
"s_memrealtime s[92:93]\n"
"s_waitcnt lgkmcnt(0)\n"
"s_sub_u32 s93, s92, s20\n"
"s_cmp_ge_u32 s93, 310\n"
"s_cbranch_scc0 .Lk_tlp_5\n"
".Lk_tok_4:\n"
"buffer_load_dwordx4 a[64:67], v39, s[40:43], s60 offen offset:0 nt\n"
"buffer_load_dwordx4 a[68:71], v39, s[40:43], s60 offen offset:1024 nt\n"
"buffer_load_dword v29, v28, s[40:43], s60 offen offset:0 sc1\n"
"s_waitcnt vmcnt(9)\n"
"buffer_load_dwordx4 a[72:75], v39, s[40:43], s60 offen offset:2048 nt\n"
"buffer_load_dwordx4 a[76:79], v39, s[40:43], s60 offen offset:3072 nt\n"
"buffer_load_dword v29, v28, s[40:43], s60 offen offset:2048 sc1\n"
"s_memrealtime s[92:93]\n"
"s_waitcnt vmcnt(9)\n"
"s_waitcnt lgkmcnt(0)\n"
"s_sub_u32 s93, s92, s20\n"
"s_cmp_ge_u32 s93, 370\n"
"s_cbranch_scc1 .Lk_tok_6\n"
".Lk_tlp_7:\n"
"s_memrealtime s[92:93]\n"
"s_waitcnt lgkmcnt(0)\n"
"s_sub_u32 s93, s92, s20\n"
"s_cmp_ge_u32 s93, 370\n"
"s_cbranch_scc0 .Lk_tlp_7\n"
".Lk_tok_6:\n"
"buffer_load_dwordx4 a[80:83], v39, s[40:43], s61 offen offset:0 nt\n"
"buffer_load_dwordx4 a[84:87], v39, s[40:43], s61 offen offset:1024 nt\n"
"buffer_load_dword v29, v28, s[40:43], s61 offen offset:0 sc1\n"
"s_waitcnt vmcnt(9)\n"
"buffer_load_dwordx4 a[88:91], v38, s[36:39], s56 offen offset:0 nt\n"
"buffer_load_dwordx4 a[92:95], v38, s[36:39], s56 offen offset:128 nt\n"
"s_memrealtime s[92:93]\n"
"s_waitcnt vmcnt(8)\n"
"s_waitcnt lgkmcnt(0)\n"
"s_sub_u32 s93, s92, s20\n"
"s_cmp_ge_u32 s93, 430\n"
"s_cbranch_scc1 .Lk_tok_8\n"
".Lk_tlp_9:\n"
"s_memrealtime s[92:93]\n"
"s_waitcnt lgkmcnt(0)\n"
"s_sub_u32 s93, s92, s20\n"
"s_cmp_ge_u32 s93, 430\n"
"s_cbranch_scc0 .Lk_tlp_9\n"
".Lk_tok_8:\n"
"buffer_load_dwordx4 a[96:99], v38, s[36:39], s56 offen offset:2048 nt\n"
"buffer_load_dwordx4 a[100:103], v38, s[36:39], s56 offen offset:2176 nt\n"
"s_waitcnt vmcnt(7)\n"
"buffer_load_dwordx4 a[104:107], v38, s[36:39], s57 offen offset:0 nt\n"
"buffer_load_dwordx4 a[108:111], v38, s[36:39], s57 offen offset:128 nt\n"
"s_memrealtime s[92:93]\n"
"s_waitcnt vmcnt(6)\n"
"s_waitcnt lgkmcnt(0)\n"
"s_sub_u32 s93, s92, s20\n"
"s_cmp_ge_u32 s93, 490\n"
"s_cbranch_scc1 .Lk_tok_10\n"
".Lk_tlp_11:\n"
"s_memrealtime s[92:93]\n"
"s_waitcnt lgkmcnt(0)\n"
"s_sub_u32 s93, s92, s20\n"
"s_cmp_ge_u32 s93, 490\n"
"s_cbranch_scc0 .Lk_tlp_11\n"
".Lk_tok_10:\n"
"buffer_load_dwordx4 a[112:115], v38, s[36:39], s57 offen offset:2048 nt\n"
"buffer_load_dwordx4 a[116:119], v38, s[36:39], s57 offen offset:2176 nt\n"
"s_waitcnt vmcnt(6)\n"
"buffer_load_dwordx4 a[120:123], v38, s[36:39], s58 offen offset:0 nt\n"
"buffer_load_dwordx4 a[124:127], v38, s[36:39], s58 offen offset:128 nt\n"
"s_memrealtime s[92:93]\n"
"s_waitcnt vmcnt(6)\n"
"s_waitcnt lgkmcnt(0)\n"
"s_sub_u32 s93, s92, s20\n"
"s_cmp_ge_u32 s93, 550\n"
"s_cbranch_scc1 .Lk_tok_12\n"
".Lk_tlp_13:\n"
"s_memrealtime s[92:93]\n"
"s_waitcnt lgkmcnt(0)\n"
"s_sub_u32 s93, s92, s20\n"
"s_cmp_ge_u32 s93, 550\n"
"s_cbranch_scc0 .Lk_tlp_13\n"
".Lk_tok_12:\n"
"s_add_u32 s44, s18, s35\n"
"s_addc_u32 s45, s19, 0\n"
"s_and_b32 s45, s45, 0xffff\n"
"s_mov_b32 s46, s75\n"
"s_mov_b32 s47, 0x20000\n"
"s_mov_b32 s18, 0xaaaaaaaa\n"
"s_mov_b32 s19, 0xaaaaaaaa\n"
"s_mov_b32 s35, 0x11111111\n"
"s_mov_b32 s34, 0x11111111\n"
"s_cmp_eq_u32 s15, 0\n"
"s_cmov_b32 s34, 0x11111113\n"
"v_readfirstlane_b32 s88, v40\n"
"v_readfirstlane_b32 s89, v41\n"
"s_mul_hi_u32 s93, s88, 0x7f4a7c15\n"
"s_mul_i32 s86, s88, 0x7f4a7c15\n"
"s_mul_i32 s94, s88, 0x9e3779b9\n"
"s_add_u32 s93, s93, s94\n"
"s_mul_i32 s94, s89, 0x7f4a7c15\n"
"s_add_u32 s93, s93, s94\n"
"s_add_u32 s86, s86, 0xd192ed03\n"
"s_addc_u32 s87, s93, 0xd1b54a32\n"
"s_barrier\n"
"v_mov_b32 v40, s86\n"
"v_mov_b32 v41, s87\n"
"s_lshl_b32 s88, s14, 3\n"
"s_add_u32 s88, s88, 4096\n"
"s_mov_b64 exec, 1\n"
"buffer_store_dwordx2 v[40:41], off, s[52:55], s88 sc1\n"
"s_mov_b64 exec, -1\n"
"buffer_load_dwordx4 a[128:131], v38, s[36:39], s58 offen offset:2048 nt\n"
"buffer_load_dwordx4 a[132:135], v38, s[36:39], s58 offen offset:2176 nt\n"
"s_waitcnt vmcnt(7)\n"
"buffer_load_dwordx4 a[136:139], v38, s[36:39], s59 offen offset:0 nt\n"
"buffer_load_dwordx4 a[140:143], v38, s[36:39], s59 offen offset:128 nt\n"
"s_memrealtime s[92:93]\n"
"s_waitcnt vmcnt(7)\n"
"s_waitcnt lgkmcnt(0)\n"
"s_sub_u32 s93, s92, s20\n"
"s_cmp_ge_u32 s93, 610\n"
"s_cbranch_scc1 .Lk_tok_14\n"
".Lk_tlp_15:\n"
"s_memrealtime s[92:93]\n"
"s_waitcnt lgkmcnt(0)\n"
"s_sub_u32 s93, s92, s20\n"
"s_cmp_ge_u32 s93, 610\n"
"s_cbranch_scc0 .Lk_tlp_15\n"
".Lk_tok_14:\n"
"buffer_load_dwordx4 a[144:147], v38, s[36:39], s59 offen offset:2048 nt\n"
"buffer_load_dwordx4 a[148:151], v38, s[36:39], s59 offen offset:2176 nt\n"
"s_waitcnt vmcnt(7)\n"
"buffer_load_dwordx4 a[152:155], v38, s[36:39], s60 offen offset:0 nt\n"
"buffer_load_dwordx4 a[156:159], v38, s[36:39], s60 offen offset:128 nt\n"
"s_memrealtime s[92:93]\n"
"s_waitcnt vmcnt(6)\n"
"s_waitcnt lgkmcnt(0)\n"
"s_sub_u32 s93, s92, s20\n"
"s_cmp_ge_u32 s93, 670\n"
"s_cbranch_scc1 .Lk_tok_16\n"
".Lk_tlp_17:\n"
"s_memrealtime s[92:93]\n"
"s_waitcnt lgkmcnt(0)\n"
"s_sub_u32 s93, s92, s20\n"
"s_cmp_ge_u32 s93, 670\n"
"s_cbranch_scc0 .Lk_tlp_17\n"
".Lk_tok_16:\n"
"buffer_load_dwordx4 a[160:163], v38, s[36:39], s60 offen offset:2048 nt\n"
"buffer_load_dwordx4 a[164:167], v38, s[36:39], s60 offen offset:2176 nt\n"
"s_waitcnt vmcnt(6)\n"
"buffer_load_dwordx4 a[168:171], v38, s[36:39], s61 offen offset:0 nt\n"
"buffer_load_dwordx4 a[172:175], v38, s[36:39], s61 offen offset:128 nt\n"
"s_memrealtime s[92:93]\n"
"s_waitcnt vmcnt(6)\n"
"s_waitcnt lgkmcnt(0)\n"
"s_sub_u32 s93, s92, s20\n"
"s_cmp_ge_u32 s93, 730\n"
"s_cbranch_scc1 .Lk_tok_18\n"
".Lk_tlp_19:\n"
"s_memrealtime s[92:93]\n"
"s_waitcnt lgkmcnt(0)\n"
"s_sub_u32 s93, s92, s20\n"
"s_cmp_ge_u32 s93, 730\n"
"s_cbranch_scc0 .Lk_tlp_19\n"
".Lk_tok_18:\n"
"buffer_load_dwordx4 a[176:179], v38, s[36:39], s61 offen offset:2048 nt\n"
"buffer_load_dwordx4 a[180:183], v38, s[36:39], s61 offen offset:2176 nt\n"
"s_waitcnt vmcnt(6)\n"
"buffer_load_dwordx4 a[184:187], v38, s[36:39], s62 offen offset:0 nt\n"
"buffer_load_dwordx4 a[188:191], v38, s[36:39], s62 offen offset:128 nt\n"
"s_memrealtime s[92:93]\n"
"s_waitcnt vmcnt(6)\n"
"s_waitcnt lgkmcnt(0)\n"
"s_sub_u32 s93, s92, s20\n"
"s_cmp_ge_u32 s93, 790\n"
"s_cbranch_scc1 .Lk_tok_20\n"
".Lk_tlp_21:\n"
"s_memrealtime s[92:93]\n"
"s_waitcnt lgkmcnt(0)\n"
"s_sub_u32 s93, s92, s20\n"
"s_cmp_ge_u32 s93, 790\n"
"s_cbranch_scc0 .Lk_tlp_21\n"
".Lk_tok_20:\n"
"buffer_load_dwordx4 a[192:195], v38, s[36:39], s62 offen offset:2048 nt\n"
"buffer_load_dwordx4 a[196:199], v38, s[36:39], s62 offen offset:2176 nt\n"
"s_waitcnt vmcnt(6)\n"
"buffer_load_dwordx4 a[200:203], v38, s[36:39], s63 offen offset:0 nt\n"
"buffer_load_dwordx4 a[204:207], v38, s[36:39], s63 offen offset:128 nt\n"
"s_memrealtime s[92:93]\n"
"s_waitcnt vmcnt(6)\n"
"s_waitcnt lgkmcnt(0)\n"
"s_sub_u32 s93, s92, s20\n"
"s_cmp_ge_u32 s93, 850\n"
"s_cbranch_scc1 .Lk_tok_22\n"
".Lk_tlp_23:\n"
"s_memrealtime s[92:93]\n"
"s_waitcnt lgkmcnt(0)\n"
"s_sub_u32 s93, s92, s20\n"
"s_cmp_ge_u32 s93, 850\n"
"s_cbranch_scc0 .Lk_tlp_23\n"
".Lk_tok_22:\n"
"buffer_load_dwordx4 a[208:211], v38, s[36:39], s63 offen offset:2048 nt\n"
"buffer_load_dwordx4 a[212:215], v38, s[36:39], s63 offen offset:2176 nt\n"
"s_waitcnt vmcnt(6)\n"
"buffer_load_dwordx4 a[216:219], v38, s[36:39], s64 offen offset:0 nt\n"
"buffer_load_dwordx4 a[220:223], v38, s[36:39], s64 offen offset:128 nt\n"
"s_memrealtime s[92:93]\n"
"s_waitcnt vmcnt(6)\n"
"s_waitcnt lgkmcnt(0)\n"
"s_sub_u32 s93, s92, s20\n"
"s_cmp_ge_u32 s93, 910\n"
"s_cbranch_scc1 .Lk_tok_24\n"
".Lk_tlp_25:\n"
"s_memrealtime s[92:93]\n"
"s_waitcnt lgkmcnt(0)\n"
"s_sub_u32 s93, s92, s20\n"
"s_cmp_ge_u32 s93, 910\n"
"s_cbranch_scc0 .Lk_tlp_25\n"
".Lk_tok_24:\n"
"buffer_load_dwordx4 a[224:227], v38, s[36:39], s64 offen offset:2048 nt\n"
"buffer_load_dwordx4 a[228:231], v38, s[36:39], s64 offen offset:2176 nt\n"
"s_waitcnt vmcnt(6)\n"
"buffer_load_dwordx4 a[232:235], v38, s[36:39], s65 offen offset:0 nt\n"
"buffer_load_dwordx4 a[236:239], v38, s[36:39], s65 offen offset:128 nt\n"
"s_memrealtime s[92:93]\n"
"s_waitcnt vmcnt(6)\n"
"s_waitcnt lgkmcnt(0)\n"
"s_sub_u32 s93, s92, s20\n"
"s_cmp_ge_u32 s93, 970\n"
"s_cbranch_scc1 .Lk_tok_26\n"
".Lk_tlp_27:\n"
"s_memrealtime s[92:93]\n"
"s_waitcnt lgkmcnt(0)\n"
"s_sub_u32 s93, s92, s20\n"
"s_cmp_ge_u32 s93, 970\n"
"s_cbranch_scc0 .Lk_tlp_27\n"
".Lk_tok_26:\n"
"buffer_load_dwordx4 a[240:243], v38, s[36:39], s65 offen offset:2048 nt\n"
"buffer_load_dwordx4 a[244:247], v38, s[36:39], s65 offen offset:2176 nt\n"
"s_waitcnt vmcnt(6)\n"
"buffer_load_dwordx4 a[248:251], v38, s[36:39], s66 offen offset:0 nt\n"
"buffer_load_dwordx4 a[252:255], v38, s[36:39], s66 offen offset:128 nt\n"
"s_memrealtime s[92:93]\n"
"s_waitcnt vmcnt(6)\n"
"s_waitcnt lgkmcnt(0)\n"
"s_sub_u32 s93, s92, s20\n"
"s_cmp_ge_u32 s93, 1030\n"
"s_cbranch_scc1 .Lk_tok_28\n"
".Lk_tlp_29:\n"
"s_memrealtime s[92:93]\n"
"s_waitcnt lgkmcnt(0)\n"
"s_sub_u32 s93, s92, s20\n"
"s_cmp_ge_u32 s93, 1030\n"
"s_cbranch_scc0 .Lk_tlp_29\n"
".Lk_tok_28:\n"
"buffer_load_dwordx4 v[56:59], v38, s[36:39], s66 offen offset:2048 nt\n"
"buffer_load_dwordx4 v[60:63], v38, s[36:39], s66 offen offset:2176 nt\n"
"s_waitcnt vmcnt(6)\n"
"buffer_load_dwordx4 v[64:67], v38, s[36:39], s67 offen offset:0 nt\n"
"buffer_load_dwordx4 v[68:71], v38, s[36:39], s67 offen offset:128 nt\n"
"s_memrealtime s[92:93]\n"
"s_waitcnt vmcnt(6)\n"
"s_waitcnt lgkmcnt(0)\n"
"s_sub_u32 s93, s92, s20\n"
"s_cmp_ge_u32 s93, 1090\n"
"s_cbranch_scc1 .Lk_tok_30\n"
".Lk_tlp_31:\n"
"s_memrealtime s[92:93]\n"
"s_waitcnt lgkmcnt(0)\n"
"s_sub_u32 s93, s92, s20\n"
"s_cmp_ge_u32 s93, 1090\n"
"s_cbranch_scc0 .Lk_tlp_31\n"
".Lk_tok_30:\n"
"buffer_load_dwordx4 v[72:75], v38, s[36:39], s67 offen offset:2048 nt\n"
"buffer_load_dwordx4 v[76:79], v38, s[36:39], s67 offen offset:2176 nt\n"
"s_waitcnt vmcnt(6)\n"
"buffer_load_dwordx4 v[80:83], v38, s[36:39], s68 offen offset:0 nt\n"
"buffer_load_dwordx4 v[84:87], v38, s[36:39], s68 offen offset:128 nt\n"
"s_memrealtime s[92:93]\n"
"s_waitcnt vmcnt(6)\n"
"s_waitcnt lgkmcnt(0)\n"
"s_sub_u32 s93, s92, s20\n"
"s_cmp_ge_u32 s93, 1150\n"
"s_cbranch_scc1 .Lk_tok_32\n"
".Lk_tlp_33:\n"
"s_memrealtime s[92:93]\n"
"s_waitcnt lgkmcnt(0)\n"
"s_sub_u32 s93, s92, s20\n"
"s_cmp_ge_u32 s93, 1150\n"
"s_cbranch_scc0 .Lk_tlp_33\n"
".Lk_tok_32:\n"
"buffer_load_dwordx4 v[88:91], v38, s[36:39], s68 offen offset:2048 nt\n"
"buffer_load_dwordx4 v[92:95], v38, s[36:39], s68 offen offset:2176 nt\n"
"s_waitcnt vmcnt(6)\n"
"buffer_load_dwordx4 v[96:99], v38, s[36:39], s69 offen offset:0 nt\n"
"buffer_load_dwordx4 v[100:103], v38, s[36:39], s69 offen offset:128 nt\n"
"s_memrealtime s[92:93]\n"
"s_waitcnt vmcnt(6)\n"
"s_waitcnt lgkmcnt(0)\n"
"s_sub_u32 s93, s92, s20\n"
"s_cmp_ge_u32 s93, 1210\n"
"s_cbranch_scc1 .Lk_tok_34\n"
".Lk_tlp_35:\n"
"s_memrealtime s[92:93]\n"
"s_waitcnt lgkmcnt(0)\n"
"s_sub_u32 s93, s92, s20\n"
"s_cmp_ge_u32 s93, 1210\n"
"s_cbranch_scc0 .Lk_tlp_35\n"
".Lk_tok_34:\n"
"s_lshl_b32 s91, s12, 3\n"
"s_add_u32 s91, s91, 4096\n"
"buffer_load_dwordx2 v[42:43], off, s[52:55], s91 sc1\n"
"buffer_load_dwordx4 v[104:107], v38, s[36:39], s69 offen offset:2048 nt\n"
"buffer_load_dwordx4 v[108:111], v38, s[36:39], s69 offen offset:2176 nt\n"
"s_waitcnt vmcnt(7)\n"
"buffer_load_dwordx4 v[112:115], v38, s[36:39], s70 offen offset:0 nt\n"
"buffer_load_dwordx4 v[116:119], v38, s[36:39], s70 offen offset:128 nt\n"
"s_memrealtime s[92:93]\n"
"s_waitcnt vmcnt(7)\n"
"s_waitcnt lgkmcnt(0)\n"
"s_sub_u32 s93, s92, s20\n"
"s_cmp_ge_u32 s93, 1270\n"
"s_cbranch_scc1 .Lk_tok_36\n"
".Lk_tlp_37:\n"
"s_memrealtime s[92:93]\n"
"s_waitcnt lgkmcnt(0)\n"
"s_sub_u32 s93, s92, s20\n"
"s_cmp_ge_u32 s93, 1270\n"
"s_cbranch_scc0 .Lk_tlp_37\n"
".Lk_tok_36:\n"
"buffer_load_dwordx4 v[120:123], v38, s[36:39], s70 offen offset:2048 nt\n"
"buffer_load_dwordx4 v[124:127], v38, s[36:39], s70 offen offset:2176 nt\n"
"s_waitcnt vmcnt(7)\n"
"buffer_load_dwordx4 v[128:131], v38, s[36:39], s71 offen offset:0 nt\n"
"buffer_load_dwordx4 v[132:135], v38, s[36:39], s71 offen offset:128 nt\n"
"s_memrealtime s[92:93]\n"
"s_waitcnt vmcnt(6)\n"
"s_waitcnt lgkmcnt(0)\n"
"s_sub_u32 s93, s92, s20\n"
"s_cmp_ge_u32 s93, 1330\n"
"s_cbranch_scc1 .Lk_tok_38\n"
".Lk_tlp_39:\n"
"s_memrealtime s[92:93]\n"
"s_waitcnt lgkmcnt(0)\n"
"s_sub_u32 s93, s92, s20\n"
"s_cmp_ge_u32 s93, 1330\n"
"s_cbranch_scc0 .Lk_tlp_39\n"
".Lk_tok_38:\n"
"buffer_load_dwordx4 v[136:139], v38, s[36:39], s71 offen offset:2048 nt\n"
"buffer_load_dwordx4 v[140:143], v38, s[36:39], s71 offen offset:2176 nt\n"
"s_waitcnt vmcnt(6)\n"
"v_readfirstlane_b32 s88, v42\n"
"v_readfirstlane_b32 s89, v43\n"
"s_cmp_eq_u64 s[88:89], s[86:87]\n"
"s_cbranch_scc1 .Lk_aok_40\n"
".Lk_alp_41:\n"
"s_cmp_eq_u32 s96, 0\n"
"s_cbranch_scc1 .Lk_aok_40\n"
"s_sub_u32 s96, s96, 1\n"
"buffer_load_dwordx2 v[42:43], off, s[52:55], s91 sc1\n"
"s_waitcnt vmcnt(0)\n"
"v_readfirstlane_b32 s88, v42\n"
"v_readfirstlane_b32 s89, v43\n"
"s_cmp_eq_u64 s[88:89], s[86:87]\n"
"s_cbranch_scc0 .Lk_alp_41\n"
".Lk_aok_40:\n"
"buffer_load_dwordx4 v[144:147], v38, s[36:39], s72 offen offset:0 nt\n"
"buffer_load_dwordx4 v[148:151], v38, s[36:39], s72 offen offset:128 nt\n"
"s_memrealtime s[92:93]\n"
"s_waitcnt vmcnt(6)\n"
"s_waitcnt lgkmcnt(0)\n"
"s_sub_u32 s93, s92, s20\n"
"s_cmp_ge_u32 s93, 1390\n"
"s_cbranch_scc1 .Lk_tok_42\n"
".Lk_tlp_43:\n"
"s_memrealtime s[92:93]\n"
"s_waitcnt lgkmcnt(0)\n"
"s_sub_u32 s93, s92, s20\n"
"s_cmp_ge_u32 s93, 1390\n"
"s_cbranch_scc0 .Lk_tlp_43\n"
".Lk_tok_42:\n"
"buffer_load_dwordx4 v[152:155], v38, s[36:39], s72 offen offset:2048 nt\n"
"buffer_load_dwordx4 v[156:159], v38, s[36:39], s72 offen offset:2176 nt\n"
"s_waitcnt vmcnt(6)\n"
"buffer_load_dwordx4 v[160:163], v38, s[36:39], s73 offen offset:0 nt\n"
"buffer_load_dwordx4 v[164:167], v38, s[36:39], s73 offen offset:128 nt\n"
"s_memrealtime s[92:93]\n"
"s_waitcnt vmcnt(6)\n"
"s_waitcnt lgkmcnt(0)\n"
"s_sub_u32 s93, s92, s20\n"
"s_cmp_ge_u32 s93, 1450\n"
"s_cbranch_scc1 .Lk_tok_44\n"
".Lk_tlp_45:\n"
"s_memrealtime s[92:93]\n"
"s_waitcnt lgkmcnt(0)\n"
"s_sub_u32 s93, s92, s20\n"
"s_cmp_ge_u32 s93, 1450\n"
"s_cbranch_scc0 .Lk_tlp_45\n"
".Lk_tok_44:\n"
"buffer_load_dwordx4 v[168:171], v38, s[36:39], s73 offen offset:2048 nt\n"
"buffer_load_dwordx4 v[172:175], v38, s[36:39], s73 offen offset:2176 nt\n"
"s_waitcnt vmcnt(6)\n"
"s_mov_b32 s76, 0xf000f\n"
"s_mov_b32 s77, 0xf000f\n"
"s_mov_b32 s78, 0xf000f0\n"
"s_mov_b32 s79, 0xf000f0\n"
"s_mov_b32 s80, 0xf000f00\n"
"s_mov_b32 s81, 0xf000f00\n"
"s_mov_b32 s82, 0xf000f000\n"
"s_mov_b32 s83, 0xf000f000\n"
"s_lshl_b32 s88, s12, 8\n"
"s_add_u32 s75, s88, 8192\n"
"v_lshrrev_b32 v45, 2, v39\n"
"v_and_b32 v45, 0xc0, v45\n"
"v_and_or_b32 v45, v39, 48, v45\n"
"buffer_load_dwordx4 v[12:15], v45, s[52:55], s75 offen sc1\n"
"v_mov_b64 v[16:17], 0\n"
"v_mov_b64 v[18:19], 0\n"
"v_mov_b64 v[20:21], 0\n"
"v_mov_b64 v[22:23], 0\n"
"v_mov_b64 v[24:25], 0\n"
"v_mov_b64 v[26:27], 0\n"
"v_lshrrev_b32 v40, 8, v39\n"
"v_bfe_u32 v41, v39, 7, 1\n"
"v_lshl_or_b32 v41, v40, 2, v41\n"
"v_bfe_u32 v40, v39, 4, 1\n"
"v_lshl_or_b32 v41, v40, 1, v41\n"
"v_bfe_u32 v40, v39, 5, 2\n"
"v_lshl_or_b32 v41, v40, 4, v41\n"
"v_lshlrev_b32 v41, 2, v41\n"
"buffer_load_dwordx4 v[48:51], v38, s[36:39], s74 offen offset:0 nt\n"
"buffer_load_dwordx4 v[52:55], v38, s[36:39], s74 offen offset:128 nt\n"
"s_memrealtime s[92:93]\n"
"s_waitcnt vmcnt(7)\n"
"s_waitcnt lgkmcnt(0)\n"
"s_sub_u32 s93, s92, s20\n"
"s_cmp_ge_u32 s93, 1510\n"
"s_cbranch_scc1 .Lk_tok_46\n"
".Lk_tlp_47:\n"
"s_memrealtime s[92:93]\n"
"s_waitcnt lgkmcnt(0)\n"
"s_sub_u32 s93, s92, s20\n"
"s_cmp_ge_u32 s93, 1510\n"
"s_cbranch_scc0 .Lk_tlp_47\n"
".Lk_tok_46:\n"
"buffer_load_dwordx4 v[176:179], v39, s[40:43], s61 offen offset:2048 nt\n"
"buffer_load_dwordx4 v[180:183], v39, s[40:43], s61 offen offset:3072 nt\n"
"s_waitcnt vmcnt(7)\n"
"buffer_load_dwordx4 v[184:187], v39, s[40:43], s62 offen offset:0 nt\n"
"buffer_load_dwordx4 v[188:191], v39, s[40:43], s62 offen offset:1024 nt\n"
"s_memrealtime s[92:93]\n"
"s_waitcnt vmcnt(7)\n"
"s_waitcnt lgkmcnt(0)\n"
"s_sub_u32 s93, s92, s20\n"
"s_cmp_ge_u32 s93, 1570\n"
"s_cbranch_scc1 .Lk_tok_48\n"
".Lk_tlp_49:\n"
"s_memrealtime s[92:93]\n"
"s_waitcnt lgkmcnt(0)\n"
"s_sub_u32 s93, s92, s20\n"
"s_cmp_ge_u32 s93, 1570\n"
"s_cbranch_scc0 .Lk_tlp_49\n"
".Lk_tok_48:\n"
"buffer_load_dwordx4 v[192:195], v39, s[40:43], s62 offen offset:2048 nt\n"
"buffer_load_dwordx4 v[196:199], v39, s[40:43], s62 offen offset:3072 nt\n"
"s_waitcnt vmcnt(6)\n"
"buffer_load_dwordx4 v[200:203], v39, s[40:43], s63 offen offset:0 nt\n"
"buffer_load_dwordx4 v[204:207], v39, s[40:43], s63 offen offset:1024 nt\n"
"v_max3_u32 v44, v12, v13, v14\n"
"v_max_u32 v44, v44, v15\n"
"v_cmp_eq_u32 vcc, -1, v44\n"
"s_cbranch_vccz .Lk_qok_50\n"
".Lk_qlp_51:\n"
"s_cmp_eq_u32 s96, 0\n"
"s_cbranch_scc1 .Lk_qok_50\n"
"s_sub_u32 s96, s96, 1\n"
"buffer_load_dwordx4 v[12:15], v45, s[52:55], s75 offen sc1\n"
"s_waitcnt vmcnt(0)\n"
"v_max3_u32 v44, v12, v13, v14\n"
"v_max_u32 v44, v44, v15\n"
"v_cmp_eq_u32 vcc, -1, v44\n"
"s_cbranch_vccnz .Lk_qlp_51\n"
".Lk_qok_50:\n"
"v_cndmask_b32 v0, 0, v12, s[76:77]\n"
"v_cndmask_b32 v1, 0, v13, s[76:77]\n"
"v_cndmask_b32 v2, 0, v14, s[76:77]\n"
"v_cndmask_b32 v3, 0, v15, s[76:77]\n"
"v_cndmask_b32 v4, 0, v12, s[78:79]\n"
"v_cndmask_b32 v5, 0, v13, s[78:79]\n"
"v_smfmac_f32_16x16x64_bf16 v[16:19], v[0:3], a[88:95], v33 abid:1\n"
"v_cndmask_b32 v6, 0, v14, s[78:79]\n"
"v_cndmask_b32 v7, 0, v15, s[78:79]\n"
"v_cndmask_b32 v8, 0, v12, s[80:81]\n"
"v_cndmask_b32 v9, 0, v13, s[80:81]\n"
"v_smfmac_f32_16x16x64_bf16 v[16:19], v[4:7], a[96:103], v33 abid:1\n"
"v_cndmask_b32 v10, 0, v14, s[80:81]\n"
"v_cndmask_b32 v11, 0, v15, s[80:81]\n"
"v_cndmask_b32 v12, 0, v12, s[82:83]\n"
"v_cndmask_b32 v13, 0, v13, s[82:83]\n"
"v_smfmac_f32_16x16x64_bf16 v[16:19], v[8:11], a[104:111], v33 abid:1\n"
"v_cndmask_b32 v14, 0, v14, s[82:83]\n"
"v_cndmask_b32 v15, 0, v15, s[82:83]\n"
"s_nop 1\n"
"v_smfmac_f32_16x16x64_bf16 v[16:19], v[12:15], a[112:119], v33 abid:1\n"
"v_smfmac_f32_16x16x64_bf16 v[20:23], v[0:3], a[120:127], v33 abid:1\n"
"v_smfmac_f32_16x16x64_bf16 v[20:23], v[4:7], a[128:135], v33 abid:1\n"
"v_smfmac_f32_16x16x64_bf16 v[20:23], v[8:11], a[136:143], v33 abid:1\n"
"buffer_load_dwordx4 v[208:211], v39, s[40:43], s63 offen offset:2048 nt\n"
"buffer_load_dwordx4 v[212:215], v39, s[40:43], s63 offen offset:3072 nt\n"
"v_smfmac_f32_16x16x64_bf16 v[20:23], v[12:15], a[144:151], v33 abid:1\n"
"v_smfmac_f32_16x16x64_bf16 v[24:27], v[0:3], a[152:159], v33 abid:1\n"
"v_smfmac_f32_16x16x64_bf16 v[24:27], v[4:7], a[160:167], v33 abid:1\n"
"v_add_f32 v17, v17, v19\n"
"v_add_f32 v16, v16, v18\n"
"v_smfmac_f32_16x16x64_bf16 v[24:27], v[8:11], a[168:175], v33 abid:1\n"
"s_nop 0\n"
"v_add_f32_dpp v28, v17, v16 quad_perm:[1,1,3,3] row_mask:0xf bank_mask:0xf\n"
"v_mov_b64 v[16:17], 0\n"
"v_smfmac_f32_16x16x64_bf16 v[24:27], v[12:15], a[176:183], v33 abid:1\n"
"v_mov_b64 v[18:19], 0\n"
"v_add_f32 v20, v20, v22\n"
"s_nop 0\n"
"v_smfmac_f32_16x16x64_bf16 v[16:19], v[0:3], a[184:191], v33 abid:1\n"
"v_add_f32 v21, v21, v23\n"
"v_smfmac_f32_16x16x64_bf16 v[16:19], v[4:7], a[192:199], v33 abid:1\n"
"s_nop 0\n"
"v_add_f32_dpp v22, v20, v21 quad_perm:[0,0,2,2] row_mask:0xf bank_mask:0xf\n"
"v_cndmask_b32 v28, v28, v22, s[18:19]\n"
"v_smfmac_f32_16x16x64_bf16 v[16:19], v[8:11], a[200:207], v33 abid:1\n"
"v_mov_b64 v[20:21], 0\n"
"v_mov_b64 v[22:23], 0\n"
"v_smfmac_f32_16x16x64_bf16 v[16:19], v[12:15], a[208:215], v33 abid:1\n"
"v_cvt_pk_bf16_f32 v28, 0, v28\n"
"v_mul_f32 v28, 0x3db504f3, v28\n"
"v_cvt_pk_bf16_f32 v28, 0, v28\n"
"v_mul_f32 v28, 0x3fb8aa3b, v28\n"
"v_smfmac_f32_16x16x64_bf16 v[20:23], v[0:3], a[216:223], v33 abid:1\n"
"v_add_f32 v25, v25, v27\n"
"v_add_f32 v24, v24, v26\n"
"v_smfmac_f32_16x16x64_bf16 v[20:23], v[4:7], a[224:231], v33 abid:1\n"
"s_nop 0\n"
"v_add_f32_dpp v29, v25, v24 quad_perm:[1,1,3,3] row_mask:0xf bank_mask:0xf\n"
"v_mov_b64 v[24:25], 0\n"
"v_smfmac_f32_16x16x64_bf16 v[20:23], v[8:11], a[232:239], v33 abid:1\n"
"buffer_load_dwordx4 v[216:219], v39, s[40:43], s64 offen offset:0 nt\n"
"buffer_load_dwordx4 v[220:223], v39, s[40:43], s64 offen offset:1024 nt\n"
"v_mov_b64 v[26:27], 0\n"
"v_add_f32 v16, v16, v18\n"
"v_smfmac_f32_16x16x64_bf16 v[20:23], v[12:15], a[240:247], v33 abid:1\n"
"v_add_f32 v17, v17, v19\n"
"v_smfmac_f32_16x16x64_bf16 v[24:27], v[0:3], a[248:255], v33 abid:1\n"
"s_nop 0\n"
"v_add_f32_dpp v18, v16, v17 quad_perm:[0,0,2,2] row_mask:0xf bank_mask:0xf\n"
"v_cndmask_b32 v29, v29, v18, s[18:19]\n"
"v_smfmac_f32_16x16x64_bf16 v[24:27], v[4:7], v[56:63], v33 abid:1\n"
"v_mov_b64 v[16:17], 0\n"
"v_mov_b64 v[18:19], 0\n"
"v_smfmac_f32_16x16x64_bf16 v[24:27], v[8:11], v[64:71], v33 abid:1\n"
"v_cvt_pk_bf16_f32 v29, 0, v29\n"
"v_mul_f32 v29, 0x3db504f3, v29\n"
"v_smfmac_f32_16x16x64_bf16 v[24:27], v[12:15], v[72:79], v33 abid:1\n"
"v_cvt_pk_bf16_f32 v29, 0, v29\n"
"v_mul_f32 v29, 0x3fb8aa3b, v29\n"
"v_smfmac_f32_16x16x64_bf16 v[16:19], v[0:3], v[80:87], v33 abid:1\n"
"v_add_f32 v21, v21, v23\n"
"v_add_f32 v20, v20, v22\n"
"v_smfmac_f32_16x16x64_bf16 v[16:19], v[4:7], v[88:95], v33 abid:1\n"
"s_nop 0\n"
"v_add_f32_dpp v30, v21, v20 quad_perm:[1,1,3,3] row_mask:0xf bank_mask:0xf\n"
"v_mov_b64 v[20:21], 0\n"
"v_smfmac_f32_16x16x64_bf16 v[16:19], v[8:11], v[96:103], v33 abid:1\n"
"v_mov_b64 v[22:23], 0\n"
"v_add_f32 v24, v24, v26\n"
"v_smfmac_f32_16x16x64_bf16 v[16:19], v[12:15], v[104:111], v33 abid:1\n"
"v_add_f32 v25, v25, v27\n"
"v_smfmac_f32_16x16x64_bf16 v[20:23], v[0:3], v[112:119], v33 abid:1\n"
"s_nop 0\n"
"v_add_f32_dpp v26, v24, v25 quad_perm:[0,0,2,2] row_mask:0xf bank_mask:0xf\n"
"v_cndmask_b32 v30, v30, v26, s[18:19]\n"
"v_smfmac_f32_16x16x64_bf16 v[20:23], v[4:7], v[120:127], v33 abid:1\n"
"v_mov_b64 v[24:25], 0\n"
"v_mov_b64 v[26:27], 0\n"
"v_smfmac_f32_16x16x64_bf16 v[20:23], v[8:11], v[128:135], v33 abid:1\n"
"buffer_load_dwordx4 v[224:227], v39, s[40:43], s64 offen offset:2048 nt\n"
"buffer_load_dwordx4 v[228:231], v39, s[40:43], s64 offen offset:3072 nt\n"
"v_cvt_pk_bf16_f32 v30, 0, v30\n"
"v_mul_f32 v30, 0x3db504f3, v30\n"
"v_smfmac_f32_16x16x64_bf16 v[20:23], v[12:15], v[136:143], v33 abid:1\n"
"v_cvt_pk_bf16_f32 v30, 0, v30\n"
"v_mul_f32 v30, 0x3fb8aa3b, v30\n"
"v_smfmac_f32_16x16x64_bf16 v[24:27], v[0:3], v[144:151], v33 abid:1\n"
"v_add_f32 v17, v17, v19\n"
"v_add_f32 v16, v16, v18\n"
"v_smfmac_f32_16x16x64_bf16 v[24:27], v[4:7], v[152:159], v33 abid:1\n"
"s_nop 0\n"
"v_add_f32_dpp v31, v17, v16 quad_perm:[1,1,3,3] row_mask:0xf bank_mask:0xf\n"
"v_mov_b64 v[16:17], 0\n"
"v_smfmac_f32_16x16x64_bf16 v[24:27], v[8:11], v[160:167], v33 abid:1\n"
"v_mov_b64 v[18:19], 0\n"
"v_add_f32 v20, v20, v22\n"
"v_smfmac_f32_16x16x64_bf16 v[24:27], v[12:15], v[168:175], v33 abid:1\n"
"v_add_f32 v21, v21, v23\n"
"v_smfmac_f32_16x16x64_bf16 v[16:19], v[0:3], v[48:55], v33 abid:1\n"
"s_nop 0\n"
"v_add_f32_dpp v22, v20, v21 quad_perm:[0,0,2,2] row_mask:0xf bank_mask:0xf\n"
"v_cndmask_b32 v31, v31, v22, s[18:19]\n"
"v_mov_b32 v40, 0xff800000\n"
"v_cvt_pk_bf16_f32 v31, 0, v31\n"
"v_mul_f32 v31, 0x3db504f3, v31\n"
"v_cvt_pk_bf16_f32 v31, 0, v31\n"
"v_mul_f32 v31, 0x3fb8aa3b, v31\n"
"v_add_f32 v25, v25, v27\n"
"v_add_f32 v24, v24, v26\n"
"v_add_f32 v16, v16, v18\n"
"s_nop 0\n"
"v_add_f32_dpp v32, v25, v24 quad_perm:[1,1,3,3] row_mask:0xf bank_mask:0xf\n"
"v_add_f32 v17, v17, v19\n"
"s_nop 1\n"
"v_add_f32_dpp v18, v16, v17 quad_perm:[0,0,2,2] row_mask:0xf bank_mask:0xf\n"
"v_cndmask_b32 v32, v32, v18, s[18:19]\n"
"v_cvt_pk_bf16_f32 v32, 0, v32\n"
"v_mul_f32 v32, 0x3db504f3, v32\n"
"v_cvt_pk_bf16_f32 v32, 0, v32\n"
"v_mul_f32 v32, 0x3fb8aa3b, v32\n"
"v_cndmask_b32 v32, v40, v32, s[84:85]\n"
"v_max3_f32 v42, v28, v29, v30\n"
"v_max3_f32 v42, v42, v31, v32\n"
"v_mov_b32 v16, 0\n"
"v_mov_b32 v17, 0\n"
"v_max_f32_dpp v42, v42, v42 row_ror:8 row_mask:0xf bank_mask:0xf\n"
"v_mov_b32 v18, 0\n"
"v_mov_b32 v19, 0\n"
"v_max_f32_dpp v42, v42, v42 row_ror:4 row_mask:0xf bank_mask:0xf\n"
"v_mov_b32 v20, 0\n"
"v_mov_b32 v21, 0\n"
"v_max_f32_dpp v42, v42, v42 row_ror:2 row_mask:0xf bank_mask:0xf\n"
"v_mov_b32 v22, 0\n"
"v_mov_b32 v23, 0\n"
"v_max_f32_dpp v42, v42, v42 row_ror:1 row_mask:0xf bank_mask:0xf\n"
"v_mov_b32 v34, 0\n"
"v_mov_b32 v35, 0\n"
"v_max_f32_dpp v42, v42, v42 row_bcast:15 row_mask:0xa bank_mask:0xf\n"
"v_mov_b32 v36, 0\n"
"v_mov_b32 v37, 0\n"
"v_max_f32_dpp v42, v42, v42 row_bcast:31 row_mask:0xc bank_mask:0xf\n"
"s_nop 0\n"
"v_readlane_b32 s88, v42, 63\n"
"s_nop 1\n"
"v_subrev_f32 v28, s88, v28\n"
"v_subrev_f32 v29, s88, v29\n"
"v_subrev_f32 v30, s88, v30\n"
"v_subrev_f32 v31, s88, v31\n"
"v_subrev_f32 v32, s88, v32\n"
"v_exp_f32 v28, v28\n"
"v_exp_f32 v29, v29\n"
"v_exp_f32 v30, v30\n"
"v_exp_f32 v31, v31\n"
"v_exp_f32 v32, v32\n"
"v_add_f32 v43, v28, v29\n"
"v_add_f32 v40, v30, v31\n"
"v_cvt_pk_bf16_f32 v28, v28, 0\n"
"v_add_f32 v43, v43, v32\n"
"v_cvt_pk_bf16_f32 v29, v29, 0\n"
"v_add_f32 v43, v43, v40\n"
"v_cvt_pk_bf16_f32 v30, v30, 0\n"
"v_cvt_pk_bf16_f32 v31, v31, 0\n"
"v_cvt_pk_bf16_f32 v32, v32, 0\n"
"ds_bpermute_b32 v28, v41, v28\n"
"ds_bpermute_b32 v29, v41, v29\n"
"ds_bpermute_b32 v30, v41, v30\n"
"ds_bpermute_b32 v31, v41, v31\n"
"ds_bpermute_b32 v32, v41, v32\n"
"ds_write_b128 v46, a[88:91] offset:0\n"
"ds_write_b128 v46, a[92:95] offset:1024\n"
"ds_write_b128 v46, a[96:99] offset:2048\n"
"ds_write_b128 v46, a[100:103] offset:3072\n"
"ds_write_b128 v46, a[104:107] offset:4096\n"
"ds_write_b128 v46, a[108:111] offset:5120\n"
"ds_write_b128 v46, a[112:115] offset:6144\n"
"ds_write_b128 v46, a[116:119] offset:7168\n"
"ds_write_b128 v46, a[120:123] offset:8192\n"
"ds_write_b128 v46, a[124:127] offset:9216\n"
"s_waitcnt lgkmcnt(12)\n"
"ds_write_b128 v46, a[128:131] offset:10240\n"
"ds_write_b128 v46, a[132:135] offset:11264\n"
"ds_write_b128 v46, a[136:139] offset:12288\n"
"s_waitcnt lgkmcnt(12)\n"
"ds_write_b128 v46, a[140:143] offset:13312\n"
"ds_write_b128 v46, a[144:147] offset:14336\n"
"ds_write_b128 v46, a[148:151] offset:15360\n"
"v_mov_b32_dpp v16, v28 row_newbcast:0 row_mask:0xf bank_mask:0x1\n"
"v_mov_b32_dpp v17, v28 row_newbcast:0 row_mask:0xf bank_mask:0x2\n"
"v_mov_b32_dpp v18, v28 row_newbcast:1 row_mask:0xf bank_mask:0x1\n"
"v_mov_b32_dpp v19, v28 row_newbcast:1 row_mask:0xf bank_mask:0x2\n"
"v_add_f32_dpp v43, v43, v43 row_ror:8 row_mask:0xf bank_mask:0xf\n"
"v_mov_b32_dpp v20, v28 row_newbcast:2 row_mask:0xf bank_mask:0x1\n"
"v_mov_b32_dpp v21, v28 row_newbcast:2 row_mask:0xf bank_mask:0x2\n"
"v_mov_b32_dpp v22, v28 row_newbcast:3 row_mask:0xf bank_mask:0x1\n"
"v_mov_b32_dpp v23, v28 row_newbcast:3 row_mask:0xf bank_mask:0x2\n"
"v_add_f32_dpp v43, v43, v43 row_ror:4 row_mask:0xf bank_mask:0xf\n"
"v_smfmac_f32_16x16x64_bf16 v[34:37], v[16:19], a[0:7], v33\n"
"v_mov_b32_dpp v16, v28 row_newbcast:4 row_mask:0xf bank_mask:0x1\n"
"v_mov_b32_dpp v17, v28 row_newbcast:4 row_mask:0xf bank_mask:0x2\n"
"v_mov_b32_dpp v18, v28 row_newbcast:5 row_mask:0xf bank_mask:0x1\n"
"v_mov_b32_dpp v19, v28 row_newbcast:5 row_mask:0xf bank_mask:0x2\n"
"v_add_f32_dpp v43, v43, v43 row_ror:2 row_mask:0xf bank_mask:0xf\n"
"v_smfmac_f32_16x16x64_bf16 v[34:37], v[20:23], a[8:15], v33\n"
"v_mov_b32_dpp v20, v28 row_newbcast:6 row_mask:0xf bank_mask:0x1\n"
"v_mov_b32_dpp v21, v28 row_newbcast:6 row_mask:0xf bank_mask:0x2\n"
"v_mov_b32_dpp v22, v28 row_newbcast:7 row_mask:0xf bank_mask:0x1\n"
"v_mov_b32_dpp v23, v28 row_newbcast:7 row_mask:0xf bank_mask:0x2\n"
"v_add_f32_dpp v43, v43, v43 row_ror:1 row_mask:0xf bank_mask:0xf\n"
"v_smfmac_f32_16x16x64_bf16 v[34:37], v[16:19], a[16:23], v33\n"
"v_mov_b32_dpp v16, v28 row_newbcast:8 row_mask:0xf bank_mask:0x1\n"
"v_mov_b32_dpp v17, v28 row_newbcast:8 row_mask:0xf bank_mask:0x2\n"
"v_mov_b32_dpp v18, v28 row_newbcast:9 row_mask:0xf bank_mask:0x1\n"
"v_mov_b32_dpp v19, v28 row_newbcast:9 row_mask:0xf bank_mask:0x2\n"
"v_add_f32_dpp v43, v43, v43 row_bcast:15 row_mask:0xa bank_mask:0xf\n"
"v_smfmac_f32_16x16x64_bf16 v[34:37], v[20:23], a[24:31], v33\n"
"v_mov_b32_dpp v20, v28 row_newbcast:10 row_mask:0xf bank_mask:0x1\n"
"v_mov_b32_dpp v21, v28 row_newbcast:10 row_mask:0xf bank_mask:0x2\n"
"v_mov_b32_dpp v22, v28 row_newbcast:11 row_mask:0xf bank_mask:0x1\n"
"v_mov_b32_dpp v23, v28 row_newbcast:11 row_mask:0xf bank_mask:0x2\n"
"v_add_f32_dpp v43, v43, v43 row_bcast:31 row_mask:0xc bank_mask:0xf\n"
"v_smfmac_f32_16x16x64_bf16 v[34:37], v[16:19], a[32:39], v33\n"
"v_mov_b32_dpp v16, v28 row_newbcast:12 row_mask:0xf bank_mask:0x1\n"
"v_mov_b32_dpp v17, v28 row_newbcast:12 row_mask:0xf bank_mask:0x2\n"
"v_mov_b32_dpp v18, v28 row_newbcast:13 row_mask:0xf bank_mask:0x1\n"
"v_mov_b32_dpp v19, v28 row_newbcast:13 row_mask:0xf bank_mask:0x2\n"
"s_lshl_b32 s88, s15, 3\n"
"v_mov_b32 v40, s88\n"
"s_mov_b32 exec_lo, 0\n"
"s_and_b32 exec_hi, exec_hi, 0x80000000\n"
"s_waitcnt lgkmcnt(12)\n"
"ds_write_b64 v40, v[42:43] offset:4608\n"
"s_mov_b64 exec, -1\n"
"v_smfmac_f32_16x16x64_bf16 v[34:37], v[20:23], a[40:47], v33\n"
"v_mov_b32_dpp v20, v28 row_newbcast:14 row_mask:0xf bank_mask:0x1\n"
"v_mov_b32_dpp v21, v28 row_newbcast:14 row_mask:0xf bank_mask:0x2\n"
"v_mov_b32_dpp v22, v28 row_newbcast:15 row_mask:0xf bank_mask:0x1\n"
"v_mov_b32_dpp v23, v28 row_newbcast:15 row_mask:0xf bank_mask:0x2\n"
"v_smfmac_f32_16x16x64_bf16 v[34:37], v[16:19], a[48:55], v33\n"
"v_mov_b32_dpp v16, v29 row_newbcast:0 row_mask:0xf bank_mask:0x1\n"
"v_mov_b32_dpp v17, v29 row_newbcast:0 row_mask:0xf bank_mask:0x2\n"
"v_mov_b32_dpp v18, v29 row_newbcast:1 row_mask:0xf bank_mask:0x1\n"
"v_mov_b32_dpp v19, v29 row_newbcast:1 row_mask:0xf bank_mask:0x2\n"
"v_smfmac_f32_16x16x64_bf16 v[34:37], v[20:23], a[56:63], v33\n"
"v_mov_b32_dpp v20, v29 row_newbcast:2 row_mask:0xf bank_mask:0x1\n"
"v_mov_b32_dpp v21, v29 row_newbcast:2 row_mask:0xf bank_mask:0x2\n"
"v_mov_b32_dpp v22, v29 row_newbcast:3 row_mask:0xf bank_mask:0x1\n"
"v_mov_b32_dpp v23, v29 row_newbcast:3 row_mask:0xf bank_mask:0x2\n"
"v_smfmac_f32_16x16x64_bf16 v[34:37], v[16:19], a[64:71], v33\n"
"v_mov_b32_dpp v16, v29 row_newbcast:4 row_mask:0xf bank_mask:0x1\n"
"v_mov_b32_dpp v17, v29 row_newbcast:4 row_mask:0xf bank_mask:0x2\n"
"v_mov_b32_dpp v18, v29 row_newbcast:5 row_mask:0xf bank_mask:0x1\n"
"v_mov_b32_dpp v19, v29 row_newbcast:5 row_mask:0xf bank_mask:0x2\n"
"v_smfmac_f32_16x16x64_bf16 v[34:37], v[20:23], a[72:79], v33\n"
"v_mov_b32_dpp v20, v29 row_newbcast:6 row_mask:0xf bank_mask:0x1\n"
"v_mov_b32_dpp v21, v29 row_newbcast:6 row_mask:0xf bank_mask:0x2\n"
"v_mov_b32_dpp v22, v29 row_newbcast:7 row_mask:0xf bank_mask:0x1\n"
"v_mov_b32_dpp v23, v29 row_newbcast:7 row_mask:0xf bank_mask:0x2\n"
"v_smfmac_f32_16x16x64_bf16 v[34:37], v[16:19], a[80:87], v33\n"
"v_mov_b32_dpp v16, v29 row_newbcast:8 row_mask:0xf bank_mask:0x1\n"
"v_mov_b32_dpp v17, v29 row_newbcast:8 row_mask:0xf bank_mask:0x2\n"
"v_mov_b32_dpp v18, v29 row_newbcast:9 row_mask:0xf bank_mask:0x1\n"
"v_mov_b32_dpp v19, v29 row_newbcast:9 row_mask:0xf bank_mask:0x2\n"
"s_waitcnt vmcnt(12)\n"
"v_smfmac_f32_16x16x64_bf16 v[34:37], v[20:23], v[176:183], v33\n"
"v_mov_b32_dpp v20, v29 row_newbcast:10 row_mask:0xf bank_mask:0x1\n"
"v_mov_b32_dpp v21, v29 row_newbcast:10 row_mask:0xf bank_mask:0x2\n"
"v_mov_b32_dpp v22, v29 row_newbcast:11 row_mask:0xf bank_mask:0x1\n"
"v_mov_b32_dpp v23, v29 row_newbcast:11 row_mask:0xf bank_mask:0x2\n"
"s_waitcnt vmcnt(10)\n"
"v_smfmac_f32_16x16x64_bf16 v[34:37], v[16:19], v[184:191], v33\n"
"v_mov_b32_dpp v16, v29 row_newbcast:12 row_mask:0xf bank_mask:0x1\n"
"v_mov_b32_dpp v17, v29 row_newbcast:12 row_mask:0xf bank_mask:0x2\n"
"v_mov_b32_dpp v18, v29 row_newbcast:13 row_mask:0xf bank_mask:0x1\n"
"v_mov_b32_dpp v19, v29 row_newbcast:13 row_mask:0xf bank_mask:0x2\n"
"s_waitcnt vmcnt(8)\n"
"v_smfmac_f32_16x16x64_bf16 v[34:37], v[20:23], v[192:199], v33\n"
"v_mov_b32_dpp v20, v29 row_newbcast:14 row_mask:0xf bank_mask:0x1\n"
"v_mov_b32_dpp v21, v29 row_newbcast:14 row_mask:0xf bank_mask:0x2\n"
"v_mov_b32_dpp v22, v29 row_newbcast:15 row_mask:0xf bank_mask:0x1\n"
"v_mov_b32_dpp v23, v29 row_newbcast:15 row_mask:0xf bank_mask:0x2\n"
"s_waitcnt vmcnt(6)\n"
"v_smfmac_f32_16x16x64_bf16 v[34:37], v[16:19], v[200:207], v33\n"
"buffer_load_dwordx4 v[232:235], v39, s[40:43], s65 offen offset:0 nt\n"
"buffer_load_dwordx4 v[236:239], v39, s[40:43], s65 offen offset:1024 nt\n"
"v_mov_b32_dpp v16, v30 row_newbcast:0 row_mask:0xf bank_mask:0x1\n"
"v_mov_b32_dpp v17, v30 row_newbcast:0 row_mask:0xf bank_mask:0x2\n"
"v_mov_b32_dpp v18, v30 row_newbcast:1 row_mask:0xf bank_mask:0x1\n"
"v_mov_b32_dpp v19, v30 row_newbcast:1 row_mask:0xf bank_mask:0x2\n"
"s_waitcnt vmcnt(6)\n"
"v_smfmac_f32_16x16x64_bf16 v[34:37], v[20:23], v[208:215], v33\n"
"s_waitcnt lgkmcnt(0)\n"
"s_memrealtime s[92:93]\n"
"s_waitcnt lgkmcnt(0)\n"
"s_sub_u32 s93, s92, s20\n"
"s_cmp_ge_u32 s93, 1750\n"
"s_cbranch_scc1 .Lk_tok_52\n"
".Lk_tlp_53:\n"
"s_memrealtime s[92:93]\n"
"s_waitcnt lgkmcnt(0)\n"
"s_sub_u32 s93, s92, s20\n"
"s_cmp_ge_u32 s93, 1750\n"
"s_cbranch_scc0 .Lk_tlp_53\n"
".Lk_tok_52:\n"
"buffer_load_dwordx4 v[240:243], v39, s[40:43], s65 offen offset:2048 nt\n"
"buffer_load_dwordx4 v[244:247], v39, s[40:43], s65 offen offset:3072 nt\n"
"v_mov_b32_dpp v20, v30 row_newbcast:2 row_mask:0xf bank_mask:0x1\n"
"v_mov_b32_dpp v21, v30 row_newbcast:2 row_mask:0xf bank_mask:0x2\n"
"v_mov_b32_dpp v22, v30 row_newbcast:3 row_mask:0xf bank_mask:0x1\n"
"v_mov_b32_dpp v23, v30 row_newbcast:3 row_mask:0xf bank_mask:0x2\n"
"s_waitcnt vmcnt(6)\n"
"v_smfmac_f32_16x16x64_bf16 v[34:37], v[16:19], v[216:223], v33\n"
"buffer_load_dwordx4 v[248:251], v39, s[40:43], s66 offen offset:0 nt\n"
"buffer_load_dwordx4 v[252:255], v39, s[40:43], s66 offen offset:1024 nt\n"
"v_mov_b32_dpp v16, v30 row_newbcast:4 row_mask:0xf bank_mask:0x1\n"
"v_mov_b32_dpp v17, v30 row_newbcast:4 row_mask:0xf bank_mask:0x2\n"
"v_mov_b32_dpp v18, v30 row_newbcast:5 row_mask:0xf bank_mask:0x1\n"
"v_mov_b32_dpp v19, v30 row_newbcast:5 row_mask:0xf bank_mask:0x2\n"
"s_waitcnt vmcnt(6)\n"
"v_smfmac_f32_16x16x64_bf16 v[34:37], v[20:23], v[224:231], v33\n"
"s_memrealtime s[92:93]\n"
"s_waitcnt lgkmcnt(0)\n"
"s_sub_u32 s93, s92, s20\n"
"s_cmp_ge_u32 s93, 1805\n"
"s_cbranch_scc1 .Lk_tok_54\n"
".Lk_tlp_55:\n"
"s_memrealtime s[92:93]\n"
"s_waitcnt lgkmcnt(0)\n"
"s_sub_u32 s93, s92, s20\n"
"s_cmp_ge_u32 s93, 1805\n"
"s_cbranch_scc0 .Lk_tlp_55\n"
".Lk_tok_54:\n"
"buffer_load_dwordx4 a[88:91], v39, s[40:43], s66 offen offset:2048 nt\n"
"buffer_load_dwordx4 a[92:95], v39, s[40:43], s66 offen offset:3072 nt\n"
"v_mov_b32_dpp v20, v30 row_newbcast:6 row_mask:0xf bank_mask:0x1\n"
"v_mov_b32_dpp v21, v30 row_newbcast:6 row_mask:0xf bank_mask:0x2\n"
"v_mov_b32_dpp v22, v30 row_newbcast:7 row_mask:0xf bank_mask:0x1\n"
"v_mov_b32_dpp v23, v30 row_newbcast:7 row_mask:0xf bank_mask:0x2\n"
"s_waitcnt vmcnt(6)\n"
"v_smfmac_f32_16x16x64_bf16 v[34:37], v[16:19], v[232:239], v33\n"
"buffer_load_dwordx4 a[96:99], v39, s[40:43], s67 offen offset:0 nt\n"
"buffer_load_dwordx4 a[100:103], v39, s[40:43], s67 offen offset:1024 nt\n"
"v_mov_b32_dpp v16, v30 row_newbcast:8 row_mask:0xf bank_mask:0x1\n"
"v_mov_b32_dpp v17, v30 row_newbcast:8 row_mask:0xf bank_mask:0x2\n"
"v_mov_b32_dpp v18, v30 row_newbcast:9 row_mask:0xf bank_mask:0x1\n"
"v_mov_b32_dpp v19, v30 row_newbcast:9 row_mask:0xf bank_mask:0x2\n"
"s_waitcnt vmcnt(6)\n"
"v_smfmac_f32_16x16x64_bf16 v[34:37], v[20:23], v[240:247], v33\n"
"s_memrealtime s[92:93]\n"
"s_waitcnt lgkmcnt(0)\n"
"s_sub_u32 s93, s92, s20\n"
"s_cmp_ge_u32 s93, 1856\n"
"s_cbranch_scc1 .Lk_tok_56\n"
".Lk_tlp_57:\n"
"s_memrealtime s[92:93]\n"
"s_waitcnt lgkmcnt(0)\n"
"s_sub_u32 s93, s92, s20\n"
"s_cmp_ge_u32 s93, 1856\n"
"s_cbranch_scc0 .Lk_tlp_57\n"
".Lk_tok_56:\n"
"buffer_load_dwordx4 a[104:107], v39, s[40:43], s67 offen offset:2048 nt\n"
"buffer_load_dwordx4 a[108:111], v39, s[40:43], s67 offen offset:3072 nt\n"
"v_mov_b32_dpp v20, v30 row_newbcast:10 row_mask:0xf bank_mask:0x1\n"
"v_mov_b32_dpp v21, v30 row_newbcast:10 row_mask:0xf bank_mask:0x2\n"
"v_mov_b32_dpp v22, v30 row_newbcast:11 row_mask:0xf bank_mask:0x1\n"
"v_mov_b32_dpp v23, v30 row_newbcast:11 row_mask:0xf bank_mask:0x2\n"
"s_waitcnt vmcnt(6)\n"
"v_smfmac_f32_16x16x64_bf16 v[34:37], v[16:19], v[248:255], v33\n"
"buffer_load_dwordx4 a[112:115], v39, s[40:43], s68 offen offset:0 nt\n"
"buffer_load_dwordx4 a[116:119], v39, s[40:43], s68 offen offset:1024 nt\n"
"v_mov_b32_dpp v16, v30 row_newbcast:12 row_mask:0xf bank_mask:0x1\n"
"v_mov_b32_dpp v17, v30 row_newbcast:12 row_mask:0xf bank_mask:0x2\n"
"v_mov_b32_dpp v18, v30 row_newbcast:13 row_mask:0xf bank_mask:0x1\n"
"v_mov_b32_dpp v19, v30 row_newbcast:13 row_mask:0xf bank_mask:0x2\n"
"s_waitcnt vmcnt(6)\n"
"v_smfmac_f32_16x16x64_bf16 v[34:37], v[20:23], a[88:95], v33\n"
"s_memrealtime s[92:93]\n"
"s_waitcnt lgkmcnt(0)\n"
"s_sub_u32 s93, s92, s20\n"
"s_cmp_ge_u32 s93, 1907\n"
"s_cbranch_scc1 .Lk_tok_58\n"
".Lk_tlp_59:\n"
"s_memrealtime s[92:93]\n"
"s_waitcnt lgkmcnt(0)\n"
"s_sub_u32 s93, s92, s20\n"
"s_cmp_ge_u32 s93, 1907\n"
"s_cbranch_scc0 .Lk_tlp_59\n"
".Lk_tok_58:\n"
"buffer_load_dwordx4 a[120:123], v39, s[40:43], s68 offen offset:2048 nt\n"
"buffer_load_dwordx4 a[124:127], v39, s[40:43], s68 offen offset:3072 nt\n"
"v_mov_b32_dpp v20, v30 row_newbcast:14 row_mask:0xf bank_mask:0x1\n"
"v_mov_b32_dpp v21, v30 row_newbcast:14 row_mask:0xf bank_mask:0x2\n"
"v_mov_b32_dpp v22, v30 row_newbcast:15 row_mask:0xf bank_mask:0x1\n"
"v_mov_b32_dpp v23, v30 row_newbcast:15 row_mask:0xf bank_mask:0x2\n"
"s_waitcnt vmcnt(6)\n"
"v_smfmac_f32_16x16x64_bf16 v[34:37], v[16:19], a[96:103], v33\n"
"buffer_load_dwordx4 a[128:131], v39, s[40:43], s69 offen offset:0 nt\n"
"buffer_load_dwordx4 a[132:135], v39, s[40:43], s69 offen offset:1024 nt\n"
"v_mov_b32_dpp v16, v31 row_newbcast:0 row_mask:0xf bank_mask:0x1\n"
"v_mov_b32_dpp v17, v31 row_newbcast:0 row_mask:0xf bank_mask:0x2\n"
"v_mov_b32_dpp v18, v31 row_newbcast:1 row_mask:0xf bank_mask:0x1\n"
"v_mov_b32_dpp v19, v31 row_newbcast:1 row_mask:0xf bank_mask:0x2\n"
"s_waitcnt vmcnt(6)\n"
"v_smfmac_f32_16x16x64_bf16 v[34:37], v[20:23], a[104:111], v33\n"
"s_memrealtime s[92:93]\n"
"s_waitcnt lgkmcnt(0)\n"
"s_sub_u32 s93, s92, s20\n"
"s_cmp_ge_u32 s93, 1958\n"
"s_cbranch_scc1 .Lk_tok_60\n"
".Lk_tlp_61:\n"
"s_memrealtime s[92:93]\n"
"s_waitcnt lgkmcnt(0)\n"
"s_sub_u32 s93, s92, s20\n"
"s_cmp_ge_u32 s93, 1958\n"
"s_cbranch_scc0 .Lk_tlp_61\n"
".Lk_tok_60:\n"
"buffer_load_dwordx4 a[136:139], v39, s[40:43], s69 offen offset:2048 nt\n"
"buffer_load_dwordx4 a[140:143], v39, s[40:43], s69 offen offset:3072 nt\n"
"v_mov_b32_dpp v20, v31 row_newbcast:2 row_mask:0xf bank_mask:0x1\n"
"v_mov_b32_dpp v21, v31 row_newbcast:2 row_mask:0xf bank_mask:0x2\n"
"v_mov_b32_dpp v22, v31 row_newbcast:3 row_mask:0xf bank_mask:0x1\n"
"v_mov_b32_dpp v23, v31 row_newbcast:3 row_mask:0xf bank_mask:0x2\n"
"s_waitcnt vmcnt(6)\n"
"v_smfmac_f32_16x16x64_bf16 v[34:37], v[16:19], a[112:119], v33\n"
"buffer_load_dwordx4 a[144:147], v39, s[40:43], s70 offen offset:0 nt\n"
"buffer_load_dwordx4 a[148:151], v39, s[40:43], s70 offen offset:1024 nt\n"
"v_mov_b32_dpp v16, v31 row_newbcast:4 row_mask:0xf bank_mask:0x1\n"
"v_mov_b32_dpp v17, v31 row_newbcast:4 row_mask:0xf bank_mask:0x2\n"
"v_mov_b32_dpp v18, v31 row_newbcast:5 row_mask:0xf bank_mask:0x1\n"
"v_mov_b32_dpp v19, v31 row_newbcast:5 row_mask:0xf bank_mask:0x2\n"
"s_waitcnt vmcnt(6)\n"
"v_smfmac_f32_16x16x64_bf16 v[34:37], v[20:23], a[120:127], v33\n"
"s_memrealtime s[92:93]\n"
"s_waitcnt lgkmcnt(0)\n"
"s_sub_u32 s93, s92, s20\n"
"s_cmp_ge_u32 s93, 2010\n"
"s_cbranch_scc1 .Lk_tok_62\n"
".Lk_tlp_63:\n"
"s_memrealtime s[92:93]\n"
"s_waitcnt lgkmcnt(0)\n"
"s_sub_u32 s93, s92, s20\n"
"s_cmp_ge_u32 s93, 2010\n"
"s_cbranch_scc0 .Lk_tlp_63\n"
".Lk_tok_62:\n"
"buffer_load_dwordx4 a[0:3], v39, s[40:43], s70 offen offset:2048 nt\n"
"buffer_load_dwordx4 a[4:7], v39, s[40:43], s70 offen offset:3072 nt\n"
"v_mov_b32_dpp v20, v31 row_newbcast:6 row_mask:0xf bank_mask:0x1\n"
"v_mov_b32_dpp v21, v31 row_newbcast:6 row_mask:0xf bank_mask:0x2\n"
"v_mov_b32_dpp v22, v31 row_newbcast:7 row_mask:0xf bank_mask:0x1\n"
"v_mov_b32_dpp v23, v31 row_newbcast:7 row_mask:0xf bank_mask:0x2\n"
"s_waitcnt vmcnt(6)\n"
"v_smfmac_f32_16x16x64_bf16 v[34:37], v[16:19], a[128:135], v33\n"
"ds_write_b128 v46, a[128:131] offset:16384\n"
"ds_write_b128 v46, a[132:135] offset:17408\n"
"buffer_load_dwordx4 a[8:11], v39, s[40:43], s71 offen offset:0 nt\n"
"buffer_load_dwordx4 a[12:15], v39, s[40:43], s71 offen offset:1024 nt\n"
"v_mov_b32_dpp v16, v31 row_newbcast:8 row_mask:0xf bank_mask:0x1\n"
"v_mov_b32_dpp v17, v31 row_newbcast:8 row_mask:0xf bank_mask:0x2\n"
"v_mov_b32_dpp v18, v31 row_newbcast:9 row_mask:0xf bank_mask:0x1\n"
"v_mov_b32_dpp v19, v31 row_newbcast:9 row_mask:0xf bank_mask:0x2\n"
"s_waitcnt vmcnt(6)\n"
"v_smfmac_f32_16x16x64_bf16 v[34:37], v[20:23], a[136:143], v33\n"
"ds_write_b128 v46, a[136:139] offset:18432\n"
"ds_write_b128 v46, a[140:143] offset:19456\n"
"s_waitcnt lgkmcnt(0)\n"
"s_memrealtime s[92:93]\n"
"s_waitcnt lgkmcnt(0)\n"
"s_sub_u32 s93, s92, s20\n"
"s_cmp_ge_u32 s93, 2060\n"
"s_cbranch_scc1 .Lk_tok_64\n"
".Lk_tlp_65:\n"
"s_memrealtime s[92:93]\n"
"s_waitcnt lgkmcnt(0)\n"
"s_sub_u32 s93, s92, s20\n"
"s_cmp_ge_u32 s93, 2060\n"
"s_cbranch_scc0 .Lk_tlp_65\n"
".Lk_tok_64:\n"
"buffer_load_dwordx4 a[16:19], v39, s[40:43], s71 offen offset:2048 nt\n"
"buffer_load_dwordx4 a[20:23], v39, s[40:43], s71 offen offset:3072 nt\n"
"v_mov_b32_dpp v20, v31 row_newbcast:10 row_mask:0xf bank_mask:0x1\n"
"v_mov_b32_dpp v21, v31 row_newbcast:10 row_mask:0xf bank_mask:0x2\n"
"v_mov_b32_dpp v22, v31 row_newbcast:11 row_mask:0xf bank_mask:0x1\n"
"v_mov_b32_dpp v23, v31 row_newbcast:11 row_mask:0xf bank_mask:0x2\n"
"s_waitcnt vmcnt(6)\n"
"v_smfmac_f32_16x16x64_bf16 v[34:37], v[16:19], a[144:151], v33\n"
"ds_write_b128 v46, a[144:147] offset:20480\n"
"ds_write_b128 v46, a[148:151] offset:21504\n"
"buffer_load_dwordx4 a[24:27], v39, s[40:43], s72 offen offset:0 nt\n"
"buffer_load_dwordx4 a[28:31], v39, s[40:43], s72 offen offset:1024 nt\n"
"v_mov_b32_dpp v16, v31 row_newbcast:12 row_mask:0xf bank_mask:0x1\n"
"v_mov_b32_dpp v17, v31 row_newbcast:12 row_mask:0xf bank_mask:0x2\n"
"v_mov_b32_dpp v18, v31 row_newbcast:13 row_mask:0xf bank_mask:0x1\n"
"v_mov_b32_dpp v19, v31 row_newbcast:13 row_mask:0xf bank_mask:0x2\n"
"s_waitcnt vmcnt(6)\n"
"v_smfmac_f32_16x16x64_bf16 v[34:37], v[20:23], a[0:7], v33\n"
"ds_write_b128 v46, a[0:3] offset:22528\n"
"ds_write_b128 v46, a[4:7] offset:23552\n"
"s_waitcnt lgkmcnt(0)\n"
"s_memrealtime s[92:93]\n"
"s_waitcnt lgkmcnt(0)\n"
"s_sub_u32 s93, s92, s20\n"
"s_cmp_ge_u32 s93, 2112\n"
"s_cbranch_scc1 .Lk_tok_66\n"
".Lk_tlp_67:\n"
"s_memrealtime s[92:93]\n"
"s_waitcnt lgkmcnt(0)\n"
"s_sub_u32 s93, s92, s20\n"
"s_cmp_ge_u32 s93, 2112\n"
"s_cbranch_scc0 .Lk_tlp_67\n"
".Lk_tok_66:\n"
"buffer_load_dwordx4 a[32:35], v39, s[40:43], s72 offen offset:2048 nt\n"
"buffer_load_dwordx4 a[36:39], v39, s[40:43], s72 offen offset:3072 nt\n"
"v_mov_b32_dpp v20, v31 row_newbcast:14 row_mask:0xf bank_mask:0x1\n"
"v_mov_b32_dpp v21, v31 row_newbcast:14 row_mask:0xf bank_mask:0x2\n"
"v_mov_b32_dpp v22, v31 row_newbcast:15 row_mask:0xf bank_mask:0x1\n"
"v_mov_b32_dpp v23, v31 row_newbcast:15 row_mask:0xf bank_mask:0x2\n"
"s_waitcnt vmcnt(6)\n"
"v_smfmac_f32_16x16x64_bf16 v[34:37], v[16:19], a[8:15], v33\n"
"ds_write_b128 v46, a[8:11] offset:24576\n"
"ds_write_b128 v46, a[12:15] offset:25600\n"
"buffer_load_dwordx4 a[40:43], v39, s[40:43], s73 offen offset:0 nt\n"
"buffer_load_dwordx4 a[44:47], v39, s[40:43], s73 offen offset:1024 nt\n"
"v_mov_b32_dpp v16, v32 row_newbcast:0 row_mask:0xf bank_mask:0x1\n"
"v_mov_b32_dpp v17, v32 row_newbcast:0 row_mask:0xf bank_mask:0x2\n"
"v_mov_b32_dpp v18, v32 row_newbcast:1 row_mask:0xf bank_mask:0x1\n"
"v_mov_b32_dpp v19, v32 row_newbcast:1 row_mask:0xf bank_mask:0x2\n"
"s_waitcnt vmcnt(6)\n"
"v_smfmac_f32_16x16x64_bf16 v[34:37], v[20:23], a[16:23], v33\n"
"ds_write_b128 v46, a[16:19] offset:26624\n"
"ds_write_b128 v46, a[20:23] offset:27648\n"
"s_waitcnt lgkmcnt(0)\n"
"s_memrealtime s[92:93]\n"
"s_waitcnt lgkmcnt(0)\n"
"s_sub_u32 s93, s92, s20\n"
"s_cmp_ge_u32 s93, 2162\n"
"s_cbranch_scc1 .Lk_tok_68\n"
".Lk_tlp_69:\n"
"s_memrealtime s[92:93]\n"
"s_waitcnt lgkmcnt(0)\n"
"s_sub_u32 s93, s92, s20\n"
"s_cmp_ge_u32 s93, 2162\n"
"s_cbranch_scc0 .Lk_tlp_69\n"
".Lk_tok_68:\n"
"buffer_load_dwordx4 a[48:51], v39, s[40:43], s73 offen offset:2048 nt\n"
"buffer_load_dwordx4 a[52:55], v39, s[40:43], s73 offen offset:3072 nt\n"
"v_mov_b32_dpp v20, v32 row_newbcast:2 row_mask:0xf bank_mask:0x1\n"
"v_mov_b32_dpp v21, v32 row_newbcast:2 row_mask:0xf bank_mask:0x2\n"
"v_mov_b32_dpp v22, v32 row_newbcast:3 row_mask:0xf bank_mask:0x1\n"
"v_mov_b32_dpp v23, v32 row_newbcast:3 row_mask:0xf bank_mask:0x2\n"
"s_waitcnt vmcnt(6)\n"
"v_smfmac_f32_16x16x64_bf16 v[34:37], v[16:19], a[24:31], v33\n"
"ds_write_b128 v46, a[24:27] offset:28672\n"
"ds_write_b128 v46, a[28:31] offset:29696\n"
"buffer_load_dwordx4 a[56:59], v39, s[40:43], s74 offen offset:0 nt\n"
"buffer_load_dwordx4 a[60:63], v39, s[40:43], s74 offen offset:1024 nt\n"
"v_mov_b32_dpp v16, v32 row_newbcast:4 row_mask:0xf bank_mask:0x1\n"
"v_mov_b32_dpp v17, v32 row_newbcast:4 row_mask:0xf bank_mask:0x2\n"
"v_mov_b32_dpp v18, v32 row_newbcast:5 row_mask:0xf bank_mask:0x1\n"
"v_mov_b32_dpp v19, v32 row_newbcast:5 row_mask:0xf bank_mask:0x2\n"
"s_waitcnt vmcnt(6)\n"
"v_smfmac_f32_16x16x64_bf16 v[34:37], v[20:23], a[32:39], v33\n"
"ds_write_b128 v46, a[32:35] offset:30720\n"
"ds_write_b128 v46, a[36:39] offset:31744\n"
"v_mov_b32_dpp v20, v32 row_newbcast:6 row_mask:0xf bank_mask:0x1\n"
"v_mov_b32_dpp v21, v32 row_newbcast:6 row_mask:0xf bank_mask:0x2\n"
"v_mov_b32_dpp v22, v32 row_newbcast:7 row_mask:0xf bank_mask:0x1\n"
"v_mov_b32_dpp v23, v32 row_newbcast:7 row_mask:0xf bank_mask:0x2\n"
"s_waitcnt vmcnt(4)\n"
"v_smfmac_f32_16x16x64_bf16 v[34:37], v[16:19], a[40:47], v33\n"
"ds_write_b128 v46, a[40:43] offset:32768\n"
"ds_write_b128 v46, a[44:47] offset:33792\n"
"s_waitcnt lgkmcnt(0)\n"
"s_memrealtime s[92:93]\n"
"s_waitcnt lgkmcnt(0)\n"
"s_sub_u32 s93, s92, s20\n"
"s_cmp_ge_u32 s93, 2214\n"
"s_cbranch_scc1 .Lk_tok_70\n"
".Lk_tlp_71:\n"
"s_memrealtime s[92:93]\n"
"s_waitcnt lgkmcnt(0)\n"
"s_sub_u32 s93, s92, s20\n"
"s_cmp_ge_u32 s93, 2214\n"
"s_cbranch_scc0 .Lk_tlp_71\n"
".Lk_tok_70:\n"
"buffer_load_dwordx4 a[64:67], v38, s[44:47], s56 offen offset:0 nt\n"
"buffer_load_dwordx4 a[68:71], v38, s[44:47], s56 offen offset:128 nt\n"
"v_mov_b32_dpp v16, v32 row_newbcast:8 row_mask:0xf bank_mask:0x1\n"
"v_mov_b32_dpp v17, v32 row_newbcast:8 row_mask:0xf bank_mask:0x2\n"
"v_mov_b32_dpp v18, v32 row_newbcast:9 row_mask:0xf bank_mask:0x1\n"
"v_mov_b32_dpp v19, v32 row_newbcast:9 row_mask:0xf bank_mask:0x2\n"
"s_waitcnt vmcnt(4)\n"
"v_smfmac_f32_16x16x64_bf16 v[34:37], v[20:23], a[48:55], v33\n"
"ds_write_b128 v46, a[48:51] offset:34816\n"
"ds_write_b128 v46, a[52:55] offset:35840\n"
"buffer_load_dwordx4 a[72:75], v38, s[44:47], s56 offen offset:2048 nt\n"
"buffer_load_dwordx4 a[76:79], v38, s[44:47], s56 offen offset:2176 nt\n"
"s_waitcnt vmcnt(4)\n"
"v_smfmac_f32_16x16x64_bf16 v[34:37], v[16:19], a[56:63], v33\n"
"ds_write_b128 v46, a[56:59] offset:36864\n"
"ds_write_b128 v46, a[60:63] offset:37888\n"
"s_waitcnt lgkmcnt(0)\n"
"s_memrealtime s[92:93]\n"
"s_waitcnt lgkmcnt(0)\n"
"s_sub_u32 s93, s92, s20\n"
"s_cmp_ge_u32 s93, 2264\n"
"s_cbranch_scc1 .Lk_tok_72\n"
".Lk_tlp_73:\n"
"s_memrealtime s[92:93]\n"
"s_waitcnt lgkmcnt(0)\n"
"s_sub_u32 s93, s92, s20\n"
"s_cmp_ge_u32 s93, 2264\n"
"s_cbranch_scc0 .Lk_tlp_73\n"
".Lk_tok_72:\n"
"buffer_load_dwordx4 a[80:83], v38, s[44:47], s57 offen offset:0 nt\n"
"buffer_load_dwordx4 a[84:87], v38, s[44:47], s57 offen offset:128 nt\n"
"s_mul_i32 s88, s15, 576\n"
"s_lshl_b32 s89, s15, 7\n"
"v_and_b32 v40, 0xf0, v39\n"
"v_lshrrev_b32 v41, 4, v39\n"
"v_lshlrev_b32 v40, 1, v40\n"
"v_and_b32 v44, 16, v41\n"
"v_add3_u32 v40, v40, v44, s88\n"
"v_and_b32 v44, 3, v41\n"
"v_lshrrev_b32 v45, 2, v41\n"
"v_lshl_add_u32 v45, v45, 3, s89\n"
"v_mul_u32_u24 v41, 576, v44\n"
"v_lshlrev_b32 v44, 3, v44\n"
"v_add_u32 v41, v41, v45\n"
"s_mov_b64 vcc, 2\n"
"v_mov_b32 v42, 512\n"
"v_cndmask_b32 v45, v45, v42, vcc\n"
"s_add_u32 s90, s95, 0\n"
"s_mov_b32 exec_hi, 0\n"
"ds_write_b128 v40, v[34:37] offset:0\n"
"s_mov_b64 exec, -1\n"
"s_waitcnt lgkmcnt(0)\n"
"s_barrier\n"
"ds_read_b64 v[16:17], v44 offset:4608\n"
"ds_read_b64 v[18:19], v41 offset:0\n"
"s_waitcnt lgkmcnt(1)\n"
"v_max_f32_dpp v20, v16, v16 quad_perm:[1,0,3,2] row_mask:0xf bank_mask:0xf\n"
"s_nop 1\n"
"v_max_f32_dpp v20, v20, v20 quad_perm:[2,3,0,1] row_mask:0xf bank_mask:0xf\n"
"v_sub_f32 v16, v16, v20\n"
"v_exp_f32 v16, v16\n"
"s_waitcnt lgkmcnt(0)\n"
"v_mul_f32 v18, v16, v18\n"
"v_mul_f32 v19, v16, v19\n"
"v_mul_f32 v21, v16, v17\n"
"v_add_f32_dpp v18, v18, v18 quad_perm:[1,0,3,2] row_mask:0xf bank_mask:0xf\n"
"v_add_f32_dpp v19, v19, v19 quad_perm:[1,0,3,2] row_mask:0xf bank_mask:0xf\n"
"v_add_f32_dpp v21, v21, v21 quad_perm:[1,0,3,2] row_mask:0xf bank_mask:0xf\n"
"v_add_f32_dpp v18, v18, v18 quad_perm:[2,3,0,1] row_mask:0xf bank_mask:0xf\n"
"v_add_f32_dpp v19, v19, v19 quad_perm:[2,3,0,1] row_mask:0xf bank_mask:0xf\n"
"v_add_f32_dpp v21, v21, v21 quad_perm:[2,3,0,1] row_mask:0xf bank_mask:0xf\n"
"v_cndmask_b32 v18, v18, v20, vcc\n"
"v_cndmask_b32 v19, v19, v21, vcc\n"
"v_min_u32 v18, -2, v18\n"
"v_min_u32 v19, -2, v19\n"
"s_mov_b64 exec, s[34:35]\n"
"buffer_store_dwordx2 v[18:19], v45, s[52:55], s90 offen sc1\n"
"s_mov_b64 exec, -1\n"
"s_waitcnt vmcnt(5)\n"
"buffer_load_dwordx4 v[176:179], v38, s[44:47], s57 offen offset:2048 nt\n"
"buffer_load_dwordx4 v[180:183], v38, s[44:47], s57 offen offset:2176 nt\n"
"s_memrealtime s[92:93]\n"
"s_waitcnt vmcnt(5)\n"
"s_waitcnt lgkmcnt(0)\n"
"s_sub_u32 s93, s92, s20\n"
"s_cmp_ge_u32 s93, 2315\n"
"s_cbranch_scc1 .Lk_tok_74\n"
".Lk_tlp_75:\n"
"s_memrealtime s[92:93]\n"
"s_waitcnt lgkmcnt(0)\n"
"s_sub_u32 s93, s92, s20\n"
"s_cmp_ge_u32 s93, 2315\n"
"s_cbranch_scc0 .Lk_tlp_75\n"
".Lk_tok_74:\n"
"buffer_load_dwordx4 v[184:187], v38, s[44:47], s58 offen offset:0 nt\n"
"buffer_load_dwordx4 v[188:191], v38, s[44:47], s58 offen offset:128 nt\n"
"s_waitcnt vmcnt(5)\n"
"buffer_load_dwordx4 v[192:195], v38, s[44:47], s58 offen offset:2048 nt\n"
"buffer_load_dwordx4 v[196:199], v38, s[44:47], s58 offen offset:2176 nt\n"
"s_memrealtime s[92:93]\n"
"s_waitcnt vmcnt(4)\n"
"s_waitcnt lgkmcnt(0)\n"
"s_sub_u32 s93, s92, s20\n"
"s_cmp_ge_u32 s93, 2366\n"
"s_cbranch_scc1 .Lk_tok_76\n"
".Lk_tlp_77:\n"
"s_memrealtime s[92:93]\n"
"s_waitcnt lgkmcnt(0)\n"
"s_sub_u32 s93, s92, s20\n"
"s_cmp_ge_u32 s93, 2366\n"
"s_cbranch_scc0 .Lk_tlp_77\n"
".Lk_tok_76:\n"
"buffer_load_dwordx4 v[200:203], v38, s[44:47], s59 offen offset:0 nt\n"
"buffer_load_dwordx4 v[204:207], v38, s[44:47], s59 offen offset:128 nt\n"
"s_waitcnt vmcnt(4)\n"
"buffer_load_dwordx4 v[208:211], v38, s[44:47], s59 offen offset:2048 nt\n"
"buffer_load_dwordx4 v[212:215], v38, s[44:47], s59 offen offset:2176 nt\n"
"s_memrealtime s[92:93]\n"
"s_waitcnt vmcnt(4)\n"
"s_waitcnt lgkmcnt(0)\n"
"s_sub_u32 s93, s92, s20\n"
"s_cmp_ge_u32 s93, 2417\n"
"s_cbranch_scc1 .Lk_tok_78\n"
".Lk_tlp_79:\n"
"s_memrealtime s[92:93]\n"
"s_waitcnt lgkmcnt(0)\n"
"s_sub_u32 s93, s92, s20\n"
"s_cmp_ge_u32 s93, 2417\n"
"s_cbranch_scc0 .Lk_tlp_79\n"
".Lk_tok_78:\n"
"buffer_load_dwordx4 v[216:219], v38, s[44:47], s60 offen offset:0 nt\n"
"buffer_load_dwordx4 v[220:223], v38, s[44:47], s60 offen offset:128 nt\n"
"s_waitcnt vmcnt(4)\n"
"buffer_load_dwordx4 v[224:227], v38, s[44:47], s60 offen offset:2048 nt\n"
"buffer_load_dwordx4 v[228:231], v38, s[44:47], s60 offen offset:2176 nt\n"
"s_memrealtime s[92:93]\n"
"s_waitcnt vmcnt(4)\n"
"s_waitcnt lgkmcnt(0)\n"
"s_sub_u32 s93, s92, s20\n"
"s_cmp_ge_u32 s93, 2468\n"
"s_cbranch_scc1 .Lk_tok_80\n"
".Lk_tlp_81:\n"
"s_memrealtime s[92:93]\n"
"s_waitcnt lgkmcnt(0)\n"
"s_sub_u32 s93, s92, s20\n"
"s_cmp_ge_u32 s93, 2468\n"
"s_cbranch_scc0 .Lk_tlp_81\n"
".Lk_tok_80:\n"
"buffer_load_dwordx4 v[232:235], v38, s[44:47], s61 offen offset:0 nt\n"
"buffer_load_dwordx4 v[236:239], v38, s[44:47], s61 offen offset:128 nt\n"
"s_waitcnt vmcnt(4)\n"
"buffer_load_dwordx4 v[240:243], v38, s[44:47], s61 offen offset:2048 nt\n"
"buffer_load_dwordx4 v[244:247], v38, s[44:47], s61 offen offset:2176 nt\n"
"s_memrealtime s[92:93]\n"
"s_waitcnt vmcnt(4)\n"
"s_waitcnt lgkmcnt(0)\n"
"s_sub_u32 s93, s92, s20\n"
"s_cmp_ge_u32 s93, 2519\n"
"s_cbranch_scc1 .Lk_tok_82\n"
".Lk_tlp_83:\n"
"s_memrealtime s[92:93]\n"
"s_waitcnt lgkmcnt(0)\n"
"s_sub_u32 s93, s92, s20\n"
"s_cmp_ge_u32 s93, 2519\n"
"s_cbranch_scc0 .Lk_tlp_83\n"
".Lk_tok_82:\n"
"buffer_load_dwordx4 v[248:251], v38, s[44:47], s62 offen offset:0 nt\n"
"buffer_load_dwordx4 v[252:255], v38, s[44:47], s62 offen offset:128 nt\n"
"s_waitcnt vmcnt(4)\n"
"buffer_load_dwordx4 a[88:91], v38, s[44:47], s62 offen offset:2048 nt\n"
"buffer_load_dwordx4 a[92:95], v38, s[44:47], s62 offen offset:2176 nt\n"
"s_memrealtime s[92:93]\n"
"s_waitcnt vmcnt(4)\n"
"s_waitcnt lgkmcnt(0)\n"
"s_sub_u32 s93, s92, s20\n"
"s_cmp_ge_u32 s93, 2570\n"
"s_cbranch_scc1 .Lk_tok_84\n"
".Lk_tlp_85:\n"
"s_memrealtime s[92:93]\n"
"s_waitcnt lgkmcnt(0)\n"
"s_sub_u32 s93, s92, s20\n"
"s_cmp_ge_u32 s93, 2570\n"
"s_cbranch_scc0 .Lk_tlp_85\n"
".Lk_tok_84:\n"
"buffer_load_dwordx4 a[96:99], v38, s[44:47], s63 offen offset:0 nt\n"
"buffer_load_dwordx4 a[100:103], v38, s[44:47], s63 offen offset:128 nt\n"
"s_waitcnt vmcnt(4)\n"
"buffer_load_dwordx4 a[104:107], v38, s[44:47], s63 offen offset:2048 nt\n"
"buffer_load_dwordx4 a[108:111], v38, s[44:47], s63 offen offset:2176 nt\n"
"s_memrealtime s[92:93]\n"
"s_waitcnt vmcnt(4)\n"
"s_waitcnt lgkmcnt(0)\n"
"s_sub_u32 s93, s92, s20\n"
"s_cmp_ge_u32 s93, 2621\n"
"s_cbranch_scc1 .Lk_tok_86\n"
".Lk_tlp_87:\n"
"s_memrealtime s[92:93]\n"
"s_waitcnt lgkmcnt(0)\n"
"s_sub_u32 s93, s92, s20\n"
"s_cmp_ge_u32 s93, 2621\n"
"s_cbranch_scc0 .Lk_tlp_87\n"
".Lk_tok_86:\n"
"buffer_load_dwordx4 a[112:115], v38, s[44:47], s64 offen offset:0 nt\n"
"buffer_load_dwordx4 a[116:119], v38, s[44:47], s64 offen offset:128 nt\n"
"s_waitcnt vmcnt(4)\n"
"buffer_load_dwordx4 a[120:123], v38, s[44:47], s64 offen offset:2048 nt\n"
"buffer_load_dwordx4 a[124:127], v38, s[44:47], s64 offen offset:2176 nt\n"
"s_memrealtime s[92:93]\n"
"s_waitcnt vmcnt(4)\n"
"s_waitcnt lgkmcnt(0)\n"
"s_sub_u32 s93, s92, s20\n"
"s_cmp_ge_u32 s93, 2672\n"
"s_cbranch_scc1 .Lk_tok_88\n"
".Lk_tlp_89:\n"
"s_memrealtime s[92:93]\n"
"s_waitcnt lgkmcnt(0)\n"
"s_sub_u32 s93, s92, s20\n"
"s_cmp_ge_u32 s93, 2672\n"
"s_cbranch_scc0 .Lk_tlp_89\n"
".Lk_tok_88:\n"
"buffer_load_dwordx4 a[128:131], v38, s[44:47], s65 offen offset:0 nt\n"
"buffer_load_dwordx4 a[132:135], v38, s[44:47], s65 offen offset:128 nt\n"
"s_waitcnt vmcnt(4)\n"
"buffer_load_dwordx4 a[136:139], v38, s[44:47], s65 offen offset:2048 nt\n"
"buffer_load_dwordx4 a[140:143], v38, s[44:47], s65 offen offset:2176 nt\n"
"s_memrealtime s[92:93]\n"
"s_waitcnt vmcnt(4)\n"
"s_waitcnt lgkmcnt(0)\n"
"s_sub_u32 s93, s92, s20\n"
"s_cmp_ge_u32 s93, 2724\n"
"s_cbranch_scc1 .Lk_tok_90\n"
".Lk_tlp_91:\n"
"s_memrealtime s[92:93]\n"
"s_waitcnt lgkmcnt(0)\n"
"s_sub_u32 s93, s92, s20\n"
"s_cmp_ge_u32 s93, 2724\n"
"s_cbranch_scc0 .Lk_tlp_91\n"
".Lk_tok_90:\n"
"buffer_load_dwordx4 a[144:147], v38, s[44:47], s66 offen offset:0 nt\n"
"buffer_load_dwordx4 a[148:151], v38, s[44:47], s66 offen offset:128 nt\n"
"s_waitcnt vmcnt(4)\n"
"buffer_load_dwordx4 a[0:3], v38, s[44:47], s66 offen offset:2048 nt\n"
"buffer_load_dwordx4 a[4:7], v38, s[44:47], s66 offen offset:2176 nt\n"
"s_memrealtime s[92:93]\n"
"s_waitcnt vmcnt(4)\n"
"s_waitcnt lgkmcnt(0)\n"
"s_sub_u32 s93, s92, s20\n"
"s_cmp_ge_u32 s93, 2774\n"
"s_cbranch_scc1 .Lk_tok_92\n"
".Lk_tlp_93:\n"
"s_memrealtime s[92:93]\n"
"s_waitcnt lgkmcnt(0)\n"
"s_sub_u32 s93, s92, s20\n"
"s_cmp_ge_u32 s93, 2774\n"
"s_cbranch_scc0 .Lk_tlp_93\n"
".Lk_tok_92:\n"
"buffer_load_dwordx4 a[8:11], v38, s[44:47], s67 offen offset:0 nt\n"
"buffer_load_dwordx4 a[12:15], v38, s[44:47], s67 offen offset:128 nt\n"
"s_waitcnt vmcnt(4)\n"
"buffer_load_dwordx4 a[16:19], v38, s[44:47], s67 offen offset:2048 nt\n"
"buffer_load_dwordx4 a[20:23], v38, s[44:47], s67 offen offset:2176 nt\n"
"s_memrealtime s[92:93]\n"
"s_waitcnt vmcnt(4)\n"
"s_waitcnt lgkmcnt(0)\n"
"s_sub_u32 s93, s92, s20\n"
"s_cmp_ge_u32 s93, 2825\n"
"s_cbranch_scc1 .Lk_tok_94\n"
".Lk_tlp_95:\n"
"s_memrealtime s[92:93]\n"
"s_waitcnt lgkmcnt(0)\n"
"s_sub_u32 s93, s92, s20\n"
"s_cmp_ge_u32 s93, 2825\n"
"s_cbranch_scc0 .Lk_tlp_95\n"
".Lk_tok_94:\n"
"buffer_load_dwordx4 a[24:27], v38, s[44:47], s68 offen offset:0 nt\n"
"buffer_load_dwordx4 a[28:31], v38, s[44:47], s68 offen offset:128 nt\n"
"s_waitcnt vmcnt(4)\n"
"buffer_load_dwordx4 a[32:35], v38, s[44:47], s68 offen offset:2048 nt\n"
"buffer_load_dwordx4 a[36:39], v38, s[44:47], s68 offen offset:2176 nt\n"
"s_memrealtime s[92:93]\n"
"s_waitcnt vmcnt(4)\n"
"s_waitcnt lgkmcnt(0)\n"
"s_sub_u32 s93, s92, s20\n"
"s_cmp_ge_u32 s93, 2876\n"
"s_cbranch_scc1 .Lk_tok_96\n"
".Lk_tlp_97:\n"
"s_memrealtime s[92:93]\n"
"s_waitcnt lgkmcnt(0)\n"
"s_sub_u32 s93, s92, s20\n"
"s_cmp_ge_u32 s93, 2876\n"
"s_cbranch_scc0 .Lk_tlp_97\n"
".Lk_tok_96:\n"
"buffer_load_dwordx4 a[40:43], v38, s[44:47], s69 offen offset:0 nt\n"
"buffer_load_dwordx4 a[44:47], v38, s[44:47], s69 offen offset:128 nt\n"
"s_waitcnt vmcnt(4)\n"
"buffer_load_dwordx4 a[48:51], v38, s[44:47], s69 offen offset:2048 nt\n"
"buffer_load_dwordx4 a[52:55], v38, s[44:47], s69 offen offset:2176 nt\n"
"s_memrealtime s[92:93]\n"
"s_waitcnt vmcnt(4)\n"
"s_waitcnt lgkmcnt(0)\n"
"s_sub_u32 s93, s92, s20\n"
"s_cmp_ge_u32 s93, 2928\n"
"s_cbranch_scc1 .Lk_tok_98\n"
".Lk_tlp_99:\n"
"s_memrealtime s[92:93]\n"
"s_waitcnt lgkmcnt(0)\n"
"s_sub_u32 s93, s92, s20\n"
"s_cmp_ge_u32 s93, 2928\n"
"s_cbranch_scc0 .Lk_tlp_99\n"
".Lk_tok_98:\n"
"s_lshl_b32 s88, s12, 8\n"
"s_add_u32 s75, s88, 24576\n"
"v_lshrrev_b32 v45, 2, v39\n"
"v_and_b32 v45, 0xc0, v45\n"
"v_and_or_b32 v45, v39, 48, v45\n"
"buffer_load_dwordx4 v[12:15], v45, s[52:55], s75 offen sc1\n"
"v_mov_b64 v[16:17], 0\n"
"v_mov_b64 v[18:19], 0\n"
"v_mov_b64 v[20:21], 0\n"
"v_mov_b64 v[22:23], 0\n"
"v_mov_b64 v[24:25], 0\n"
"v_mov_b64 v[26:27], 0\n"
"v_lshrrev_b32 v40, 8, v39\n"
"v_bfe_u32 v41, v39, 7, 1\n"
"v_lshl_or_b32 v41, v40, 2, v41\n"
"v_bfe_u32 v40, v39, 4, 1\n"
"v_lshl_or_b32 v41, v40, 1, v41\n"
"v_bfe_u32 v40, v39, 5, 2\n"
"v_lshl_or_b32 v41, v40, 4, v41\n"
"v_lshlrev_b32 v41, 2, v41\n"
"buffer_load_dwordx4 a[56:59], v38, s[44:47], s70 offen offset:0 nt\n"
"buffer_load_dwordx4 a[60:63], v38, s[44:47], s70 offen offset:128 nt\n"
"s_waitcnt vmcnt(2)\n"
"v_max3_u32 v44, v12, v13, v14\n"
"v_max_u32 v44, v44, v15\n"
"v_cmp_eq_u32 vcc, -1, v44\n"
"s_cbranch_vccz .Lk_qok_100\n"
".Lk_qlp_101:\n"
"s_cmp_eq_u32 s96, 0\n"
"s_cbranch_scc1 .Lk_qok_100\n"
"s_sub_u32 s96, s96, 1\n"
"buffer_load_dwordx4 v[12:15], v45, s[52:55], s75 offen sc1\n"
"s_waitcnt vmcnt(0)\n"
"v_max3_u32 v44, v12, v13, v14\n"
"v_max_u32 v44, v44, v15\n"
"v_cmp_eq_u32 vcc, -1, v44\n"
"s_cbranch_vccnz .Lk_qlp_101\n"
"s_memrealtime s[92:93]\n"
"s_waitcnt lgkmcnt(0)\n"
"s_sub_u32 s93, s92, s20\n"
"s_sub_u32 s93, s93, 3070\n"
"s_max_i32 s93, s93, 0\n"
"s_min_i32 s93, s93, 1000\n"
"s_mul_i32 s93, s93, 10\n"
"s_lshr_b32 s93, s93, 4\n"
"s_add_u32 s20, s20, s93\n"
".Lk_qok_100:\n"
"v_cndmask_b32 v0, 0, v12, s[76:77]\n"
"v_cndmask_b32 v1, 0, v13, s[76:77]\n"
"v_cndmask_b32 v2, 0, v14, s[76:77]\n"
"v_cndmask_b32 v3, 0, v15, s[76:77]\n"
"v_cndmask_b32 v4, 0, v12, s[78:79]\n"
"v_cndmask_b32 v5, 0, v13, s[78:79]\n"
"v_smfmac_f32_16x16x64_bf16 v[16:19], v[0:3], a[152:159], v33 abid:1\n"
"buffer_load_dwordx4 a[152:155], v38, s[44:47], s70 offen offset:2048 nt\n"
"buffer_load_dwordx4 a[156:159], v38, s[44:47], s70 offen offset:2176 nt\n"
"v_cndmask_b32 v6, 0, v14, s[78:79]\n"
"v_cndmask_b32 v7, 0, v15, s[78:79]\n"
"v_cndmask_b32 v8, 0, v12, s[80:81]\n"
"v_cndmask_b32 v9, 0, v13, s[80:81]\n"
"v_smfmac_f32_16x16x64_bf16 v[16:19], v[4:7], a[160:167], v33 abid:1\n"
"buffer_load_dwordx4 a[160:163], v38, s[44:47], s71 offen offset:0 nt\n"
"buffer_load_dwordx4 a[164:167], v38, s[44:47], s71 offen offset:128 nt\n"
"v_cndmask_b32 v10, 0, v14, s[80:81]\n"
"v_cndmask_b32 v11, 0, v15, s[80:81]\n"
"v_cndmask_b32 v12, 0, v12, s[82:83]\n"
"v_cndmask_b32 v13, 0, v13, s[82:83]\n"
"v_smfmac_f32_16x16x64_bf16 v[16:19], v[8:11], a[168:175], v33 abid:1\n"
"buffer_load_dwordx4 a[168:171], v38, s[44:47], s71 offen offset:2048 nt\n"
"buffer_load_dwordx4 a[172:175], v38, s[44:47], s71 offen offset:2176 nt\n"
"v_cndmask_b32 v14, 0, v14, s[82:83]\n"
"v_cndmask_b32 v15, 0, v15, s[82:83]\n"
"s_nop 1\n"
"v_smfmac_f32_16x16x64_bf16 v[16:19], v[12:15], a[176:183], v33 abid:1\n"
"v_smfmac_f32_16x16x64_bf16 v[20:23], v[0:3], a[184:191], v33 abid:1\n"
"ds_read_b128 a[176:179], v46 offset:0\n"
"ds_read_b128 a[180:183], v46 offset:1024\n"
"v_smfmac_f32_16x16x64_bf16 v[20:23], v[4:7], a[192:199], v33 abid:1\n"
"ds_read_b128 a[184:187], v46 offset:2048\n"
"ds_read_b128 a[188:191], v46 offset:3072\n"
"v_smfmac_f32_16x16x64_bf16 v[20:23], v[8:11], a[200:207], v33 abid:1\n"
"ds_read_b128 a[192:195], v46 offset:4096\n"
"ds_read_b128 a[196:199], v46 offset:5120\n"
"v_add_f32 v17, v17, v19\n"
"v_add_f32 v16, v16, v18\n"
"v_smfmac_f32_16x16x64_bf16 v[20:23], v[12:15], a[208:215], v33 abid:1\n"
"ds_read_b128 a[200:203], v46 offset:6144\n"
"ds_read_b128 a[204:207], v46 offset:7168\n"
"v_add_f32_dpp v29, v17, v16 quad_perm:[1,1,3,3] row_mask:0xf bank_mask:0xf\n"
"v_mov_b64 v[16:17], 0\n"
"v_smfmac_f32_16x16x64_bf16 v[24:27], v[0:3], a[216:223], v33 abid:1\n"
"ds_read_b128 a[208:211], v46 offset:8192\n"
"ds_read_b128 a[212:215], v46 offset:9216\n"
"v_mov_b64 v[18:19], 0\n"
"v_add_f32 v20, v20, v22\n"
"v_smfmac_f32_16x16x64_bf16 v[24:27], v[4:7], a[224:231], v33 abid:1\n"
"ds_read_b128 a[216:219], v46 offset:10240\n"
"ds_read_b128 a[220:223], v46 offset:11264\n"
"v_add_f32 v21, v21, v23\n"
"v_smfmac_f32_16x16x64_bf16 v[24:27], v[8:11], a[232:239], v33 abid:1\n"
"ds_read_b128 a[224:227], v46 offset:12288\n"
"ds_read_b128 a[228:231], v46 offset:13312\n"
"v_add_f32_dpp v22, v20, v21 quad_perm:[0,0,2,2] row_mask:0xf bank_mask:0xf\n"
"v_cndmask_b32 v29, v29, v22, s[18:19]\n"
"v_smfmac_f32_16x16x64_bf16 v[24:27], v[12:15], a[240:247], v33 abid:1\n"
"ds_read_b128 a[232:235], v46 offset:14336\n"
"s_waitcnt lgkmcnt(12)\n"
"ds_read_b128 a[236:239], v46 offset:15360\n"
"v_mov_b64 v[20:21], 0\n"
"v_mov_b64 v[22:23], 0\n"
"v_smfmac_f32_16x16x64_bf16 v[16:19], v[0:3], a[248:255], v33 abid:1\n"
"ds_read_b128 a[240:243], v46 offset:16384\n"
"ds_read_b128 a[244:247], v46 offset:17408\n"
"v_cvt_pk_bf16_f32 v29, 0, v29\n"
"v_mul_f32 v29, 0x3db504f3, v29\n"
"v_smfmac_f32_16x16x64_bf16 v[16:19], v[4:7], v[56:63], v33 abid:1\n"
"s_waitcnt lgkmcnt(12)\n"
"ds_read_b128 a[248:251], v46 offset:18432\n"
"ds_read_b128 a[252:255], v46 offset:19456\n"
"v_cvt_pk_bf16_f32 v29, 0, v29\n"
"v_mul_f32 v29, 0x3fb8aa3b, v29\n"
"v_smfmac_f32_16x16x64_bf16 v[16:19], v[8:11], v[64:71], v33 abid:1\n"
"ds_read_b128 v[56:59], v46 offset:20480\n"
"s_waitcnt lgkmcnt(12)\n"
"ds_read_b128 v[60:63], v46 offset:21504\n"
"v_add_f32 v25, v25, v27\n"
"v_add_f32 v24, v24, v26\n"
"v_smfmac_f32_16x16x64_bf16 v[16:19], v[12:15], v[72:79], v33 abid:1\n"
"ds_read_b128 v[64:67], v46 offset:22528\n"
"ds_read_b128 v[68:71], v46 offset:23552\n"
"v_add_f32_dpp v30, v25, v24 quad_perm:[1,1,3,3] row_mask:0xf bank_mask:0xf\n"
"v_mov_b64 v[24:25], 0\n"
"v_smfmac_f32_16x16x64_bf16 v[20:23], v[0:3], v[80:87], v33 abid:1\n"
"s_waitcnt lgkmcnt(12)\n"
"ds_read_b128 v[72:75], v46 offset:24576\n"
"ds_read_b128 v[76:79], v46 offset:25600\n"
"v_mov_b64 v[26:27], 0\n"
"v_add_f32 v16, v16, v18\n"
"v_smfmac_f32_16x16x64_bf16 v[20:23], v[4:7], v[88:95], v33 abid:1\n"
"ds_read_b128 v[80:83], v46 offset:26624\n"
"s_waitcnt lgkmcnt(12)\n"
"ds_read_b128 v[84:87], v46 offset:27648\n"
"v_add_f32 v17, v17, v19\n"
"v_smfmac_f32_16x16x64_bf16 v[20:23], v[8:11], v[96:103], v33 abid:1\n"
"ds_read_b128 v[88:91], v46 offset:28672\n"
"ds_read_b128 v[92:95], v46 offset:29696\n"
"v_add_f32_dpp v18, v16, v17 quad_perm:[0,0,2,2] row_mask:0xf bank_mask:0xf\n"
"v_cndmask_b32 v30, v30, v18, s[18:19]\n"
"v_smfmac_f32_16x16x64_bf16 v[20:23], v[12:15], v[104:111], v33 abid:1\n"
"s_waitcnt lgkmcnt(12)\n"
"ds_read_b128 v[96:99], v46 offset:30720\n"
"ds_read_b128 v[100:103], v46 offset:31744\n"
"v_mov_b64 v[16:17], 0\n"
"v_mov_b64 v[18:19], 0\n"
"v_smfmac_f32_16x16x64_bf16 v[24:27], v[0:3], v[112:119], v33 abid:1\n"
"ds_read_b128 v[104:107], v46 offset:32768\n"
"s_waitcnt lgkmcnt(12)\n"
"ds_read_b128 v[108:111], v46 offset:33792\n"
"v_cvt_pk_bf16_f32 v30, 0, v30\n"
"v_mul_f32 v30, 0x3db504f3, v30\n"
"v_smfmac_f32_16x16x64_bf16 v[24:27], v[4:7], v[120:127], v33 abid:1\n"
"ds_read_b128 v[112:115], v46 offset:34816\n"
"ds_read_b128 v[116:119], v46 offset:35840\n"
"v_cvt_pk_bf16_f32 v30, 0, v30\n"
"v_mul_f32 v30, 0x3fb8aa3b, v30\n"
"v_smfmac_f32_16x16x64_bf16 v[24:27], v[8:11], v[128:135], v33 abid:1\n"
"s_waitcnt lgkmcnt(12)\n"
"ds_read_b128 v[120:123], v46 offset:36864\n"
"ds_read_b128 v[124:127], v46 offset:37888\n"
"v_add_f32 v21, v21, v23\n"
"v_add_f32 v20, v20, v22\n"
"v_smfmac_f32_16x16x64_bf16 v[24:27], v[12:15], v[136:143], v33 abid:1\n"
"s_nop 0\n"
"v_add_f32_dpp v31, v21, v20 quad_perm:[1,1,3,3] row_mask:0xf bank_mask:0xf\n"
"v_mov_b64 v[20:21], 0\n"
"v_smfmac_f32_16x16x64_bf16 v[16:19], v[0:3], v[144:151], v33 abid:1\n"
"buffer_load_dwordx4 v[128:131], v38, s[44:47], s72 offen offset:0 nt\n"
"buffer_load_dwordx4 v[132:135], v38, s[44:47], s72 offen offset:128 nt\n"
"v_mov_b64 v[22:23], 0\n"
"v_smfmac_f32_16x16x64_bf16 v[16:19], v[4:7], v[152:159], v33 abid:1\n"
"v_add_f32 v24, v24, v26\n"
"v_add_f32 v25, v25, v27\n"
"v_smfmac_f32_16x16x64_bf16 v[16:19], v[8:11], v[160:167], v33 abid:1\n"
"s_nop 0\n"
"v_add_f32_dpp v26, v24, v25 quad_perm:[0,0,2,2] row_mask:0xf bank_mask:0xf\n"
"v_cndmask_b32 v31, v31, v26, s[18:19]\n"
"v_smfmac_f32_16x16x64_bf16 v[16:19], v[12:15], v[168:175], v33 abid:1\n"
"v_mov_b64 v[24:25], 0\n"
"v_mov_b64 v[26:27], 0\n"
"v_smfmac_f32_16x16x64_bf16 v[20:23], v[0:3], v[48:55], v33 abid:1\n"
"v_cvt_pk_bf16_f32 v31, 0, v31\n"
"v_mul_f32 v31, 0x3db504f3, v31\n"
"v_cvt_pk_bf16_f32 v31, 0, v31\n"
"v_mul_f32 v31, 0x3fb8aa3b, v31\n"
"v_smfmac_f32_16x16x64_bf16 v[24:27], v[0:3], a[176:183], v33 abid:1\n"
"v_add_f32 v17, v17, v19\n"
"v_add_f32 v16, v16, v18\n"
"v_smfmac_f32_16x16x64_bf16 v[24:27], v[4:7], a[184:191], v33 abid:1\n"
"s_nop 0\n"
"v_add_f32_dpp v32, v17, v16 quad_perm:[1,1,3,3] row_mask:0xf bank_mask:0xf\n"
"v_mov_b64 v[16:17], 0\n"
"v_smfmac_f32_16x16x64_bf16 v[24:27], v[8:11], a[192:199], v33 abid:1\n"
"v_mov_b64 v[18:19], 0\n"
"v_add_f32 v20, v20, v22\n"
"v_smfmac_f32_16x16x64_bf16 v[24:27], v[12:15], a[200:207], v33 abid:1\n"
"v_add_f32 v21, v21, v23\n"
"v_smfmac_f32_16x16x64_bf16 v[16:19], v[0:3], a[208:215], v33 abid:1\n"
"s_nop 0\n"
"v_add_f32_dpp v22, v20, v21 quad_perm:[0,0,2,2] row_mask:0xf bank_mask:0xf\n"
"v_cndmask_b32 v32, v32, v22, s[18:19]\n"
"v_smfmac_f32_16x16x64_bf16 v[16:19], v[4:7], a[216:223], v33 abid:1\n"
"v_cvt_pk_bf16_f32 v32, 0, v32\n"
"v_mul_f32 v32, 0x3db504f3, v32\n"
"v_smfmac_f32_16x16x64_bf16 v[16:19], v[8:11], a[224:231], v33 abid:1\n"
"v_cvt_pk_bf16_f32 v32, 0, v32\n"
"v_mul_f32 v32, 0x3fb8aa3b, v32\n"
"v_smfmac_f32_16x16x64_bf16 v[16:19], v[12:15], a[232:239], v33 abid:1\n"
"v_add_f32 v25, v25, v27\n"
"v_add_f32 v24, v24, v26\n"
"v_mov_b32 v40, 0xff800000\n"
"s_nop 0\n"
"v_add_f32_dpp v28, v25, v24 quad_perm:[1,1,3,3] row_mask:0xf bank_mask:0xf\n"
"s_nop 2\n"
"v_add_f32 v16, v16, v18\n"
"v_add_f32 v17, v17, v19\n"
"s_nop 1\n"
"v_add_f32_dpp v18, v16, v17 quad_perm:[0,0,2,2] row_mask:0xf bank_mask:0xf\n"
"v_cndmask_b32 v28, v28, v18, s[18:19]\n"
"v_cvt_pk_bf16_f32 v28, 0, v28\n"
"v_mul_f32 v28, 0x3db504f3, v28\n"
"v_cvt_pk_bf16_f32 v28, 0, v28\n"
"v_mul_f32 v28, 0x3fb8aa3b, v28\n"
"s_lshl_b32 s88, s12, 8\n"
"s_add_u32 s75, s88, 16384\n"
"v_lshrrev_b32 v45, 2, v39\n"
"v_and_b32 v45, 0xc0, v45\n"
"v_and_or_b32 v45, v39, 48, v45\n"
"buffer_load_dwordx4 v[12:15], v45, s[52:55], s75 offen sc1\n"
"v_cndmask_b32 v32, v40, v32, s[84:85]\n"
"v_max3_f32 v42, v28, v29, v30\n"
"v_max3_f32 v42, v42, v31, v32\n"
"v_mov_b32 v16, 0\n"
"v_mov_b32 v17, 0\n"
"v_max_f32_dpp v42, v42, v42 row_ror:8 row_mask:0xf bank_mask:0xf\n"
"v_mov_b32 v18, 0\n"
"v_mov_b32 v19, 0\n"
"v_max_f32_dpp v42, v42, v42 row_ror:4 row_mask:0xf bank_mask:0xf\n"
"v_mov_b32 v20, 0\n"
"v_mov_b32 v21, 0\n"
"v_max_f32_dpp v42, v42, v42 row_ror:2 row_mask:0xf bank_mask:0xf\n"
"v_mov_b32 v22, 0\n"
"v_mov_b32 v23, 0\n"
"v_max_f32_dpp v42, v42, v42 row_ror:1 row_mask:0xf bank_mask:0xf\n"
"v_mov_b32 v34, 0\n"
"v_mov_b32 v35, 0\n"
"v_max_f32_dpp v42, v42, v42 row_bcast:15 row_mask:0xa bank_mask:0xf\n"
"v_mov_b32 v36, 0\n"
"v_mov_b32 v37, 0\n"
"v_max_f32_dpp v42, v42, v42 row_bcast:31 row_mask:0xc bank_mask:0xf\n"
"s_nop 0\n"
"v_readlane_b32 s88, v42, 63\n"
"s_nop 1\n"
"v_subrev_f32 v28, s88, v28\n"
"v_subrev_f32 v29, s88, v29\n"
"v_subrev_f32 v30, s88, v30\n"
"v_subrev_f32 v31, s88, v31\n"
"v_subrev_f32 v32, s88, v32\n"
"v_exp_f32 v28, v28\n"
"v_exp_f32 v29, v29\n"
"v_exp_f32 v30, v30\n"
"v_exp_f32 v31, v31\n"
"v_exp_f32 v32, v32\n"
"v_add_f32 v43, v28, v29\n"
"v_add_f32 v40, v30, v31\n"
"v_cvt_pk_bf16_f32 v28, v28, 0\n"
"v_add_f32 v43, v43, v32\n"
"v_cvt_pk_bf16_f32 v29, v29, 0\n"
"v_add_f32 v43, v43, v40\n"
"v_cvt_pk_bf16_f32 v30, v30, 0\n"
"v_cvt_pk_bf16_f32 v31, v31, 0\n"
"v_cvt_pk_bf16_f32 v32, v32, 0\n"
"ds_bpermute_b32 v28, v41, v28\n"
"s_waitcnt lgkmcnt(12)\n"
"ds_bpermute_b32 v29, v41, v29\n"
"ds_bpermute_b32 v30, v41, v30\n"
"ds_bpermute_b32 v31, v41, v31\n"
"s_waitcnt lgkmcnt(12)\n"
"ds_bpermute_b32 v32, v41, v32\n"
"buffer_load_dwordx4 v[136:139], v38, s[44:47], s72 offen offset:2048 nt\n"
"buffer_load_dwordx4 v[140:143], v38, s[44:47], s72 offen offset:2176 nt\n"
"s_waitcnt lgkmcnt(1)\n"
"v_mov_b32_dpp v20, v31 row_newbcast:4 row_mask:0xf bank_mask:0x1\n"
"v_mov_b32_dpp v21, v31 row_newbcast:4 row_mask:0xf bank_mask:0x2\n"
"v_mov_b32_dpp v22, v31 row_newbcast:5 row_mask:0xf bank_mask:0x1\n"
"v_mov_b32_dpp v23, v31 row_newbcast:5 row_mask:0xf bank_mask:0x2\n"
"v_add_f32_dpp v43, v43, v43 row_ror:8 row_mask:0xf bank_mask:0xf\n"
"v_mov_b32_dpp v16, v31 row_newbcast:6 row_mask:0xf bank_mask:0x1\n"
"v_mov_b32_dpp v17, v31 row_newbcast:6 row_mask:0xf bank_mask:0x2\n"
"v_mov_b32_dpp v18, v31 row_newbcast:7 row_mask:0xf bank_mask:0x1\n"
"v_mov_b32_dpp v19, v31 row_newbcast:7 row_mask:0xf bank_mask:0x2\n"
"v_add_f32_dpp v43, v43, v43 row_ror:4 row_mask:0xf bank_mask:0xf\n"
"v_smfmac_f32_16x16x64_bf16 v[34:37], v[20:23], a[240:247], v33\n"
"v_mov_b32_dpp v20, v31 row_newbcast:8 row_mask:0xf bank_mask:0x1\n"
"v_mov_b32_dpp v21, v31 row_newbcast:8 row_mask:0xf bank_mask:0x2\n"
"v_mov_b32_dpp v22, v31 row_newbcast:9 row_mask:0xf bank_mask:0x1\n"
"v_mov_b32_dpp v23, v31 row_newbcast:9 row_mask:0xf bank_mask:0x2\n"
"v_add_f32_dpp v43, v43, v43 row_ror:2 row_mask:0xf bank_mask:0xf\n"
"v_smfmac_f32_16x16x64_bf16 v[34:37], v[16:19], a[248:255], v33\n"
"v_mov_b32_dpp v16, v31 row_newbcast:10 row_mask:0xf bank_mask:0x1\n"
"v_mov_b32_dpp v17, v31 row_newbcast:10 row_mask:0xf bank_mask:0x2\n"
"v_mov_b32_dpp v18, v31 row_newbcast:11 row_mask:0xf bank_mask:0x1\n"
"v_mov_b32_dpp v19, v31 row_newbcast:11 row_mask:0xf bank_mask:0x2\n"
"v_add_f32_dpp v43, v43, v43 row_ror:1 row_mask:0xf bank_mask:0xf\n"
"v_smfmac_f32_16x16x64_bf16 v[34:37], v[20:23], v[56:63], v33\n"
"v_mov_b32_dpp v20, v31 row_newbcast:12 row_mask:0xf bank_mask:0x1\n"
"v_mov_b32_dpp v21, v31 row_newbcast:12 row_mask:0xf bank_mask:0x2\n"
"v_mov_b32_dpp v22, v31 row_newbcast:13 row_mask:0xf bank_mask:0x1\n"
"v_mov_b32_dpp v23, v31 row_newbcast:13 row_mask:0xf bank_mask:0x2\n"
"v_add_f32_dpp v43, v43, v43 row_bcast:15 row_mask:0xa bank_mask:0xf\n"
"v_smfmac_f32_16x16x64_bf16 v[34:37], v[16:19], v[64:71], v33\n"
"v_mov_b32_dpp v16, v31 row_newbcast:14 row_mask:0xf bank_mask:0x1\n"
"v_mov_b32_dpp v17, v31 row_newbcast:14 row_mask:0xf bank_mask:0x2\n"
"v_mov_b32_dpp v18, v31 row_newbcast:15 row_mask:0xf bank_mask:0x1\n"
"v_mov_b32_dpp v19, v31 row_newbcast:15 row_mask:0xf bank_mask:0x2\n"
"v_add_f32_dpp v43, v43, v43 row_bcast:31 row_mask:0xc bank_mask:0xf\n"
"v_smfmac_f32_16x16x64_bf16 v[34:37], v[20:23], v[72:79], v33\n"
"s_waitcnt lgkmcnt(0)\n"
"v_mov_b32_dpp v20, v32 row_newbcast:0 row_mask:0xf bank_mask:0x1\n"
"v_mov_b32_dpp v21, v32 row_newbcast:0 row_mask:0xf bank_mask:0x2\n"
"v_mov_b32_dpp v22, v32 row_newbcast:1 row_mask:0xf bank_mask:0x1\n"
"v_mov_b32_dpp v23, v32 row_newbcast:1 row_mask:0xf bank_mask:0x2\n"
"s_lshl_b32 s88, s15, 3\n"
"v_mov_b32 v40, s88\n"
"s_mov_b32 exec_lo, 0\n"
"s_and_b32 exec_hi, exec_hi, 0x80000000\n"
"ds_write_b64 v40, v[42:43] offset:4672\n"
"s_mov_b64 exec, -1\n"
"v_smfmac_f32_16x16x64_bf16 v[34:37], v[16:19], v[80:87], v33\n"
"v_mov_b32_dpp v16, v32 row_newbcast:2 row_mask:0xf bank_mask:0x1\n"
"v_mov_b32_dpp v17, v32 row_newbcast:2 row_mask:0xf bank_mask:0x2\n"
"v_mov_b32_dpp v18, v32 row_newbcast:3 row_mask:0xf bank_mask:0x1\n"
"v_mov_b32_dpp v19, v32 row_newbcast:3 row_mask:0xf bank_mask:0x2\n"
"v_smfmac_f32_16x16x64_bf16 v[34:37], v[20:23], v[88:95], v33\n"
"v_mov_b32_dpp v20, v32 row_newbcast:4 row_mask:0xf bank_mask:0x1\n"
"v_mov_b32_dpp v21, v32 row_newbcast:4 row_mask:0xf bank_mask:0x2\n"
"v_mov_b32_dpp v22, v32 row_newbcast:5 row_mask:0xf bank_mask:0x1\n"
"v_mov_b32_dpp v23, v32 row_newbcast:5 row_mask:0xf bank_mask:0x2\n"
"v_smfmac_f32_16x16x64_bf16 v[34:37], v[16:19], v[96:103], v33\n"
"v_mov_b32_dpp v16, v32 row_newbcast:6 row_mask:0xf bank_mask:0x1\n"
"v_mov_b32_dpp v17, v32 row_newbcast:6 row_mask:0xf bank_mask:0x2\n"
"v_mov_b32_dpp v18, v32 row_newbcast:7 row_mask:0xf bank_mask:0x1\n"
"v_mov_b32_dpp v19, v32 row_newbcast:7 row_mask:0xf bank_mask:0x2\n"
"v_smfmac_f32_16x16x64_bf16 v[34:37], v[20:23], v[104:111], v33\n"
"v_mov_b32_dpp v20, v32 row_newbcast:8 row_mask:0xf bank_mask:0x1\n"
"v_mov_b32_dpp v21, v32 row_newbcast:8 row_mask:0xf bank_mask:0x2\n"
"v_mov_b32_dpp v22, v32 row_newbcast:9 row_mask:0xf bank_mask:0x1\n"
"v_mov_b32_dpp v23, v32 row_newbcast:9 row_mask:0xf bank_mask:0x2\n"
"v_smfmac_f32_16x16x64_bf16 v[34:37], v[16:19], v[112:119], v33\n"
"s_nop 0\n"
"v_smfmac_f32_16x16x64_bf16 v[34:37], v[20:23], v[120:127], v33\n"
"v_mov_b64 v[16:17], 0\n"
"v_mov_b64 v[18:19], 0\n"
"v_mov_b64 v[20:21], 0\n"
"v_mov_b64 v[22:23], 0\n"
"v_mov_b64 v[24:25], 0\n"
"v_mov_b64 v[26:27], 0\n"
"v_lshrrev_b32 v40, 8, v39\n"
"v_bfe_u32 v41, v39, 7, 1\n"
"v_lshl_or_b32 v41, v40, 2, v41\n"
"v_bfe_u32 v40, v39, 4, 1\n"
"v_lshl_or_b32 v41, v40, 1, v41\n"
"v_bfe_u32 v40, v39, 5, 2\n"
"v_lshl_or_b32 v41, v40, 4, v41\n"
"v_lshlrev_b32 v41, 2, v41\n"
"s_waitcnt lgkmcnt(0)\n"
"s_memrealtime s[92:93]\n"
"s_waitcnt vmcnt(11)\n"
"s_waitcnt lgkmcnt(0)\n"
"s_sub_u32 s93, s92, s20\n"
"s_cmp_ge_u32 s93, 3134\n"
"s_cbranch_scc1 .Lk_tok_102\n"
".Lk_tlp_103:\n"
"s_memrealtime s[92:93]\n"
"s_waitcnt lgkmcnt(0)\n"
"s_sub_u32 s93, s92, s20\n"
"s_cmp_ge_u32 s93, 3134\n"
"s_cbranch_scc0 .Lk_tlp_103\n"
".Lk_tok_102:\n"
"buffer_load_dwordx4 v[144:147], v38, s[44:47], s73 offen offset:0 nt\n"
"buffer_load_dwordx4 v[148:151], v38, s[44:47], s73 offen offset:128 nt\n"
"s_waitcnt vmcnt(11)\n"
"buffer_load_dwordx4 v[152:155], v38, s[44:47], s73 offen offset:2048 nt\n"
"buffer_load_dwordx4 v[156:159], v38, s[44:47], s73 offen offset:2176 nt\n"
"s_memrealtime s[92:93]\n"
"s_waitcnt vmcnt(11)\n"
"s_waitcnt lgkmcnt(0)\n"
"s_sub_u32 s93, s92, s20\n"
"s_cmp_ge_u32 s93, 3183\n"
"s_cbranch_scc1 .Lk_tok_104\n"
".Lk_tlp_105:\n"
"s_memrealtime s[92:93]\n"
"s_waitcnt lgkmcnt(0)\n"
"s_sub_u32 s93, s92, s20\n"
"s_cmp_ge_u32 s93, 3183\n"
"s_cbranch_scc0 .Lk_tlp_105\n"
".Lk_tok_104:\n"
"buffer_load_dwordx4 v[160:163], v38, s[44:47], s74 offen offset:0 nt\n"
"buffer_load_dwordx4 v[164:167], v38, s[44:47], s74 offen offset:128 nt\n"
"s_waitcnt vmcnt(11)\n"
"buffer_load_dwordx4 v[168:171], v39, s[48:51], s56 offen offset:0 nt\n"
"buffer_load_dwordx4 v[172:175], v39, s[48:51], s56 offen offset:1024 nt\n"
"s_memrealtime s[92:93]\n"
"s_waitcnt vmcnt(11)\n"
"s_waitcnt lgkmcnt(0)\n"
"s_sub_u32 s93, s92, s20\n"
"s_cmp_ge_u32 s93, 3232\n"
"s_cbranch_scc1 .Lk_tok_106\n"
".Lk_tlp_107:\n"
"s_memrealtime s[92:93]\n"
"s_waitcnt lgkmcnt(0)\n"
"s_sub_u32 s93, s92, s20\n"
"s_cmp_ge_u32 s93, 3232\n"
"s_cbranch_scc0 .Lk_tlp_107\n"
".Lk_tok_106:\n"
"buffer_load_dwordx4 a[176:179], v39, s[48:51], s56 offen offset:2048 nt\n"
"buffer_load_dwordx4 a[180:183], v39, s[48:51], s56 offen offset:3072 nt\n"
"s_waitcnt vmcnt(10)\n"
"buffer_load_dwordx4 a[184:187], v39, s[48:51], s57 offen offset:0 nt\n"
"buffer_load_dwordx4 a[188:191], v39, s[48:51], s57 offen offset:1024 nt\n"
"s_memrealtime s[92:93]\n"
"s_waitcnt vmcnt(10)\n"
"s_waitcnt lgkmcnt(0)\n"
"s_sub_u32 s93, s92, s20\n"
"s_cmp_ge_u32 s93, 3282\n"
"s_cbranch_scc1 .Lk_tok_108\n"
".Lk_tlp_109:\n"
"s_memrealtime s[92:93]\n"
"s_waitcnt lgkmcnt(0)\n"
"s_sub_u32 s93, s92, s20\n"
"s_cmp_ge_u32 s93, 3282\n"
"s_cbranch_scc0 .Lk_tlp_109\n"
".Lk_tok_108:\n"
"buffer_load_dwordx4 a[192:195], v39, s[48:51], s57 offen offset:2048 nt\n"
"buffer_load_dwordx4 a[196:199], v39, s[48:51], s57 offen offset:3072 nt\n"
"s_waitcnt vmcnt(10)\n"
"buffer_load_dwordx4 a[200:203], v39, s[48:51], s58 offen offset:0 nt\n"
"buffer_load_dwordx4 a[204:207], v39, s[48:51], s58 offen offset:1024 nt\n"
"s_memrealtime s[92:93]\n"
"s_waitcnt vmcnt(10)\n"
"s_waitcnt lgkmcnt(0)\n"
"s_sub_u32 s93, s92, s20\n"
"s_cmp_ge_u32 s93, 3331\n"
"s_cbranch_scc1 .Lk_tok_110\n"
".Lk_tlp_111:\n"
"s_memrealtime s[92:93]\n"
"s_waitcnt lgkmcnt(0)\n"
"s_sub_u32 s93, s92, s20\n"
"s_cmp_ge_u32 s93, 3331\n"
"s_cbranch_scc0 .Lk_tlp_111\n"
".Lk_tok_110:\n"
"buffer_load_dwordx4 a[208:211], v39, s[48:51], s58 offen offset:2048 nt\n"
"buffer_load_dwordx4 a[212:215], v39, s[48:51], s58 offen offset:3072 nt\n"
"v_max3_u32 v44, v12, v13, v14\n"
"v_max_u32 v44, v44, v15\n"
"v_cmp_eq_u32 vcc, -1, v44\n"
"s_cbranch_vccz .Lk_qok_112\n"
".Lk_qlp_113:\n"
"s_cmp_eq_u32 s96, 0\n"
"s_cbranch_scc1 .Lk_qok_112\n"
"s_sub_u32 s96, s96, 1\n"
"buffer_load_dwordx4 v[12:15], v45, s[52:55], s75 offen sc1\n"
"s_waitcnt vmcnt(0)\n"
"v_max3_u32 v44, v12, v13, v14\n"
"v_max_u32 v44, v44, v15\n"
"v_cmp_eq_u32 vcc, -1, v44\n"
"s_cbranch_vccnz .Lk_qlp_113\n"
".Lk_qok_112:\n"
"v_cndmask_b32 v0, 0, v12, s[76:77]\n"
"v_cndmask_b32 v1, 0, v13, s[76:77]\n"
"v_cndmask_b32 v2, 0, v14, s[76:77]\n"
"v_cndmask_b32 v3, 0, v15, s[76:77]\n"
"v_cndmask_b32 v4, 0, v12, s[78:79]\n"
"v_cndmask_b32 v5, 0, v13, s[78:79]\n"
"v_smfmac_f32_16x16x64_bf16 v[16:19], v[0:3], a[64:71], v33 abid:1\n"
"v_cndmask_b32 v6, 0, v14, s[78:79]\n"
"v_cndmask_b32 v7, 0, v15, s[78:79]\n"
"v_cndmask_b32 v8, 0, v12, s[80:81]\n"
"v_cndmask_b32 v9, 0, v13, s[80:81]\n"
"v_smfmac_f32_16x16x64_bf16 v[16:19], v[4:7], a[72:79], v33 abid:1\n"
"v_cndmask_b32 v10, 0, v14, s[80:81]\n"
"v_cndmask_b32 v11, 0, v15, s[80:81]\n"
"v_cndmask_b32 v12, 0, v12, s[82:83]\n"
"v_cndmask_b32 v13, 0, v13, s[82:83]\n"
"v_smfmac_f32_16x16x64_bf16 v[16:19], v[8:11], a[80:87], v33 abid:1\n"
"v_cndmask_b32 v14, 0, v14, s[82:83]\n"
"v_cndmask_b32 v15, 0, v15, s[82:83]\n"
"s_nop 1\n"
"v_smfmac_f32_16x16x64_bf16 v[16:19], v[12:15], v[176:183], v33 abid:1\n"
"v_smfmac_f32_16x16x64_bf16 v[20:23], v[0:3], v[184:191], v33 abid:1\n"
"v_smfmac_f32_16x16x64_bf16 v[20:23], v[4:7], v[192:199], v33 abid:1\n"
"v_smfmac_f32_16x16x64_bf16 v[20:23], v[8:11], v[200:207], v33 abid:1\n"
"buffer_load_dwordx4 a[216:219], v39, s[48:51], s59 offen offset:0 nt\n"
"buffer_load_dwordx4 a[220:223], v39, s[48:51], s59 offen offset:1024 nt\n"
"v_smfmac_f32_16x16x64_bf16 v[20:23], v[12:15], v[208:215], v33 abid:1\n"
"v_smfmac_f32_16x16x64_bf16 v[24:27], v[0:3], v[216:223], v33 abid:1\n"
"v_smfmac_f32_16x16x64_bf16 v[24:27], v[4:7], v[224:231], v33 abid:1\n"
"v_add_f32 v17, v17, v19\n"
"v_add_f32 v16, v16, v18\n"
"v_smfmac_f32_16x16x64_bf16 v[24:27], v[8:11], v[232:239], v33 abid:1\n"
"s_nop 0\n"
"v_add_f32_dpp v47, v17, v16 quad_perm:[1,1,3,3] row_mask:0xf bank_mask:0xf\n"
"v_mov_b64 v[16:17], 0\n"
"v_smfmac_f32_16x16x64_bf16 v[24:27], v[12:15], v[240:247], v33 abid:1\n"
"v_mov_b64 v[18:19], 0\n"
"v_add_f32 v20, v20, v22\n"
"s_nop 0\n"
"v_smfmac_f32_16x16x64_bf16 v[16:19], v[0:3], v[248:255], v33 abid:1\n"
"v_add_f32 v21, v21, v23\n"
"v_smfmac_f32_16x16x64_bf16 v[16:19], v[4:7], a[88:95], v33 abid:1\n"
"s_nop 0\n"
"v_add_f32_dpp v22, v20, v21 quad_perm:[0,0,2,2] row_mask:0xf bank_mask:0xf\n"
"v_cndmask_b32 v47, v47, v22, s[18:19]\n"
"v_smfmac_f32_16x16x64_bf16 v[16:19], v[8:11], a[96:103], v33 abid:1\n"
"v_mov_b64 v[20:21], 0\n"
"v_mov_b64 v[22:23], 0\n"
"v_smfmac_f32_16x16x64_bf16 v[16:19], v[12:15], a[104:111], v33 abid:1\n"
"v_cvt_pk_bf16_f32 v47, 0, v47\n"
"v_mul_f32 v47, 0x3db504f3, v47\n"
"v_cvt_pk_bf16_f32 v47, 0, v47\n"
"v_mul_f32 v47, 0x3fb8aa3b, v47\n"
"v_smfmac_f32_16x16x64_bf16 v[20:23], v[0:3], a[112:119], v33 abid:1\n"
"v_add_f32 v25, v25, v27\n"
"v_add_f32 v24, v24, v26\n"
"v_smfmac_f32_16x16x64_bf16 v[20:23], v[4:7], a[120:127], v33 abid:1\n"
"s_nop 0\n"
"v_add_f32_dpp v48, v25, v24 quad_perm:[1,1,3,3] row_mask:0xf bank_mask:0xf\n"
"v_mov_b64 v[24:25], 0\n"
"v_smfmac_f32_16x16x64_bf16 v[20:23], v[8:11], a[128:135], v33 abid:1\n"
"buffer_load_dwordx4 a[224:227], v39, s[48:51], s59 offen offset:2048 nt\n"
"buffer_load_dwordx4 a[228:231], v39, s[48:51], s59 offen offset:3072 nt\n"
"v_mov_b64 v[26:27], 0\n"
"v_add_f32 v16, v16, v18\n"
"v_smfmac_f32_16x16x64_bf16 v[20:23], v[12:15], a[136:143], v33 abid:1\n"
"v_add_f32 v17, v17, v19\n"
"v_smfmac_f32_16x16x64_bf16 v[24:27], v[0:3], a[144:151], v33 abid:1\n"
"s_nop 0\n"
"v_add_f32_dpp v18, v16, v17 quad_perm:[0,0,2,2] row_mask:0xf bank_mask:0xf\n"
"v_cndmask_b32 v48, v48, v18, s[18:19]\n"
"v_smfmac_f32_16x16x64_bf16 v[24:27], v[4:7], a[0:7], v33 abid:1\n"
"v_mov_b64 v[16:17], 0\n"
"v_mov_b64 v[18:19], 0\n"
"v_smfmac_f32_16x16x64_bf16 v[24:27], v[8:11], a[8:15], v33 abid:1\n"
"v_cvt_pk_bf16_f32 v48, 0, v48\n"
"v_mul_f32 v48, 0x3db504f3, v48\n"
"v_smfmac_f32_16x16x64_bf16 v[24:27], v[12:15], a[16:23], v33 abid:1\n"
"v_cvt_pk_bf16_f32 v48, 0, v48\n"
"v_mul_f32 v48, 0x3fb8aa3b, v48\n"
"v_smfmac_f32_16x16x64_bf16 v[16:19], v[0:3], a[24:31], v33 abid:1\n"
"v_add_f32 v21, v21, v23\n"
"v_add_f32 v20, v20, v22\n"
"v_smfmac_f32_16x16x64_bf16 v[16:19], v[4:7], a[32:39], v33 abid:1\n"
"s_nop 0\n"
"v_add_f32_dpp v49, v21, v20 quad_perm:[1,1,3,3] row_mask:0xf bank_mask:0xf\n"
"v_mov_b64 v[20:21], 0\n"
"v_smfmac_f32_16x16x64_bf16 v[16:19], v[8:11], a[40:47], v33 abid:1\n"
"v_mov_b64 v[22:23], 0\n"
"v_add_f32 v24, v24, v26\n"
"v_smfmac_f32_16x16x64_bf16 v[16:19], v[12:15], a[48:55], v33 abid:1\n"
"v_add_f32 v25, v25, v27\n"
"v_smfmac_f32_16x16x64_bf16 v[20:23], v[0:3], a[56:63], v33 abid:1\n"
"s_nop 0\n"
"v_add_f32_dpp v26, v24, v25 quad_perm:[0,0,2,2] row_mask:0xf bank_mask:0xf\n"
"v_cndmask_b32 v49, v49, v26, s[18:19]\n"
"v_smfmac_f32_16x16x64_bf16 v[20:23], v[4:7], a[152:159], v33 abid:1\n"
"v_mov_b64 v[24:25], 0\n"
"v_mov_b64 v[26:27], 0\n"
"v_smfmac_f32_16x16x64_bf16 v[20:23], v[8:11], a[160:167], v33 abid:1\n"
"buffer_load_dwordx4 a[232:235], v39, s[48:51], s60 offen offset:0 nt\n"
"buffer_load_dwordx4 a[236:239], v39, s[48:51], s60 offen offset:1024 nt\n"
"v_cvt_pk_bf16_f32 v49, 0, v49\n"
"v_mul_f32 v49, 0x3db504f3, v49\n"
"v_smfmac_f32_16x16x64_bf16 v[20:23], v[12:15], a[168:175], v33 abid:1\n"
"v_cvt_pk_bf16_f32 v49, 0, v49\n"
"v_mul_f32 v49, 0x3fb8aa3b, v49\n"
"v_smfmac_f32_16x16x64_bf16 v[24:27], v[0:3], v[128:135], v33 abid:1\n"
"v_add_f32 v17, v17, v19\n"
"v_add_f32 v16, v16, v18\n"
"v_smfmac_f32_16x16x64_bf16 v[24:27], v[4:7], v[136:143], v33 abid:1\n"
"s_nop 0\n"
"v_add_f32_dpp v50, v17, v16 quad_perm:[1,1,3,3] row_mask:0xf bank_mask:0xf\n"
"v_mov_b64 v[16:17], 0\n"
"v_smfmac_f32_16x16x64_bf16 v[24:27], v[8:11], v[144:151], v33 abid:1\n"
"v_mov_b64 v[18:19], 0\n"
"v_add_f32 v20, v20, v22\n"
"v_smfmac_f32_16x16x64_bf16 v[24:27], v[12:15], v[152:159], v33 abid:1\n"
"v_add_f32 v21, v21, v23\n"
"v_smfmac_f32_16x16x64_bf16 v[16:19], v[0:3], v[160:167], v33 abid:1\n"
"s_nop 0\n"
"v_add_f32_dpp v22, v20, v21 quad_perm:[0,0,2,2] row_mask:0xf bank_mask:0xf\n"
"v_cndmask_b32 v50, v50, v22, s[18:19]\n"
"v_mov_b32 v40, 0xff800000\n"
"v_cvt_pk_bf16_f32 v50, 0, v50\n"
"v_mul_f32 v50, 0x3db504f3, v50\n"
"v_cvt_pk_bf16_f32 v50, 0, v50\n"
"v_mul_f32 v50, 0x3fb8aa3b, v50\n"
"v_add_f32 v25, v25, v27\n"
"v_add_f32 v24, v24, v26\n"
"v_add_f32 v16, v16, v18\n"
"s_nop 0\n"
"v_add_f32_dpp v51, v25, v24 quad_perm:[1,1,3,3] row_mask:0xf bank_mask:0xf\n"
"v_add_f32 v17, v17, v19\n"
"s_nop 1\n"
"v_add_f32_dpp v18, v16, v17 quad_perm:[0,0,2,2] row_mask:0xf bank_mask:0xf\n"
"v_cndmask_b32 v51, v51, v18, s[18:19]\n"
"v_cvt_pk_bf16_f32 v51, 0, v51\n"
"v_mul_f32 v51, 0x3db504f3, v51\n"
"v_cvt_pk_bf16_f32 v51, 0, v51\n"
"v_mul_f32 v51, 0x3fb8aa3b, v51\n"
"v_cndmask_b32 v51, v40, v51, s[84:85]\n"
"v_max3_f32 v42, v47, v48, v49\n"
"v_max3_f32 v42, v42, v50, v51\n"
"v_mov_b32 v16, 0\n"
"v_mov_b32 v17, 0\n"
"v_max_f32_dpp v42, v42, v42 row_ror:8 row_mask:0xf bank_mask:0xf\n"
"v_mov_b32 v18, 0\n"
"v_mov_b32 v19, 0\n"
"v_max_f32_dpp v42, v42, v42 row_ror:4 row_mask:0xf bank_mask:0xf\n"
"v_mov_b32 v20, 0\n"
"v_mov_b32 v21, 0\n"
"v_max_f32_dpp v42, v42, v42 row_ror:2 row_mask:0xf bank_mask:0xf\n"
"v_mov_b32 v22, 0\n"
"v_mov_b32 v23, 0\n"
"v_max_f32_dpp v42, v42, v42 row_ror:1 row_mask:0xf bank_mask:0xf\n"
"v_mov_b32 v52, 0\n"
"v_mov_b32 v53, 0\n"
"v_max_f32_dpp v42, v42, v42 row_bcast:15 row_mask:0xa bank_mask:0xf\n"
"v_mov_b32 v54, 0\n"
"v_mov_b32 v55, 0\n"
"v_max_f32_dpp v42, v42, v42 row_bcast:31 row_mask:0xc bank_mask:0xf\n"
"s_nop 0\n"
"v_readlane_b32 s88, v42, 63\n"
"s_nop 1\n"
"v_subrev_f32 v47, s88, v47\n"
"v_subrev_f32 v48, s88, v48\n"
"v_subrev_f32 v49, s88, v49\n"
"v_subrev_f32 v50, s88, v50\n"
"v_subrev_f32 v51, s88, v51\n"
"v_exp_f32 v47, v47\n"
"v_exp_f32 v48, v48\n"
"v_exp_f32 v49, v49\n"
"v_exp_f32 v50, v50\n"
"v_exp_f32 v51, v51\n"
"v_add_f32 v43, v47, v48\n"
"v_add_f32 v40, v49, v50\n"
"v_cvt_pk_bf16_f32 v47, v47, 0\n"
"v_add_f32 v43, v43, v51\n"
"v_cvt_pk_bf16_f32 v48, v48, 0\n"
"v_add_f32 v43, v43, v40\n"
"v_cvt_pk_bf16_f32 v49, v49, 0\n"
"v_cvt_pk_bf16_f32 v50, v50, 0\n"
"v_cvt_pk_bf16_f32 v51, v51, 0\n"
"ds_bpermute_b32 v47, v41, v47\n"
"ds_bpermute_b32 v48, v41, v48\n"
"ds_bpermute_b32 v49, v41, v49\n"
"ds_bpermute_b32 v50, v41, v50\n"
"ds_bpermute_b32 v51, v41, v51\n"
"s_waitcnt lgkmcnt(4)\n"
"v_mov_b32_dpp v16, v47 row_newbcast:0 row_mask:0xf bank_mask:0x1\n"
"v_mov_b32_dpp v17, v47 row_newbcast:0 row_mask:0xf bank_mask:0x2\n"
"v_mov_b32_dpp v18, v47 row_newbcast:1 row_mask:0xf bank_mask:0x1\n"
"v_mov_b32_dpp v19, v47 row_newbcast:1 row_mask:0xf bank_mask:0x2\n"
"v_add_f32_dpp v43, v43, v43 row_ror:8 row_mask:0xf bank_mask:0xf\n"
"v_mov_b32_dpp v20, v47 row_newbcast:2 row_mask:0xf bank_mask:0x1\n"
"v_mov_b32_dpp v21, v47 row_newbcast:2 row_mask:0xf bank_mask:0x2\n"
"v_mov_b32_dpp v22, v47 row_newbcast:3 row_mask:0xf bank_mask:0x1\n"
"v_mov_b32_dpp v23, v47 row_newbcast:3 row_mask:0xf bank_mask:0x2\n"
"v_add_f32_dpp v43, v43, v43 row_ror:4 row_mask:0xf bank_mask:0xf\n"
"s_waitcnt vmcnt(16)\n"
"v_smfmac_f32_16x16x64_bf16 v[52:55], v[16:19], v[168:175], v33\n"
"ds_write_b128 v46, v[168:171] offset:0\n"
"ds_write_b128 v46, v[172:175] offset:1024\n"
"v_mov_b32_dpp v16, v47 row_newbcast:4 row_mask:0xf bank_mask:0x1\n"
"v_mov_b32_dpp v17, v47 row_newbcast:4 row_mask:0xf bank_mask:0x2\n"
"v_mov_b32_dpp v18, v47 row_newbcast:5 row_mask:0xf bank_mask:0x1\n"
"v_mov_b32_dpp v19, v47 row_newbcast:5 row_mask:0xf bank_mask:0x2\n"
"v_add_f32_dpp v43, v43, v43 row_ror:2 row_mask:0xf bank_mask:0xf\n"
"s_waitcnt vmcnt(14)\n"
"v_smfmac_f32_16x16x64_bf16 v[52:55], v[20:23], a[176:183], v33\n"
"ds_write_b128 v46, a[176:179] offset:2048\n"
"ds_write_b128 v46, a[180:183] offset:3072\n"
"v_mov_b32_dpp v20, v47 row_newbcast:6 row_mask:0xf bank_mask:0x1\n"
"v_mov_b32_dpp v21, v47 row_newbcast:6 row_mask:0xf bank_mask:0x2\n"
"v_mov_b32_dpp v22, v47 row_newbcast:7 row_mask:0xf bank_mask:0x1\n"
"v_mov_b32_dpp v23, v47 row_newbcast:7 row_mask:0xf bank_mask:0x2\n"
"v_add_f32_dpp v43, v43, v43 row_ror:1 row_mask:0xf bank_mask:0xf\n"
"s_waitcnt vmcnt(12)\n"
"v_smfmac_f32_16x16x64_bf16 v[52:55], v[16:19], a[184:191], v33\n"
"ds_write_b128 v46, a[184:187] offset:4096\n"
"ds_write_b128 v46, a[188:191] offset:5120\n"
"v_mov_b32_dpp v16, v47 row_newbcast:8 row_mask:0xf bank_mask:0x1\n"
"v_mov_b32_dpp v17, v47 row_newbcast:8 row_mask:0xf bank_mask:0x2\n"
"v_mov_b32_dpp v18, v47 row_newbcast:9 row_mask:0xf bank_mask:0x1\n"
"v_mov_b32_dpp v19, v47 row_newbcast:9 row_mask:0xf bank_mask:0x2\n"
"v_add_f32_dpp v43, v43, v43 row_bcast:15 row_mask:0xa bank_mask:0xf\n"
"s_waitcnt vmcnt(10)\n"
"v_smfmac_f32_16x16x64_bf16 v[52:55], v[20:23], a[192:199], v33\n"
"ds_write_b128 v46, a[192:195] offset:6144\n"
"ds_write_b128 v46, a[196:199] offset:7168\n"
"s_waitcnt lgkmcnt(0)\n"
"s_memrealtime s[92:93]\n"
"s_waitcnt lgkmcnt(0)\n"
"s_sub_u32 s93, s92, s20\n"
"s_cmp_ge_u32 s93, 3430\n"
"s_cbranch_scc1 .Lk_tok_114\n"
".Lk_tlp_115:\n"
"s_memrealtime s[92:93]\n"
"s_waitcnt lgkmcnt(0)\n"
"s_sub_u32 s93, s92, s20\n"
"s_cmp_ge_u32 s93, 3430\n"
"s_cbranch_scc0 .Lk_tlp_115\n"
".Lk_tok_114:\n"
"buffer_load_dwordx4 a[240:243], v39, s[48:51], s60 offen offset:2048 nt\n"
"buffer_load_dwordx4 a[244:247], v39, s[48:51], s60 offen offset:3072 nt\n"
"v_mov_b32_dpp v20, v47 row_newbcast:10 row_mask:0xf bank_mask:0x1\n"
"v_mov_b32_dpp v21, v47 row_newbcast:10 row_mask:0xf bank_mask:0x2\n"
"v_mov_b32_dpp v22, v47 row_newbcast:11 row_mask:0xf bank_mask:0x1\n"
"v_mov_b32_dpp v23, v47 row_newbcast:11 row_mask:0xf bank_mask:0x2\n"
"v_add_f32_dpp v43, v43, v43 row_bcast:31 row_mask:0xc bank_mask:0xf\n"
"s_waitcnt vmcnt(10)\n"
"v_smfmac_f32_16x16x64_bf16 v[52:55], v[16:19], a[200:207], v33\n"
"ds_write_b128 v46, a[200:203] offset:8192\n"
"ds_write_b128 v46, a[204:207] offset:9216\n"
"buffer_load_dwordx4 a[248:251], v39, s[48:51], s61 offen offset:0 nt\n"
"buffer_load_dwordx4 a[252:255], v39, s[48:51], s61 offen offset:1024 nt\n"
"v_mov_b32_dpp v16, v47 row_newbcast:12 row_mask:0xf bank_mask:0x1\n"
"v_mov_b32_dpp v17, v47 row_newbcast:12 row_mask:0xf bank_mask:0x2\n"
"v_mov_b32_dpp v18, v47 row_newbcast:13 row_mask:0xf bank_mask:0x1\n"
"v_mov_b32_dpp v19, v47 row_newbcast:13 row_mask:0xf bank_mask:0x2\n"
"s_lshl_b32 s88, s15, 3\n"
"v_mov_b32 v40, s88\n"
"s_mov_b32 exec_lo, 0\n"
"s_and_b32 exec_hi, exec_hi, 0x80000000\n"
"ds_write_b64 v40, v[42:43] offset:4640\n"
"s_mov_b64 exec, -1\n"
"s_waitcnt vmcnt(10)\n"
"v_smfmac_f32_16x16x64_bf16 v[52:55], v[20:23], a[208:215], v33\n"
"ds_write_b128 v46, a[208:211] offset:10240\n"
"ds_write_b128 v46, a[212:215] offset:11264\n"
"s_waitcnt lgkmcnt(0)\n"
"s_memrealtime s[92:93]\n"
"s_waitcnt lgkmcnt(0)\n"
"s_sub_u32 s93, s92, s20\n"
"s_cmp_ge_u32 s93, 3479\n"
"s_cbranch_scc1 .Lk_tok_116\n"
".Lk_tlp_117:\n"
"s_memrealtime s[92:93]\n"
"s_waitcnt lgkmcnt(0)\n"
"s_sub_u32 s93, s92, s20\n"
"s_cmp_ge_u32 s93, 3479\n"
"s_cbranch_scc0 .Lk_tlp_117\n"
".Lk_tok_116:\n"
"buffer_load_dwordx4 v[56:59], v39, s[48:51], s61 offen offset:2048 nt\n"
"buffer_load_dwordx4 v[60:63], v39, s[48:51], s61 offen offset:3072 nt\n"
"v_mov_b32_dpp v20, v47 row_newbcast:14 row_mask:0xf bank_mask:0x1\n"
"v_mov_b32_dpp v21, v47 row_newbcast:14 row_mask:0xf bank_mask:0x2\n"
"v_mov_b32_dpp v22, v47 row_newbcast:15 row_mask:0xf bank_mask:0x1\n"
"v_mov_b32_dpp v23, v47 row_newbcast:15 row_mask:0xf bank_mask:0x2\n"
"s_waitcnt vmcnt(10)\n"
"v_smfmac_f32_16x16x64_bf16 v[52:55], v[16:19], a[216:223], v33\n"
"ds_write_b128 v46, a[216:219] offset:12288\n"
"ds_write_b128 v46, a[220:223] offset:13312\n"
"buffer_load_dwordx4 v[64:67], v39, s[48:51], s62 offen offset:0 nt\n"
"buffer_load_dwordx4 v[68:71], v39, s[48:51], s62 offen offset:1024 nt\n"
"v_mov_b32_dpp v16, v48 row_newbcast:0 row_mask:0xf bank_mask:0x1\n"
"v_mov_b32_dpp v17, v48 row_newbcast:0 row_mask:0xf bank_mask:0x2\n"
"v_mov_b32_dpp v18, v48 row_newbcast:1 row_mask:0xf bank_mask:0x1\n"
"v_mov_b32_dpp v19, v48 row_newbcast:1 row_mask:0xf bank_mask:0x2\n"
"s_waitcnt vmcnt(10)\n"
"v_smfmac_f32_16x16x64_bf16 v[52:55], v[20:23], a[224:231], v33\n"
"ds_write_b128 v46, a[224:227] offset:14336\n"
"ds_write_b128 v46, a[228:231] offset:15360\n"
"s_waitcnt lgkmcnt(0)\n"
"s_memrealtime s[92:93]\n"
"s_waitcnt lgkmcnt(0)\n"
"s_sub_u32 s93, s92, s20\n"
"s_cmp_ge_u32 s93, 3529\n"
"s_cbranch_scc1 .Lk_tok_118\n"
".Lk_tlp_119:\n"
"s_memrealtime s[92:93]\n"
"s_waitcnt lgkmcnt(0)\n"
"s_sub_u32 s93, s92, s20\n"
"s_cmp_ge_u32 s93, 3529\n"
"s_cbranch_scc0 .Lk_tlp_119\n"
".Lk_tok_118:\n"
"buffer_load_dwordx4 v[72:75], v39, s[48:51], s62 offen offset:2048 nt\n"
"buffer_load_dwordx4 v[76:79], v39, s[48:51], s62 offen offset:3072 nt\n"
"v_mov_b32_dpp v20, v48 row_newbcast:2 row_mask:0xf bank_mask:0x1\n"
"v_mov_b32_dpp v21, v48 row_newbcast:2 row_mask:0xf bank_mask:0x2\n"
"v_mov_b32_dpp v22, v48 row_newbcast:3 row_mask:0xf bank_mask:0x1\n"
"v_mov_b32_dpp v23, v48 row_newbcast:3 row_mask:0xf bank_mask:0x2\n"
"s_waitcnt vmcnt(10)\n"
"v_smfmac_f32_16x16x64_bf16 v[52:55], v[16:19], a[232:239], v33\n"
"ds_write_b128 v46, a[232:235] offset:16384\n"
"ds_write_b128 v46, a[236:239] offset:17408\n"
"buffer_load_dwordx4 v[80:83], v39, s[48:51], s63 offen offset:0 nt\n"
"buffer_load_dwordx4 v[84:87], v39, s[48:51], s63 offen offset:1024 nt\n"
"v_mov_b32_dpp v16, v48 row_newbcast:4 row_mask:0xf bank_mask:0x1\n"
"v_mov_b32_dpp v17, v48 row_newbcast:4 row_mask:0xf bank_mask:0x2\n"
"v_mov_b32_dpp v18, v48 row_newbcast:5 row_mask:0xf bank_mask:0x1\n"
"v_mov_b32_dpp v19, v48 row_newbcast:5 row_mask:0xf bank_mask:0x2\n"
"s_waitcnt vmcnt(10)\n"
"v_smfmac_f32_16x16x64_bf16 v[52:55], v[20:23], a[240:247], v33\n"
"ds_write_b128 v46, a[240:243] offset:18432\n"
"ds_write_b128 v46, a[244:247] offset:19456\n"
"s_waitcnt lgkmcnt(0)\n"
"s_memrealtime s[92:93]\n"
"s_waitcnt lgkmcnt(0)\n"
"s_sub_u32 s93, s92, s20\n"
"s_cmp_ge_u32 s93, 3578\n"
"s_cbranch_scc1 .Lk_tok_120\n"
".Lk_tlp_121:\n"
"s_memrealtime s[92:93]\n"
"s_waitcnt lgkmcnt(0)\n"
"s_sub_u32 s93, s92, s20\n"
"s_cmp_ge_u32 s93, 3578\n"
"s_cbranch_scc0 .Lk_tlp_121\n"
".Lk_tok_120:\n"
"buffer_load_dwordx4 v[88:91], v39, s[48:51], s63 offen offset:2048 nt\n"
"buffer_load_dwordx4 v[92:95], v39, s[48:51], s63 offen offset:3072 nt\n"
"v_mov_b32_dpp v20, v48 row_newbcast:6 row_mask:0xf bank_mask:0x1\n"
"v_mov_b32_dpp v21, v48 row_newbcast:6 row_mask:0xf bank_mask:0x2\n"
"v_mov_b32_dpp v22, v48 row_newbcast:7 row_mask:0xf bank_mask:0x1\n"
"v_mov_b32_dpp v23, v48 row_newbcast:7 row_mask:0xf bank_mask:0x2\n"
"s_waitcnt vmcnt(10)\n"
"v_smfmac_f32_16x16x64_bf16 v[52:55], v[16:19], a[248:255], v33\n"
"ds_write_b128 v46, a[248:251] offset:20480\n"
"ds_write_b128 v46, a[252:255] offset:21504\n"
"buffer_load_dwordx4 v[96:99], v39, s[48:51], s64 offen offset:0 nt\n"
"buffer_load_dwordx4 v[100:103], v39, s[48:51], s64 offen offset:1024 nt\n"
"v_mov_b32_dpp v16, v48 row_newbcast:8 row_mask:0xf bank_mask:0x1\n"
"v_mov_b32_dpp v17, v48 row_newbcast:8 row_mask:0xf bank_mask:0x2\n"
"v_mov_b32_dpp v18, v48 row_newbcast:9 row_mask:0xf bank_mask:0x1\n"
"v_mov_b32_dpp v19, v48 row_newbcast:9 row_mask:0xf bank_mask:0x2\n"
"s_waitcnt vmcnt(10)\n"
"v_smfmac_f32_16x16x64_bf16 v[52:55], v[20:23], v[56:63], v33\n"
"ds_write_b128 v46, v[56:59] offset:22528\n"
"ds_write_b128 v46, v[60:63] offset:23552\n"
"s_waitcnt lgkmcnt(0)\n"
"s_memrealtime s[92:93]\n"
"s_waitcnt lgkmcnt(0)\n"
"s_sub_u32 s93, s92, s20\n"
"s_cmp_ge_u32 s93, 3628\n"
"s_cbranch_scc1 .Lk_tok_122\n"
".Lk_tlp_123:\n"
"s_memrealtime s[92:93]\n"
"s_waitcnt lgkmcnt(0)\n"
"s_sub_u32 s93, s92, s20\n"
"s_cmp_ge_u32 s93, 3628\n"
"s_cbranch_scc0 .Lk_tlp_123\n"
".Lk_tok_122:\n"
"buffer_load_dwordx4 v[104:107], v39, s[48:51], s64 offen offset:2048 nt\n"
"buffer_load_dwordx4 v[108:111], v39, s[48:51], s64 offen offset:3072 nt\n"
"v_mov_b32_dpp v20, v48 row_newbcast:10 row_mask:0xf bank_mask:0x1\n"
"v_mov_b32_dpp v21, v48 row_newbcast:10 row_mask:0xf bank_mask:0x2\n"
"v_mov_b32_dpp v22, v48 row_newbcast:11 row_mask:0xf bank_mask:0x1\n"
"v_mov_b32_dpp v23, v48 row_newbcast:11 row_mask:0xf bank_mask:0x2\n"
"s_waitcnt vmcnt(10)\n"
"v_smfmac_f32_16x16x64_bf16 v[52:55], v[16:19], v[64:71], v33\n"
"ds_write_b128 v46, v[64:67] offset:24576\n"
"ds_write_b128 v46, v[68:71] offset:25600\n"
"buffer_load_dwordx4 v[112:115], v39, s[48:51], s65 offen offset:0 nt\n"
"buffer_load_dwordx4 v[116:119], v39, s[48:51], s65 offen offset:1024 nt\n"
"v_mov_b32_dpp v16, v48 row_newbcast:12 row_mask:0xf bank_mask:0x1\n"
"v_mov_b32_dpp v17, v48 row_newbcast:12 row_mask:0xf bank_mask:0x2\n"
"v_mov_b32_dpp v18, v48 row_newbcast:13 row_mask:0xf bank_mask:0x1\n"
"v_mov_b32_dpp v19, v48 row_newbcast:13 row_mask:0xf bank_mask:0x2\n"
"s_waitcnt vmcnt(10)\n"
"v_smfmac_f32_16x16x64_bf16 v[52:55], v[20:23], v[72:79], v33\n"
"ds_write_b128 v46, v[72:75] offset:26624\n"
"ds_write_b128 v46, v[76:79] offset:27648\n"
"s_waitcnt lgkmcnt(0)\n"
"s_memrealtime s[92:93]\n"
"s_waitcnt lgkmcnt(0)\n"
"s_sub_u32 s93, s92, s20\n"
"s_cmp_ge_u32 s93, 3677\n"
"s_cbranch_scc1 .Lk_tok_124\n"
".Lk_tlp_125:\n"
"s_memrealtime s[92:93]\n"
"s_waitcnt lgkmcnt(0)\n"
"s_sub_u32 s93, s92, s20\n"
"s_cmp_ge_u32 s93, 3677\n"
"s_cbranch_scc0 .Lk_tlp_125\n"
".Lk_tok_124:\n"
"buffer_load_dwordx4 v[120:123], v39, s[48:51], s65 offen offset:2048 nt\n"
"buffer_load_dwordx4 v[124:127], v39, s[48:51], s65 offen offset:3072 nt\n"
"v_mov_b32_dpp v20, v48 row_newbcast:14 row_mask:0xf bank_mask:0x1\n"
"v_mov_b32_dpp v21, v48 row_newbcast:14 row_mask:0xf bank_mask:0x2\n"
"v_mov_b32_dpp v22, v48 row_newbcast:15 row_mask:0xf bank_mask:0x1\n"
"v_mov_b32_dpp v23, v48 row_newbcast:15 row_mask:0xf bank_mask:0x2\n"
"s_waitcnt vmcnt(10)\n"
"v_smfmac_f32_16x16x64_bf16 v[52:55], v[16:19], v[80:87], v33\n"
"ds_write_b128 v46, v[80:83] offset:28672\n"
"ds_write_b128 v46, v[84:87] offset:29696\n"
"buffer_load_dwordx4 v[168:171], v39, s[48:51], s66 offen offset:0 nt\n"
"buffer_load_dwordx4 v[172:175], v39, s[48:51], s66 offen offset:1024 nt\n"
"v_mov_b32_dpp v16, v49 row_newbcast:0 row_mask:0xf bank_mask:0x1\n"
"v_mov_b32_dpp v17, v49 row_newbcast:0 row_mask:0xf bank_mask:0x2\n"
"v_mov_b32_dpp v18, v49 row_newbcast:1 row_mask:0xf bank_mask:0x1\n"
"v_mov_b32_dpp v19, v49 row_newbcast:1 row_mask:0xf bank_mask:0x2\n"
"s_waitcnt vmcnt(10)\n"
"v_smfmac_f32_16x16x64_bf16 v[52:55], v[20:23], v[88:95], v33\n"
"ds_write_b128 v46, v[88:91] offset:30720\n"
"ds_write_b128 v46, v[92:95] offset:31744\n"
"s_waitcnt lgkmcnt(0)\n"
"s_memrealtime s[92:93]\n"
"s_waitcnt lgkmcnt(0)\n"
"s_sub_u32 s93, s92, s20\n"
"s_cmp_ge_u32 s93, 3726\n"
"s_cbranch_scc1 .Lk_tok_126\n"
".Lk_tlp_127:\n"
"s_memrealtime s[92:93]\n"
"s_waitcnt lgkmcnt(0)\n"
"s_sub_u32 s93, s92, s20\n"
"s_cmp_ge_u32 s93, 3726\n"
"s_cbranch_scc0 .Lk_tlp_127\n"
".Lk_tok_126:\n"
"buffer_load_dwordx4 a[176:179], v39, s[48:51], s66 offen offset:2048 nt\n"
"buffer_load_dwordx4 a[180:183], v39, s[48:51], s66 offen offset:3072 nt\n"
"v_mov_b32_dpp v20, v49 row_newbcast:2 row_mask:0xf bank_mask:0x1\n"
"v_mov_b32_dpp v21, v49 row_newbcast:2 row_mask:0xf bank_mask:0x2\n"
"v_mov_b32_dpp v22, v49 row_newbcast:3 row_mask:0xf bank_mask:0x1\n"
"v_mov_b32_dpp v23, v49 row_newbcast:3 row_mask:0xf bank_mask:0x2\n"
"s_waitcnt vmcnt(10)\n"
"v_smfmac_f32_16x16x64_bf16 v[52:55], v[16:19], v[96:103], v33\n"
"ds_write_b128 v46, v[96:99] offset:32768\n"
"ds_write_b128 v46, v[100:103] offset:33792\n"
"buffer_load_dwordx4 a[184:187], v39, s[48:51], s67 offen offset:0 nt\n"
"buffer_load_dwordx4 a[188:191], v39, s[48:51], s67 offen offset:1024 nt\n"
"v_mov_b32_dpp v16, v49 row_newbcast:4 row_mask:0xf bank_mask:0x1\n"
"v_mov_b32_dpp v17, v49 row_newbcast:4 row_mask:0xf bank_mask:0x2\n"
"v_mov_b32_dpp v18, v49 row_newbcast:5 row_mask:0xf bank_mask:0x1\n"
"v_mov_b32_dpp v19, v49 row_newbcast:5 row_mask:0xf bank_mask:0x2\n"
"s_waitcnt vmcnt(10)\n"
"v_smfmac_f32_16x16x64_bf16 v[52:55], v[20:23], v[104:111], v33\n"
"ds_write_b128 v46, v[104:107] offset:34816\n"
"ds_write_b128 v46, v[108:111] offset:35840\n"
"s_waitcnt lgkmcnt(0)\n"
"s_memrealtime s[92:93]\n"
"s_waitcnt lgkmcnt(0)\n"
"s_sub_u32 s93, s92, s20\n"
"s_cmp_ge_u32 s93, 3776\n"
"s_cbranch_scc1 .Lk_tok_128\n"
".Lk_tlp_129:\n"
"s_memrealtime s[92:93]\n"
"s_waitcnt lgkmcnt(0)\n"
"s_sub_u32 s93, s92, s20\n"
"s_cmp_ge_u32 s93, 3776\n"
"s_cbranch_scc0 .Lk_tlp_129\n"
".Lk_tok_128:\n"
"buffer_load_dwordx4 a[192:195], v39, s[48:51], s67 offen offset:2048 nt\n"
"buffer_load_dwordx4 a[196:199], v39, s[48:51], s67 offen offset:3072 nt\n"
"v_mov_b32_dpp v20, v49 row_newbcast:6 row_mask:0xf bank_mask:0x1\n"
"v_mov_b32_dpp v21, v49 row_newbcast:6 row_mask:0xf bank_mask:0x2\n"
"v_mov_b32_dpp v22, v49 row_newbcast:7 row_mask:0xf bank_mask:0x1\n"
"v_mov_b32_dpp v23, v49 row_newbcast:7 row_mask:0xf bank_mask:0x2\n"
"s_waitcnt vmcnt(10)\n"
"v_smfmac_f32_16x16x64_bf16 v[52:55], v[16:19], v[112:119], v33\n"
"ds_write_b128 v46, v[112:115] offset:36864\n"
"ds_write_b128 v46, v[116:119] offset:37888\n"
"buffer_load_dwordx4 a[200:203], v39, s[48:51], s68 offen offset:0 nt\n"
"buffer_load_dwordx4 a[204:207], v39, s[48:51], s68 offen offset:1024 nt\n"
"v_mov_b32_dpp v16, v49 row_newbcast:8 row_mask:0xf bank_mask:0x1\n"
"v_mov_b32_dpp v17, v49 row_newbcast:8 row_mask:0xf bank_mask:0x2\n"
"v_mov_b32_dpp v18, v49 row_newbcast:9 row_mask:0xf bank_mask:0x1\n"
"v_mov_b32_dpp v19, v49 row_newbcast:9 row_mask:0xf bank_mask:0x2\n"
"s_waitcnt vmcnt(10)\n"
"v_smfmac_f32_16x16x64_bf16 v[52:55], v[20:23], v[120:127], v33\n"
"s_waitcnt lgkmcnt(0)\n"
"s_memrealtime s[92:93]\n"
"s_waitcnt lgkmcnt(0)\n"
"s_sub_u32 s93, s92, s20\n"
"s_cmp_ge_u32 s93, 3825\n"
"s_cbranch_scc1 .Lk_tok_130\n"
".Lk_tlp_131:\n"
"s_memrealtime s[92:93]\n"
"s_waitcnt lgkmcnt(0)\n"
"s_sub_u32 s93, s92, s20\n"
"s_cmp_ge_u32 s93, 3825\n"
"s_cbranch_scc0 .Lk_tlp_131\n"
".Lk_tok_130:\n"
"buffer_load_dwordx4 a[208:211], v39, s[48:51], s68 offen offset:2048 nt\n"
"buffer_load_dwordx4 a[212:215], v39, s[48:51], s68 offen offset:3072 nt\n"
"v_mov_b32_dpp v20, v49 row_newbcast:10 row_mask:0xf bank_mask:0x1\n"
"v_mov_b32_dpp v21, v49 row_newbcast:10 row_mask:0xf bank_mask:0x2\n"
"v_mov_b32_dpp v22, v49 row_newbcast:11 row_mask:0xf bank_mask:0x1\n"
"v_mov_b32_dpp v23, v49 row_newbcast:11 row_mask:0xf bank_mask:0x2\n"
"s_waitcnt vmcnt(10)\n"
"v_smfmac_f32_16x16x64_bf16 v[52:55], v[16:19], v[168:175], v33\n"
"buffer_load_dwordx4 a[216:219], v39, s[48:51], s69 offen offset:0 nt\n"
"buffer_load_dwordx4 a[220:223], v39, s[48:51], s69 offen offset:1024 nt\n"
"v_mov_b32_dpp v16, v49 row_newbcast:12 row_mask:0xf bank_mask:0x1\n"
"v_mov_b32_dpp v17, v49 row_newbcast:12 row_mask:0xf bank_mask:0x2\n"
"v_mov_b32_dpp v18, v49 row_newbcast:13 row_mask:0xf bank_mask:0x1\n"
"v_mov_b32_dpp v19, v49 row_newbcast:13 row_mask:0xf bank_mask:0x2\n"
"s_waitcnt vmcnt(10)\n"
"v_smfmac_f32_16x16x64_bf16 v[52:55], v[20:23], a[176:183], v33\n"
"s_memrealtime s[92:93]\n"
"s_waitcnt lgkmcnt(0)\n"
"s_sub_u32 s93, s92, s20\n"
"s_cmp_ge_u32 s93, 3875\n"
"s_cbranch_scc1 .Lk_tok_132\n"
".Lk_tlp_133:\n"
"s_memrealtime s[92:93]\n"
"s_waitcnt lgkmcnt(0)\n"
"s_sub_u32 s93, s92, s20\n"
"s_cmp_ge_u32 s93, 3875\n"
"s_cbranch_scc0 .Lk_tlp_133\n"
".Lk_tok_132:\n"
"buffer_load_dwordx4 a[224:227], v39, s[48:51], s69 offen offset:2048 nt\n"
"buffer_load_dwordx4 a[228:231], v39, s[48:51], s69 offen offset:3072 nt\n"
"v_mov_b32_dpp v20, v49 row_newbcast:14 row_mask:0xf bank_mask:0x1\n"
"v_mov_b32_dpp v21, v49 row_newbcast:14 row_mask:0xf bank_mask:0x2\n"
"v_mov_b32_dpp v22, v49 row_newbcast:15 row_mask:0xf bank_mask:0x1\n"
"v_mov_b32_dpp v23, v49 row_newbcast:15 row_mask:0xf bank_mask:0x2\n"
"s_waitcnt vmcnt(10)\n"
"v_smfmac_f32_16x16x64_bf16 v[52:55], v[16:19], a[184:191], v33\n"
"buffer_load_dwordx4 a[232:235], v39, s[48:51], s70 offen offset:0 nt\n"
"buffer_load_dwordx4 a[236:239], v39, s[48:51], s70 offen offset:1024 nt\n"
"v_mov_b32_dpp v16, v50 row_newbcast:0 row_mask:0xf bank_mask:0x1\n"
"v_mov_b32_dpp v17, v50 row_newbcast:0 row_mask:0xf bank_mask:0x2\n"
"v_mov_b32_dpp v18, v50 row_newbcast:1 row_mask:0xf bank_mask:0x1\n"
"v_mov_b32_dpp v19, v50 row_newbcast:1 row_mask:0xf bank_mask:0x2\n"
"s_waitcnt vmcnt(10)\n"
"v_smfmac_f32_16x16x64_bf16 v[52:55], v[20:23], a[192:199], v33\n"
"s_memrealtime s[92:93]\n"
"s_waitcnt lgkmcnt(0)\n"
"s_sub_u32 s93, s92, s20\n"
"s_cmp_ge_u32 s93, 3924\n"
"s_cbranch_scc1 .Lk_tok_134\n"
".Lk_tlp_135:\n"
"s_memrealtime s[92:93]\n"
"s_waitcnt lgkmcnt(0)\n"
"s_sub_u32 s93, s92, s20\n"
"s_cmp_ge_u32 s93, 3924\n"
"s_cbranch_scc0 .Lk_tlp_135\n"
".Lk_tok_134:\n"
"buffer_load_dwordx4 a[240:243], v39, s[48:51], s70 offen offset:2048 nt\n"
"buffer_load_dwordx4 a[244:247], v39, s[48:51], s70 offen offset:3072 nt\n"
"v_mov_b32_dpp v20, v50 row_newbcast:2 row_mask:0xf bank_mask:0x1\n"
"v_mov_b32_dpp v21, v50 row_newbcast:2 row_mask:0xf bank_mask:0x2\n"
"v_mov_b32_dpp v22, v50 row_newbcast:3 row_mask:0xf bank_mask:0x1\n"
"v_mov_b32_dpp v23, v50 row_newbcast:3 row_mask:0xf bank_mask:0x2\n"
"s_waitcnt vmcnt(10)\n"
"v_smfmac_f32_16x16x64_bf16 v[52:55], v[16:19], a[200:207], v33\n"
"buffer_load_dwordx4 a[248:251], v39, s[48:51], s71 offen offset:0 nt\n"
"buffer_load_dwordx4 a[252:255], v39, s[48:51], s71 offen offset:1024 nt\n"
"v_mov_b32_dpp v16, v50 row_newbcast:4 row_mask:0xf bank_mask:0x1\n"
"v_mov_b32_dpp v17, v50 row_newbcast:4 row_mask:0xf bank_mask:0x2\n"
"v_mov_b32_dpp v18, v50 row_newbcast:5 row_mask:0xf bank_mask:0x1\n"
"v_mov_b32_dpp v19, v50 row_newbcast:5 row_mask:0xf bank_mask:0x2\n"
"s_waitcnt vmcnt(10)\n"
"v_smfmac_f32_16x16x64_bf16 v[52:55], v[20:23], a[208:215], v33\n"
"s_memrealtime s[92:93]\n"
"s_waitcnt lgkmcnt(0)\n"
"s_sub_u32 s93, s92, s20\n"
"s_cmp_ge_u32 s93, 3973\n"
"s_cbranch_scc1 .Lk_tok_136\n"
".Lk_tlp_137:\n"
"s_memrealtime s[92:93]\n"
"s_waitcnt lgkmcnt(0)\n"
"s_sub_u32 s93, s92, s20\n"
"s_cmp_ge_u32 s93, 3973\n"
"s_cbranch_scc0 .Lk_tlp_137\n"
".Lk_tok_136:\n"
"buffer_load_dwordx4 v[56:59], v39, s[48:51], s71 offen offset:2048 nt\n"
"buffer_load_dwordx4 v[60:63], v39, s[48:51], s71 offen offset:3072 nt\n"
"v_mov_b32_dpp v20, v50 row_newbcast:6 row_mask:0xf bank_mask:0x1\n"
"v_mov_b32_dpp v21, v50 row_newbcast:6 row_mask:0xf bank_mask:0x2\n"
"v_mov_b32_dpp v22, v50 row_newbcast:7 row_mask:0xf bank_mask:0x1\n"
"v_mov_b32_dpp v23, v50 row_newbcast:7 row_mask:0xf bank_mask:0x2\n"
"s_waitcnt vmcnt(10)\n"
"v_smfmac_f32_16x16x64_bf16 v[52:55], v[16:19], a[216:223], v33\n"
"buffer_load_dwordx4 v[64:67], v39, s[48:51], s72 offen offset:0 nt\n"
"buffer_load_dwordx4 v[68:71], v39, s[48:51], s72 offen offset:1024 nt\n"
"v_mov_b32_dpp v16, v50 row_newbcast:8 row_mask:0xf bank_mask:0x1\n"
"v_mov_b32_dpp v17, v50 row_newbcast:8 row_mask:0xf bank_mask:0x2\n"
"v_mov_b32_dpp v18, v50 row_newbcast:9 row_mask:0xf bank_mask:0x1\n"
"v_mov_b32_dpp v19, v50 row_newbcast:9 row_mask:0xf bank_mask:0x2\n"
"s_waitcnt vmcnt(10)\n"
"v_smfmac_f32_16x16x64_bf16 v[52:55], v[20:23], a[224:231], v33\n"
"s_memrealtime s[92:93]\n"
"s_waitcnt lgkmcnt(0)\n"
"s_sub_u32 s93, s92, s20\n"
"s_cmp_ge_u32 s93, 4023\n"
"s_cbranch_scc1 .Lk_tok_138\n"
".Lk_tlp_139:\n"
"s_memrealtime s[92:93]\n"
"s_waitcnt lgkmcnt(0)\n"
"s_sub_u32 s93, s92, s20\n"
"s_cmp_ge_u32 s93, 4023\n"
"s_cbranch_scc0 .Lk_tlp_139\n"
".Lk_tok_138:\n"
"buffer_load_dwordx4 v[72:75], v39, s[48:51], s72 offen offset:2048 nt\n"
"buffer_load_dwordx4 v[76:79], v39, s[48:51], s72 offen offset:3072 nt\n"
"v_mov_b32_dpp v20, v50 row_newbcast:10 row_mask:0xf bank_mask:0x1\n"
"v_mov_b32_dpp v21, v50 row_newbcast:10 row_mask:0xf bank_mask:0x2\n"
"v_mov_b32_dpp v22, v50 row_newbcast:11 row_mask:0xf bank_mask:0x1\n"
"v_mov_b32_dpp v23, v50 row_newbcast:11 row_mask:0xf bank_mask:0x2\n"
"s_waitcnt vmcnt(10)\n"
"v_smfmac_f32_16x16x64_bf16 v[52:55], v[16:19], a[232:239], v33\n"
"buffer_load_dwordx4 v[80:83], v39, s[48:51], s73 offen offset:0 nt\n"
"buffer_load_dwordx4 v[84:87], v39, s[48:51], s73 offen offset:1024 nt\n"
"v_mov_b32_dpp v16, v50 row_newbcast:12 row_mask:0xf bank_mask:0x1\n"
"v_mov_b32_dpp v17, v50 row_newbcast:12 row_mask:0xf bank_mask:0x2\n"
"v_mov_b32_dpp v18, v50 row_newbcast:13 row_mask:0xf bank_mask:0x1\n"
"v_mov_b32_dpp v19, v50 row_newbcast:13 row_mask:0xf bank_mask:0x2\n"
"s_waitcnt vmcnt(10)\n"
"v_smfmac_f32_16x16x64_bf16 v[52:55], v[20:23], a[240:247], v33\n"
"s_memrealtime s[92:93]\n"
"s_waitcnt lgkmcnt(0)\n"
"s_sub_u32 s93, s92, s20\n"
"s_cmp_ge_u32 s93, 4072\n"
"s_cbranch_scc1 .Lk_tok_140\n"
".Lk_tlp_141:\n"
"s_memrealtime s[92:93]\n"
"s_waitcnt lgkmcnt(0)\n"
"s_sub_u32 s93, s92, s20\n"
"s_cmp_ge_u32 s93, 4072\n"
"s_cbranch_scc0 .Lk_tlp_141\n"
".Lk_tok_140:\n"
"buffer_load_dwordx4 v[88:91], v39, s[48:51], s73 offen offset:2048 nt\n"
"buffer_load_dwordx4 v[92:95], v39, s[48:51], s73 offen offset:3072 nt\n"
"v_mov_b32_dpp v20, v50 row_newbcast:14 row_mask:0xf bank_mask:0x1\n"
"v_mov_b32_dpp v21, v50 row_newbcast:14 row_mask:0xf bank_mask:0x2\n"
"v_mov_b32_dpp v22, v50 row_newbcast:15 row_mask:0xf bank_mask:0x1\n"
"v_mov_b32_dpp v23, v50 row_newbcast:15 row_mask:0xf bank_mask:0x2\n"
"s_waitcnt vmcnt(10)\n"
"v_smfmac_f32_16x16x64_bf16 v[52:55], v[16:19], a[248:255], v33\n"
"buffer_load_dwordx4 v[96:99], v39, s[48:51], s74 offen offset:0 nt\n"
"buffer_load_dwordx4 v[100:103], v39, s[48:51], s74 offen offset:1024 nt\n"
"v_mov_b32_dpp v16, v51 row_newbcast:0 row_mask:0xf bank_mask:0x1\n"
"v_mov_b32_dpp v17, v51 row_newbcast:0 row_mask:0xf bank_mask:0x2\n"
"v_mov_b32_dpp v18, v51 row_newbcast:1 row_mask:0xf bank_mask:0x1\n"
"v_mov_b32_dpp v19, v51 row_newbcast:1 row_mask:0xf bank_mask:0x2\n"
"s_waitcnt vmcnt(10)\n"
"v_smfmac_f32_16x16x64_bf16 v[52:55], v[20:23], v[56:63], v33\n"
"v_mov_b32_dpp v20, v51 row_newbcast:2 row_mask:0xf bank_mask:0x1\n"
"v_mov_b32_dpp v21, v51 row_newbcast:2 row_mask:0xf bank_mask:0x2\n"
"v_mov_b32_dpp v22, v51 row_newbcast:3 row_mask:0xf bank_mask:0x1\n"
"v_mov_b32_dpp v23, v51 row_newbcast:3 row_mask:0xf bank_mask:0x2\n"
"s_waitcnt vmcnt(8)\n"
"v_smfmac_f32_16x16x64_bf16 v[52:55], v[16:19], v[64:71], v33\n"
"v_mov_b32_dpp v16, v51 row_newbcast:4 row_mask:0xf bank_mask:0x1\n"
"v_mov_b32_dpp v17, v51 row_newbcast:4 row_mask:0xf bank_mask:0x2\n"
"v_mov_b32_dpp v18, v51 row_newbcast:5 row_mask:0xf bank_mask:0x1\n"
"v_mov_b32_dpp v19, v51 row_newbcast:5 row_mask:0xf bank_mask:0x2\n"
"s_waitcnt vmcnt(6)\n"
"v_smfmac_f32_16x16x64_bf16 v[52:55], v[20:23], v[72:79], v33\n"
"buffer_load_dwordx4 v[104:107], v39, s[40:43], s61 offen offset:2048 nt\n"
"buffer_load_dwordx4 v[108:111], v39, s[40:43], s61 offen offset:3072 nt\n"
"v_mov_b32_dpp v20, v51 row_newbcast:6 row_mask:0xf bank_mask:0x1\n"
"v_mov_b32_dpp v21, v51 row_newbcast:6 row_mask:0xf bank_mask:0x2\n"
"v_mov_b32_dpp v22, v51 row_newbcast:7 row_mask:0xf bank_mask:0x1\n"
"v_mov_b32_dpp v23, v51 row_newbcast:7 row_mask:0xf bank_mask:0x2\n"
"s_waitcnt vmcnt(6)\n"
"v_smfmac_f32_16x16x64_bf16 v[52:55], v[16:19], v[80:87], v33\n"
"buffer_load_dwordx4 v[112:115], v39, s[40:43], s56 offen offset:0 nt\n"
"buffer_load_dwordx4 v[116:119], v39, s[40:43], s56 offen offset:1024 nt\n"
"v_mov_b32_dpp v16, v51 row_newbcast:8 row_mask:0xf bank_mask:0x1\n"
"v_mov_b32_dpp v17, v51 row_newbcast:8 row_mask:0xf bank_mask:0x2\n"
"v_mov_b32_dpp v18, v51 row_newbcast:9 row_mask:0xf bank_mask:0x1\n"
"v_mov_b32_dpp v19, v51 row_newbcast:9 row_mask:0xf bank_mask:0x2\n"
"s_waitcnt vmcnt(6)\n"
"v_smfmac_f32_16x16x64_bf16 v[52:55], v[20:23], v[88:95], v33\n"
"buffer_load_dwordx4 v[0:3], v39, s[40:43], s62 offen offset:0 nt\n"
"buffer_load_dwordx4 v[4:7], v39, s[40:43], s62 offen offset:1024 nt\n"
"s_waitcnt vmcnt(6)\n"
"v_smfmac_f32_16x16x64_bf16 v[52:55], v[16:19], v[96:103], v33\n"
"buffer_load_dwordx4 v[8:11], v39, s[40:43], s56 offen offset:2048 nt\n"
"buffer_load_dwordx4 v[12:15], v39, s[40:43], s56 offen offset:3072 nt\n"
"s_mul_i32 s88, s15, 576\n"
"s_lshl_b32 s89, s15, 7\n"
"v_and_b32 v40, 0xf0, v39\n"
"v_lshrrev_b32 v41, 4, v39\n"
"v_lshlrev_b32 v40, 1, v40\n"
"v_and_b32 v44, 16, v41\n"
"v_add3_u32 v40, v40, v44, s88\n"
"v_and_b32 v44, 3, v41\n"
"v_lshrrev_b32 v45, 2, v41\n"
"v_lshl_add_u32 v45, v45, 3, s89\n"
"v_mul_u32_u24 v41, 576, v44\n"
"v_lshlrev_b32 v44, 3, v44\n"
"v_add_u32 v41, v41, v45\n"
"s_mov_b64 vcc, 2\n"
"v_mov_b32 v42, 512\n"
"v_cndmask_b32 v45, v45, v42, vcc\n"
"s_add_u32 s90, s95, 143360\n"
"s_mov_b32 exec_hi, 0\n"
"ds_write_b128 v40, v[52:55] offset:2304\n"
"s_mov_b64 exec, -1\n"
"s_waitcnt lgkmcnt(0)\n"
"s_barrier\n"
"ds_read_b64 v[16:17], v44 offset:4640\n"
"ds_read_b64 v[18:19], v41 offset:2304\n"
"s_waitcnt lgkmcnt(1)\n"
"v_max_f32_dpp v20, v16, v16 quad_perm:[1,0,3,2] row_mask:0xf bank_mask:0xf\n"
"s_nop 1\n"
"v_max_f32_dpp v20, v20, v20 quad_perm:[2,3,0,1] row_mask:0xf bank_mask:0xf\n"
"v_sub_f32 v16, v16, v20\n"
"v_exp_f32 v16, v16\n"
"s_waitcnt lgkmcnt(0)\n"
"v_mul_f32 v18, v16, v18\n"
"v_mul_f32 v19, v16, v19\n"
"v_mul_f32 v21, v16, v17\n"
"v_add_f32_dpp v18, v18, v18 quad_perm:[1,0,3,2] row_mask:0xf bank_mask:0xf\n"
"v_add_f32_dpp v19, v19, v19 quad_perm:[1,0,3,2] row_mask:0xf bank_mask:0xf\n"
"v_add_f32_dpp v21, v21, v21 quad_perm:[1,0,3,2] row_mask:0xf bank_mask:0xf\n"
"v_add_f32_dpp v18, v18, v18 quad_perm:[2,3,0,1] row_mask:0xf bank_mask:0xf\n"
"v_add_f32_dpp v19, v19, v19 quad_perm:[2,3,0,1] row_mask:0xf bank_mask:0xf\n"
"v_add_f32_dpp v21, v21, v21 quad_perm:[2,3,0,1] row_mask:0xf bank_mask:0xf\n"
"v_cndmask_b32 v18, v18, v20, vcc\n"
"v_cndmask_b32 v19, v19, v21, vcc\n"
"v_min_u32 v18, -2, v18\n"
"v_min_u32 v19, -2, v19\n"
"s_mov_b64 exec, s[34:35]\n"
"buffer_store_dwordx2 v[18:19], v45, s[52:55], s90 offen sc1\n"
"s_mov_b64 exec, -1\n"
"v_mov_b32 v16, 0\n"
"v_mov_b32 v17, 0\n"
"v_mov_b32 v18, 0\n"
"v_mov_b32 v19, 0\n"
"v_mov_b32 v20, 0\n"
"v_mov_b32 v21, 0\n"
"v_mov_b32 v22, 0\n"
"v_mov_b32 v23, 0\n"
"v_mov_b32_dpp v20, v29 row_newbcast:6 row_mask:0xf bank_mask:0x1\n"
"v_mov_b32_dpp v21, v29 row_newbcast:6 row_mask:0xf bank_mask:0x2\n"
"v_mov_b32_dpp v22, v29 row_newbcast:7 row_mask:0xf bank_mask:0x1\n"
"v_mov_b32_dpp v23, v29 row_newbcast:7 row_mask:0xf bank_mask:0x2\n"
"v_mov_b32_dpp v16, v28 row_newbcast:0 row_mask:0xf bank_mask:0x1\n"
"v_mov_b32_dpp v17, v28 row_newbcast:0 row_mask:0xf bank_mask:0x2\n"
"v_mov_b32_dpp v18, v28 row_newbcast:1 row_mask:0xf bank_mask:0x1\n"
"v_mov_b32_dpp v19, v28 row_newbcast:1 row_mask:0xf bank_mask:0x2\n"
"s_waitcnt vmcnt(7)\n"
"v_smfmac_f32_16x16x64_bf16 v[34:37], v[20:23], v[104:111], v33\n"
"buffer_load_dwordx4 v[104:107], v39, s[40:43], s62 offen offset:2048 nt\n"
"buffer_load_dwordx4 v[108:111], v39, s[40:43], s62 offen offset:3072 nt\n"
"v_mov_b32_dpp v20, v29 row_newbcast:8 row_mask:0xf bank_mask:0x1\n"
"v_mov_b32_dpp v21, v29 row_newbcast:8 row_mask:0xf bank_mask:0x2\n"
"v_mov_b32_dpp v22, v29 row_newbcast:9 row_mask:0xf bank_mask:0x1\n"
"v_mov_b32_dpp v23, v29 row_newbcast:9 row_mask:0xf bank_mask:0x2\n"
"s_waitcnt vmcnt(7)\n"
"v_smfmac_f32_16x16x64_bf16 v[34:37], v[16:19], v[112:119], v33\n"
"buffer_load_dwordx4 v[112:115], v39, s[40:43], s57 offen offset:0 nt\n"
"buffer_load_dwordx4 v[116:119], v39, s[40:43], s57 offen offset:1024 nt\n"
"v_mov_b32_dpp v16, v28 row_newbcast:2 row_mask:0xf bank_mask:0x1\n"
"v_mov_b32_dpp v17, v28 row_newbcast:2 row_mask:0xf bank_mask:0x2\n"
"v_mov_b32_dpp v18, v28 row_newbcast:3 row_mask:0xf bank_mask:0x1\n"
"v_mov_b32_dpp v19, v28 row_newbcast:3 row_mask:0xf bank_mask:0x2\n"
"s_waitcnt vmcnt(7)\n"
"v_smfmac_f32_16x16x64_bf16 v[34:37], v[20:23], v[0:7], v33\n"
"buffer_load_dwordx4 v[0:3], v39, s[40:43], s63 offen offset:0 nt\n"
"buffer_load_dwordx4 v[4:7], v39, s[40:43], s63 offen offset:1024 nt\n"
"v_mov_b32_dpp v20, v29 row_newbcast:10 row_mask:0xf bank_mask:0x1\n"
"v_mov_b32_dpp v21, v29 row_newbcast:10 row_mask:0xf bank_mask:0x2\n"
"v_mov_b32_dpp v22, v29 row_newbcast:11 row_mask:0xf bank_mask:0x1\n"
"v_mov_b32_dpp v23, v29 row_newbcast:11 row_mask:0xf bank_mask:0x2\n"
"s_waitcnt vmcnt(7)\n"
"v_smfmac_f32_16x16x64_bf16 v[34:37], v[16:19], v[8:15], v33\n"
"buffer_load_dwordx4 v[8:11], v39, s[40:43], s63 offen offset:2048 nt\n"
"buffer_load_dwordx4 v[12:15], v39, s[40:43], s63 offen offset:3072 nt\n"
"v_mov_b32_dpp v16, v28 row_newbcast:4 row_mask:0xf bank_mask:0x1\n"
"v_mov_b32_dpp v17, v28 row_newbcast:4 row_mask:0xf bank_mask:0x2\n"
"v_mov_b32_dpp v18, v28 row_newbcast:5 row_mask:0xf bank_mask:0x1\n"
"v_mov_b32_dpp v19, v28 row_newbcast:5 row_mask:0xf bank_mask:0x2\n"
"s_waitcnt vmcnt(6)\n"
"v_smfmac_f32_16x16x64_bf16 v[34:37], v[20:23], v[104:111], v33\n"
"buffer_load_dwordx4 v[104:107], v39, s[40:43], s57 offen offset:2048 nt\n"
"buffer_load_dwordx4 v[108:111], v39, s[40:43], s57 offen offset:3072 nt\n"
"v_mov_b32_dpp v20, v29 row_newbcast:12 row_mask:0xf bank_mask:0x1\n"
"v_mov_b32_dpp v21, v29 row_newbcast:12 row_mask:0xf bank_mask:0x2\n"
"v_mov_b32_dpp v22, v29 row_newbcast:13 row_mask:0xf bank_mask:0x1\n"
"v_mov_b32_dpp v23, v29 row_newbcast:13 row_mask:0xf bank_mask:0x2\n"
"s_waitcnt vmcnt(6)\n"
"v_smfmac_f32_16x16x64_bf16 v[34:37], v[16:19], v[112:119], v33\n"
"buffer_load_dwordx4 v[112:115], v39, s[40:43], s64 offen offset:0 nt\n"
"buffer_load_dwordx4 v[116:119], v39, s[40:43], s64 offen offset:1024 nt\n"
"v_mov_b32_dpp v16, v29 row_newbcast:14 row_mask:0xf bank_mask:0x1\n"
"v_mov_b32_dpp v17, v29 row_newbcast:14 row_mask:0xf bank_mask:0x2\n"
"v_mov_b32_dpp v18, v29 row_newbcast:15 row_mask:0xf bank_mask:0x1\n"
"v_mov_b32_dpp v19, v29 row_newbcast:15 row_mask:0xf bank_mask:0x2\n"
"s_waitcnt vmcnt(6)\n"
"v_smfmac_f32_16x16x64_bf16 v[34:37], v[20:23], v[0:7], v33\n"
"buffer_load_dwordx4 v[0:3], v39, s[40:43], s58 offen offset:0 nt\n"
"buffer_load_dwordx4 v[4:7], v39, s[40:43], s58 offen offset:1024 nt\n"
"v_mov_b32_dpp v20, v28 row_newbcast:6 row_mask:0xf bank_mask:0x1\n"
"v_mov_b32_dpp v21, v28 row_newbcast:6 row_mask:0xf bank_mask:0x2\n"
"v_mov_b32_dpp v22, v28 row_newbcast:7 row_mask:0xf bank_mask:0x1\n"
"v_mov_b32_dpp v23, v28 row_newbcast:7 row_mask:0xf bank_mask:0x2\n"
"s_waitcnt vmcnt(6)\n"
"v_smfmac_f32_16x16x64_bf16 v[34:37], v[16:19], v[8:15], v33\n"
"buffer_load_dwordx4 v[8:11], v39, s[40:43], s64 offen offset:2048 nt\n"
"buffer_load_dwordx4 v[12:15], v39, s[40:43], s64 offen offset:3072 nt\n"
"v_mov_b32_dpp v16, v30 row_newbcast:0 row_mask:0xf bank_mask:0x1\n"
"v_mov_b32_dpp v17, v30 row_newbcast:0 row_mask:0xf bank_mask:0x2\n"
"v_mov_b32_dpp v18, v30 row_newbcast:1 row_mask:0xf bank_mask:0x1\n"
"v_mov_b32_dpp v19, v30 row_newbcast:1 row_mask:0xf bank_mask:0x2\n"
"s_waitcnt vmcnt(6)\n"
"v_smfmac_f32_16x16x64_bf16 v[34:37], v[20:23], v[104:111], v33\n"
"buffer_load_dwordx4 v[104:107], v39, s[40:43], s58 offen offset:2048 nt\n"
"buffer_load_dwordx4 v[108:111], v39, s[40:43], s58 offen offset:3072 nt\n"
"v_mov_b32_dpp v20, v28 row_newbcast:8 row_mask:0xf bank_mask:0x1\n"
"v_mov_b32_dpp v21, v28 row_newbcast:8 row_mask:0xf bank_mask:0x2\n"
"v_mov_b32_dpp v22, v28 row_newbcast:9 row_mask:0xf bank_mask:0x1\n"
"v_mov_b32_dpp v23, v28 row_newbcast:9 row_mask:0xf bank_mask:0x2\n"
"s_waitcnt vmcnt(6)\n"
"v_smfmac_f32_16x16x64_bf16 v[34:37], v[16:19], v[112:119], v33\n"
"buffer_load_dwordx4 v[112:115], v39, s[40:43], s65 offen offset:0 nt\n"
"buffer_load_dwordx4 v[116:119], v39, s[40:43], s65 offen offset:1024 nt\n"
"v_mov_b32_dpp v16, v30 row_newbcast:2 row_mask:0xf bank_mask:0x1\n"
"v_mov_b32_dpp v17, v30 row_newbcast:2 row_mask:0xf bank_mask:0x2\n"
"v_mov_b32_dpp v18, v30 row_newbcast:3 row_mask:0xf bank_mask:0x1\n"
"v_mov_b32_dpp v19, v30 row_newbcast:3 row_mask:0xf bank_mask:0x2\n"
"s_waitcnt vmcnt(6)\n"
"v_smfmac_f32_16x16x64_bf16 v[34:37], v[20:23], v[0:7], v33\n"
"buffer_load_dwordx4 v[0:3], v39, s[40:43], s65 offen offset:2048 nt\n"
"buffer_load_dwordx4 v[4:7], v39, s[40:43], s65 offen offset:3072 nt\n"
"v_mov_b32_dpp v20, v28 row_newbcast:10 row_mask:0xf bank_mask:0x1\n"
"v_mov_b32_dpp v21, v28 row_newbcast:10 row_mask:0xf bank_mask:0x2\n"
"v_mov_b32_dpp v22, v28 row_newbcast:11 row_mask:0xf bank_mask:0x1\n"
"v_mov_b32_dpp v23, v28 row_newbcast:11 row_mask:0xf bank_mask:0x2\n"
"s_waitcnt vmcnt(6)\n"
"v_smfmac_f32_16x16x64_bf16 v[34:37], v[16:19], v[8:15], v33\n"
"buffer_load_dwordx4 v[8:11], v39, s[40:43], s59 offen offset:0 nt\n"
"buffer_load_dwordx4 v[12:15], v39, s[40:43], s59 offen offset:1024 nt\n"
"v_mov_b32_dpp v16, v30 row_newbcast:4 row_mask:0xf bank_mask:0x1\n"
"v_mov_b32_dpp v17, v30 row_newbcast:4 row_mask:0xf bank_mask:0x2\n"
"v_mov_b32_dpp v18, v30 row_newbcast:5 row_mask:0xf bank_mask:0x1\n"
"v_mov_b32_dpp v19, v30 row_newbcast:5 row_mask:0xf bank_mask:0x2\n"
"s_waitcnt vmcnt(6)\n"
"v_smfmac_f32_16x16x64_bf16 v[34:37], v[20:23], v[104:111], v33\n"
"buffer_load_dwordx4 v[104:107], v39, s[40:43], s66 offen offset:0 nt\n"
"buffer_load_dwordx4 v[108:111], v39, s[40:43], s66 offen offset:1024 nt\n"
"v_mov_b32_dpp v20, v30 row_newbcast:6 row_mask:0xf bank_mask:0x1\n"
"v_mov_b32_dpp v21, v30 row_newbcast:6 row_mask:0xf bank_mask:0x2\n"
"v_mov_b32_dpp v22, v30 row_newbcast:7 row_mask:0xf bank_mask:0x1\n"
"v_mov_b32_dpp v23, v30 row_newbcast:7 row_mask:0xf bank_mask:0x2\n"
"s_waitcnt vmcnt(6)\n"
"v_smfmac_f32_16x16x64_bf16 v[34:37], v[16:19], v[112:119], v33\n"
"buffer_load_dwordx4 v[112:115], v39, s[40:43], s59 offen offset:2048 nt\n"
"buffer_load_dwordx4 v[116:119], v39, s[40:43], s59 offen offset:3072 nt\n"
"v_mov_b32_dpp v16, v28 row_newbcast:12 row_mask:0xf bank_mask:0x1\n"
"v_mov_b32_dpp v17, v28 row_newbcast:12 row_mask:0xf bank_mask:0x2\n"
"v_mov_b32_dpp v18, v28 row_newbcast:13 row_mask:0xf bank_mask:0x1\n"
"v_mov_b32_dpp v19, v28 row_newbcast:13 row_mask:0xf bank_mask:0x2\n"
"s_waitcnt vmcnt(6)\n"
"v_smfmac_f32_16x16x64_bf16 v[34:37], v[20:23], v[0:7], v33\n"
"buffer_load_dwordx4 v[0:3], v39, s[40:43], s66 offen offset:2048 nt\n"
"buffer_load_dwordx4 v[4:7], v39, s[40:43], s66 offen offset:3072 nt\n"
"v_mov_b32_dpp v20, v30 row_newbcast:8 row_mask:0xf bank_mask:0x1\n"
"v_mov_b32_dpp v21, v30 row_newbcast:8 row_mask:0xf bank_mask:0x2\n"
"v_mov_b32_dpp v22, v30 row_newbcast:9 row_mask:0xf bank_mask:0x1\n"
"v_mov_b32_dpp v23, v30 row_newbcast:9 row_mask:0xf bank_mask:0x2\n"
"s_waitcnt vmcnt(6)\n"
"v_smfmac_f32_16x16x64_bf16 v[34:37], v[16:19], v[8:15], v33\n"
"buffer_load_dwordx4 v[8:11], v39, s[40:43], s67 offen offset:0 nt\n"
"buffer_load_dwordx4 v[12:15], v39, s[40:43], s67 offen offset:1024 nt\n"
"v_mov_b32_dpp v16, v28 row_newbcast:14 row_mask:0xf bank_mask:0x1\n"
"v_mov_b32_dpp v17, v28 row_newbcast:14 row_mask:0xf bank_mask:0x2\n"
"v_mov_b32_dpp v18, v28 row_newbcast:15 row_mask:0xf bank_mask:0x1\n"
"v_mov_b32_dpp v19, v28 row_newbcast:15 row_mask:0xf bank_mask:0x2\n"
"s_waitcnt vmcnt(6)\n"
"v_smfmac_f32_16x16x64_bf16 v[34:37], v[20:23], v[104:111], v33\n"
"buffer_load_dwordx4 v[104:107], v39, s[40:43], s60 offen offset:0 nt\n"
"buffer_load_dwordx4 v[108:111], v39, s[40:43], s60 offen offset:1024 nt\n"
"v_mov_b32_dpp v20, v30 row_newbcast:10 row_mask:0xf bank_mask:0x1\n"
"v_mov_b32_dpp v21, v30 row_newbcast:10 row_mask:0xf bank_mask:0x2\n"
"v_mov_b32_dpp v22, v30 row_newbcast:11 row_mask:0xf bank_mask:0x1\n"
"v_mov_b32_dpp v23, v30 row_newbcast:11 row_mask:0xf bank_mask:0x2\n"
"s_waitcnt vmcnt(6)\n"
"v_smfmac_f32_16x16x64_bf16 v[34:37], v[16:19], v[112:119], v33\n"
"buffer_load_dwordx4 v[112:115], v39, s[40:43], s67 offen offset:2048 nt\n"
"buffer_load_dwordx4 v[116:119], v39, s[40:43], s67 offen offset:3072 nt\n"
"v_mov_b32_dpp v16, v30 row_newbcast:12 row_mask:0xf bank_mask:0x1\n"
"v_mov_b32_dpp v17, v30 row_newbcast:12 row_mask:0xf bank_mask:0x2\n"
"v_mov_b32_dpp v18, v30 row_newbcast:13 row_mask:0xf bank_mask:0x1\n"
"v_mov_b32_dpp v19, v30 row_newbcast:13 row_mask:0xf bank_mask:0x2\n"
"s_waitcnt vmcnt(6)\n"
"v_smfmac_f32_16x16x64_bf16 v[34:37], v[20:23], v[0:7], v33\n"
"buffer_load_dwordx4 v[0:3], v39, s[40:43], s60 offen offset:2048 nt\n"
"buffer_load_dwordx4 v[4:7], v39, s[40:43], s60 offen offset:3072 nt\n"
"v_mov_b32_dpp v20, v29 row_newbcast:0 row_mask:0xf bank_mask:0x1\n"
"v_mov_b32_dpp v21, v29 row_newbcast:0 row_mask:0xf bank_mask:0x2\n"
"v_mov_b32_dpp v22, v29 row_newbcast:1 row_mask:0xf bank_mask:0x1\n"
"v_mov_b32_dpp v23, v29 row_newbcast:1 row_mask:0xf bank_mask:0x2\n"
"s_waitcnt vmcnt(6)\n"
"v_smfmac_f32_16x16x64_bf16 v[34:37], v[16:19], v[8:15], v33\n"
"buffer_load_dwordx4 v[8:11], v39, s[40:43], s68 offen offset:0 nt\n"
"buffer_load_dwordx4 v[12:15], v39, s[40:43], s68 offen offset:1024 nt\n"
"v_mov_b32_dpp v16, v30 row_newbcast:14 row_mask:0xf bank_mask:0x1\n"
"v_mov_b32_dpp v17, v30 row_newbcast:14 row_mask:0xf bank_mask:0x2\n"
"v_mov_b32_dpp v18, v30 row_newbcast:15 row_mask:0xf bank_mask:0x1\n"
"v_mov_b32_dpp v19, v30 row_newbcast:15 row_mask:0xf bank_mask:0x2\n"
"s_waitcnt vmcnt(6)\n"
"v_smfmac_f32_16x16x64_bf16 v[34:37], v[20:23], v[104:111], v33\n"
"buffer_load_dwordx4 v[104:107], v39, s[40:43], s61 offen offset:0 nt\n"
"buffer_load_dwordx4 v[108:111], v39, s[40:43], s61 offen offset:1024 nt\n"
"v_mov_b32_dpp v20, v29 row_newbcast:2 row_mask:0xf bank_mask:0x1\n"
"v_mov_b32_dpp v21, v29 row_newbcast:2 row_mask:0xf bank_mask:0x2\n"
"v_mov_b32_dpp v22, v29 row_newbcast:3 row_mask:0xf bank_mask:0x1\n"
"v_mov_b32_dpp v23, v29 row_newbcast:3 row_mask:0xf bank_mask:0x2\n"
"s_waitcnt vmcnt(6)\n"
"v_smfmac_f32_16x16x64_bf16 v[34:37], v[16:19], v[112:119], v33\n"
"buffer_load_dwordx4 v[112:115], v39, s[40:43], s68 offen offset:2048 nt\n"
"buffer_load_dwordx4 v[116:119], v39, s[40:43], s68 offen offset:3072 nt\n"
"v_mov_b32_dpp v16, v31 row_newbcast:0 row_mask:0xf bank_mask:0x1\n"
"v_mov_b32_dpp v17, v31 row_newbcast:0 row_mask:0xf bank_mask:0x2\n"
"v_mov_b32_dpp v18, v31 row_newbcast:1 row_mask:0xf bank_mask:0x1\n"
"v_mov_b32_dpp v19, v31 row_newbcast:1 row_mask:0xf bank_mask:0x2\n"
"s_waitcnt vmcnt(6)\n"
"v_smfmac_f32_16x16x64_bf16 v[34:37], v[20:23], v[0:7], v33\n"
"v_mov_b32_dpp v20, v29 row_newbcast:4 row_mask:0xf bank_mask:0x1\n"
"v_mov_b32_dpp v21, v29 row_newbcast:4 row_mask:0xf bank_mask:0x2\n"
"v_mov_b32_dpp v22, v29 row_newbcast:5 row_mask:0xf bank_mask:0x1\n"
"v_mov_b32_dpp v23, v29 row_newbcast:5 row_mask:0xf bank_mask:0x2\n"
"s_waitcnt vmcnt(4)\n"
"v_smfmac_f32_16x16x64_bf16 v[34:37], v[16:19], v[8:15], v33\n"
"v_mov_b32_dpp v16, v31 row_newbcast:2 row_mask:0xf bank_mask:0x1\n"
"v_mov_b32_dpp v17, v31 row_newbcast:2 row_mask:0xf bank_mask:0x2\n"
"v_mov_b32_dpp v18, v31 row_newbcast:3 row_mask:0xf bank_mask:0x1\n"
"v_mov_b32_dpp v19, v31 row_newbcast:3 row_mask:0xf bank_mask:0x2\n"
"s_waitcnt vmcnt(2)\n"
"v_smfmac_f32_16x16x64_bf16 v[34:37], v[20:23], v[104:111], v33\n"
"s_waitcnt vmcnt(0)\n"
"v_smfmac_f32_16x16x64_bf16 v[34:37], v[16:19], v[112:119], v33\n"
"s_mul_i32 s88, s15, 576\n"
"s_lshl_b32 s89, s15, 7\n"
"v_and_b32 v40, 0xf0, v39\n"
"v_lshrrev_b32 v41, 4, v39\n"
"v_lshlrev_b32 v40, 1, v40\n"
"v_and_b32 v44, 16, v41\n"
"v_add3_u32 v40, v40, v44, s88\n"
"v_and_b32 v44, 3, v41\n"
"v_lshrrev_b32 v45, 2, v41\n"
"v_lshl_add_u32 v45, v45, 3, s89\n"
"v_mul_u32_u24 v41, 576, v44\n"
"v_lshlrev_b32 v44, 3, v44\n"
"v_add_u32 v41, v41, v45\n"
"s_mov_b64 vcc, 2\n"
"v_mov_b32 v42, 512\n"
"v_cndmask_b32 v45, v45, v42, vcc\n"
"s_add_u32 s90, s95, 286720\n"
"s_mov_b32 exec_hi, 0\n"
"ds_write_b128 v40, v[34:37] offset:0\n"
"s_mov_b64 exec, -1\n"
"s_waitcnt lgkmcnt(0)\n"
"s_barrier\n"
"ds_read_b64 v[16:17], v44 offset:4672\n"
"ds_read_b64 v[18:19], v41 offset:0\n"
"s_waitcnt lgkmcnt(1)\n"
"v_max_f32_dpp v20, v16, v16 quad_perm:[1,0,3,2] row_mask:0xf bank_mask:0xf\n"
"s_nop 1\n"
"v_max_f32_dpp v20, v20, v20 quad_perm:[2,3,0,1] row_mask:0xf bank_mask:0xf\n"
"v_sub_f32 v16, v16, v20\n"
"v_exp_f32 v16, v16\n"
"s_waitcnt lgkmcnt(0)\n"
"v_mul_f32 v18, v16, v18\n"
"v_mul_f32 v19, v16, v19\n"
"v_mul_f32 v21, v16, v17\n"
"v_add_f32_dpp v18, v18, v18 quad_perm:[1,0,3,2] row_mask:0xf bank_mask:0xf\n"
"v_add_f32_dpp v19, v19, v19 quad_perm:[1,0,3,2] row_mask:0xf bank_mask:0xf\n"
"v_add_f32_dpp v21, v21, v21 quad_perm:[1,0,3,2] row_mask:0xf bank_mask:0xf\n"
"v_add_f32_dpp v18, v18, v18 quad_perm:[2,3,0,1] row_mask:0xf bank_mask:0xf\n"
"v_add_f32_dpp v19, v19, v19 quad_perm:[2,3,0,1] row_mask:0xf bank_mask:0xf\n"
"v_add_f32_dpp v21, v21, v21 quad_perm:[2,3,0,1] row_mask:0xf bank_mask:0xf\n"
"v_cndmask_b32 v18, v18, v20, vcc\n"
"v_cndmask_b32 v19, v19, v21, vcc\n"
"v_min_u32 v18, -2, v18\n"
"v_min_u32 v19, -2, v19\n"
"s_mov_b64 exec, s[34:35]\n"
"buffer_store_dwordx2 v[18:19], v45, s[52:55], s90 offen sc1\n"
"s_mov_b64 exec, -1\n"
"ds_read_b128 v[104:107], v46 offset:0\n"
"ds_read_b128 v[108:111], v46 offset:1024\n"
"ds_read_b128 v[112:115], v46 offset:2048\n"
"ds_read_b128 v[116:119], v46 offset:3072\n"
"s_lshl_b32 s88, s12, 8\n"
"s_add_u32 s75, s88, 32768\n"
"v_lshrrev_b32 v45, 2, v39\n"
"v_and_b32 v45, 0xc0, v45\n"
"v_and_or_b32 v45, v39, 48, v45\n"
"v_mov_b64 v[16:17], 0\n"
"v_mov_b64 v[18:19], 0\n"
"v_mov_b64 v[20:21], 0\n"
"v_mov_b64 v[22:23], 0\n"
"v_mov_b64 v[24:25], 0\n"
"v_mov_b64 v[26:27], 0\n"
"v_sub_u32 v40, v46, v39\n"
"v_subrev_u32 v40, 16, v40\n"
"v_lshrrev_b32 v41, 8, v39\n"
"v_lshl_add_u32 v38, v41, 7, v40\n"
"v_lshl_add_u32 v28, v41, 5, v40\n"
"v_bfe_u32 v41, v39, 5, 1\n"
"v_lshl_add_u32 v38, v41, 3, v38\n"
"v_bfe_u32 v41, v39, 6, 2\n"
"v_lshl_add_u32 v38, v41, 5, v38\n"
"v_min_u32 v41, 1, v41\n"
"v_lshl_add_u32 v28, v41, 4, v28\n"
"v_mov_b64 v[52:53], 0\n"
"v_mov_b64 v[54:55], 0\n"
"s_mul_i32 s88, s15, -38176\n"
"s_add_u32 s88, s88, -3216\n"
"v_bfe_u32 v41, v39, 4, 1\n"
"v_add_u32 v29, s88, v38\n"
"v_add_u32 v30, s88, v28\n"
"v_mul_u32_u24 v40, 2064, v41\n"
"v_add_u32 v38, v38, v40\n"
"v_mul_u32_u24 v40, 528, v41\n"
"v_add_u32 v29, v29, v40\n"
"v_subrev_u32 v40, 16, v46\n"
"v_add_u32 v41, s88, v40\n"
"ds_write_b128 v40, v[52:55] offset:0\n"
"ds_write_b128 v40, v[52:55] offset:1024\n"
"ds_write_b128 v40, v[52:55] offset:2048\n"
"ds_write_b128 v40, v[52:55] offset:3072\n"
"ds_write_b128 v46, v[52:55] offset:3072\n"
"s_mov_b32 exec_hi, 0xffff\n"
"ds_write_b128 v41, v[52:55]\n"
"s_mov_b32 exec_hi, -1\n"
"s_mul_i32 s88, s15, 576\n"
"s_lshl_b32 s89, s15, 7\n"
"v_and_b32 v34, 0xf0, v39\n"
"v_lshrrev_b32 v35, 4, v39\n"
"v_lshlrev_b32 v34, 1, v34\n"
"v_and_b32 v36, 16, v35\n"
"v_add3_u32 v34, v34, v36, s88\n"
"v_and_b32 v36, 3, v35\n"
"v_lshrrev_b32 v37, 2, v35\n"
"v_lshl_add_u32 v37, v37, 3, s89\n"
"v_mul_u32_u24 v35, 576, v36\n"
"v_lshlrev_b32 v36, 3, v36\n"
"v_add_u32 v35, v35, v37\n"
"s_mov_b64 vcc, 2\n"
"v_mov_b32 v40, 512\n"
"v_cndmask_b32 v37, v37, v40, vcc\n"
"s_add_u32 s90, s95, 430080\n"
"s_lshl_b32 s88, s15, 3\n"
"v_mov_b32 v31, s88\n"
"v_mov_b32 v32, 0x3fb8aa3b\n"
"v_mov_b32 v40, 0xff800000\n"
"v_mov_b64 v[0:1], 0\n"
"v_mov_b64 v[2:3], 0\n"
"v_mov_b64 v[4:5], 0\n"
"v_mov_b64 v[6:7], 0\n"
"v_mov_b64 v[8:9], 0\n"
"v_mov_b64 v[10:11], 0\n"
"s_waitcnt vmcnt(0)\n"
"s_waitcnt lgkmcnt(0)\n"
"s_nop 1\n"
".Lk_q3_142:\n"
"s_cmp_eq_u32 s96, 0\n"
"s_cbranch_scc1 .Lk_q3ok_143\n"
"s_sub_u32 s96, s96, 1\n"
"buffer_load_dwordx4 v[12:15], v45, s[52:55], s75 offen sc1\n"
"s_waitcnt vmcnt(0)\n"
"v_max3_u32 v44, v12, v13, v14\n"
"v_max_u32 v44, v44, v15\n"
"v_cmp_eq_u32 vcc, -1, v44\n"
"s_cbranch_vccnz .Lk_q3_142\n"
".Lk_q3ok_143:\n"
"s_mov_b64 exec, s[76:77]\n"
"v_mov_b64 v[0:1], v[12:13]\n"
"v_mov_b64 v[2:3], v[14:15]\n"
"s_mov_b64 exec, s[78:79]\n"
"v_mov_b64 v[4:5], v[12:13]\n"
"v_mov_b64 v[6:7], v[14:15]\n"
"s_mov_b64 exec, -1\n"
"v_smfmac_f32_16x16x64_bf16 v[16:19], v[0:3], v[128:135], v33 abid:1\n"
"s_mov_b64 exec, s[80:81]\n"
"v_mov_b64 v[8:9], v[12:13]\n"
"v_mov_b64 v[10:11], v[14:15]\n"
"s_mov_b64 exec, -1\n"
"v_smfmac_f32_16x16x64_bf16 v[16:19], v[4:7], v[136:143], v33 abid:1\n"
"ds_read_b128 v[128:131], v46 offset:4096\n"
"s_not_b64 exec, s[82:83]\n"
"v_mov_b64 v[12:13], 0\n"
"v_mov_b64 v[14:15], 0\n"
"s_mov_b64 exec, -1\n"
"v_smfmac_f32_16x16x64_bf16 v[16:19], v[8:11], v[144:151], v33 abid:1\n"
"ds_read_b128 v[132:135], v46 offset:5120\n"
"v_smfmac_f32_16x16x64_bf16 v[16:19], v[12:15], v[152:159], v33 abid:1\n"
"ds_read_b128 v[136:139], v46 offset:6144\n"
"v_smfmac_f32_16x16x64_bf16 v[20:23], v[0:3], v[160:167], v33 abid:1\n"
"ds_read_b128 v[140:143], v46 offset:7168\n"
"v_smfmac_f32_16x16x64_bf16 v[24:27], v[0:3], a[64:71], v33 abid:1\n"
"ds_read_b128 v[144:147], v46 offset:8192\n"
"v_smfmac_f32_16x16x64_bf16 v[24:27], v[4:7], a[72:79], v33 abid:1\n"
"ds_read_b128 v[148:151], v46 offset:9216\n"
"v_smfmac_f32_16x16x64_bf16 v[24:27], v[8:11], a[80:87], v33 abid:1\n"
"ds_read_b128 v[152:155], v46 offset:10240\n"
"v_add_f32 v17, v17, v19\n"
"v_add_f32 v16, v16, v18\n"
"v_smfmac_f32_16x16x64_bf16 v[24:27], v[12:15], v[176:183], v33 abid:1\n"
"ds_read_b128 v[156:159], v46 offset:11264\n"
"v_add_f32_dpp v51, v17, v16 quad_perm:[1,1,3,3] row_mask:0xf bank_mask:0xf\n"
"v_mov_b64 v[16:17], 0\n"
"v_mov_b64 v[18:19], 0\n"
"s_nop 1\n"
"v_smfmac_f32_16x16x64_bf16 v[16:19], v[0:3], v[184:191], v33 abid:1\n"
"ds_read_b128 v[160:163], v46 offset:12288\n"
"v_add_f32 v20, v20, v22\n"
"v_add_f32 v21, v21, v23\n"
"v_smfmac_f32_16x16x64_bf16 v[16:19], v[4:7], v[192:199], v33 abid:1\n"
"ds_read_b128 v[164:167], v46 offset:13312\n"
"v_add_f32_dpp v22, v20, v21 quad_perm:[0,0,2,2] row_mask:0xf bank_mask:0xf\n"
"v_cndmask_b32 v51, v51, v22, s[18:19]\n"
"v_smfmac_f32_16x16x64_bf16 v[16:19], v[8:11], v[200:207], v33 abid:1\n"
"ds_read_b128 a[64:67], v46 offset:14336\n"
"v_mov_b64 v[20:21], 0\n"
"v_mov_b64 v[22:23], 0\n"
"v_smfmac_f32_16x16x64_bf16 v[16:19], v[12:15], v[208:215], v33 abid:1\n"
"ds_read_b128 a[68:71], v46 offset:15360\n"
"v_cndmask_b32 v51, v40, v51, s[84:85]\n"
"v_add_f32 v25, v25, v27\n"
"v_smfmac_f32_16x16x64_bf16 v[20:23], v[0:3], v[216:223], v33 abid:1\n"
"ds_read_b128 a[72:75], v46 offset:16384\n"
"v_add_f32 v24, v24, v26\n"
"v_smfmac_f32_16x16x64_bf16 v[20:23], v[4:7], v[224:231], v33 abid:1\n"
"ds_read_b128 a[76:79], v46 offset:17408\n"
"v_add_f32_dpp v47, v25, v24 quad_perm:[1,1,3,3] row_mask:0xf bank_mask:0xf\n"
"v_mov_b64 v[24:25], 0\n"
"v_smfmac_f32_16x16x64_bf16 v[20:23], v[8:11], v[232:239], v33 abid:1\n"
"ds_read_b128 a[80:83], v46 offset:18432\n"
"v_mov_b64 v[26:27], 0\n"
"v_add_f32 v16, v16, v18\n"
"v_smfmac_f32_16x16x64_bf16 v[20:23], v[12:15], v[240:247], v33 abid:1\n"
"s_waitcnt lgkmcnt(12)\n"
"ds_read_b128 a[84:87], v46 offset:19456\n"
"v_add_f32 v17, v17, v19\n"
"v_smfmac_f32_16x16x64_bf16 v[24:27], v[0:3], v[248:255], v33 abid:1\n"
"ds_read_b128 v[176:179], v46 offset:20480\n"
"v_add_f32_dpp v18, v16, v17 quad_perm:[0,0,2,2] row_mask:0xf bank_mask:0xf\n"
"v_cndmask_b32 v47, v47, v18, s[18:19]\n"
"v_smfmac_f32_16x16x64_bf16 v[24:27], v[4:7], a[88:95], v33 abid:1\n"
"ds_read_b128 v[180:183], v46 offset:21504\n"
"v_mov_b64 v[16:17], 0\n"
"v_mov_b64 v[18:19], 0\n"
"v_smfmac_f32_16x16x64_bf16 v[24:27], v[8:11], a[96:103], v33 abid:1\n"
"s_waitcnt lgkmcnt(12)\n"
"ds_read_b128 v[184:187], v46 offset:22528\n"
"v_add_f32 v21, v21, v23\n"
"v_add_f32 v20, v20, v22\n"
"v_smfmac_f32_16x16x64_bf16 v[24:27], v[12:15], a[104:111], v33 abid:1\n"
"ds_read_b128 v[188:191], v46 offset:23552\n"
"v_add_f32_dpp v48, v21, v20 quad_perm:[1,1,3,3] row_mask:0xf bank_mask:0xf\n"
"v_mov_b64 v[20:21], 0\n"
"v_smfmac_f32_16x16x64_bf16 v[16:19], v[0:3], a[112:119], v33 abid:1\n"
"ds_read_b128 v[192:195], v46 offset:24576\n"
"v_mov_b64 v[22:23], 0\n"
"v_smfmac_f32_16x16x64_bf16 v[16:19], v[4:7], a[120:127], v33 abid:1\n"
"s_waitcnt lgkmcnt(12)\n"
"ds_read_b128 v[196:199], v46 offset:25600\n"
"v_add_f32 v24, v24, v26\n"
"v_add_f32 v25, v25, v27\n"
"v_smfmac_f32_16x16x64_bf16 v[16:19], v[8:11], a[128:135], v33 abid:1\n"
"ds_read_b128 v[200:203], v46 offset:26624\n"
"v_add_f32_dpp v26, v24, v25 quad_perm:[0,0,2,2] row_mask:0xf bank_mask:0xf\n"
"v_cndmask_b32 v48, v48, v26, s[18:19]\n"
"v_smfmac_f32_16x16x64_bf16 v[16:19], v[12:15], a[136:143], v33 abid:1\n"
"ds_read_b128 v[204:207], v46 offset:27648\n"
"v_mov_b64 v[24:25], 0\n"
"v_mov_b64 v[26:27], 0\n"
"v_smfmac_f32_16x16x64_bf16 v[20:23], v[0:3], a[144:151], v33 abid:1\n"
"s_waitcnt lgkmcnt(12)\n"
"ds_read_b128 v[208:211], v46 offset:28672\n"
"v_max3_f32 v42, v51, v47, v48\n"
"v_smfmac_f32_16x16x64_bf16 v[20:23], v[4:7], a[0:7], v33 abid:1\n"
"ds_read_b128 v[212:215], v46 offset:29696\n"
"v_add_f32 v17, v17, v19\n"
"v_add_f32 v16, v16, v18\n"
"v_smfmac_f32_16x16x64_bf16 v[20:23], v[8:11], a[8:15], v33 abid:1\n"
"ds_read_b128 v[216:219], v46 offset:30720\n"
"v_add_f32_dpp v49, v17, v16 quad_perm:[1,1,3,3] row_mask:0xf bank_mask:0xf\n"
"v_mov_b64 v[16:17], 0\n"
"v_smfmac_f32_16x16x64_bf16 v[20:23], v[12:15], a[16:23], v33 abid:1\n"
"s_waitcnt lgkmcnt(12)\n"
"ds_read_b128 v[220:223], v46 offset:31744\n"
"v_mov_b64 v[18:19], 0\n"
"v_smfmac_f32_16x16x64_bf16 v[24:27], v[0:3], a[24:31], v33 abid:1\n"
"ds_read_b128 v[224:227], v46 offset:32768\n"
"v_smfmac_f32_16x16x64_bf16 v[24:27], v[4:7], a[32:39], v33 abid:1\n"
"ds_read_b128 v[228:231], v46 offset:33792\n"
"v_smfmac_f32_16x16x64_bf16 v[24:27], v[8:11], a[40:47], v33 abid:1\n"
"s_waitcnt lgkmcnt(12)\n"
"ds_read_b128 v[232:235], v46 offset:34816\n"
"v_add_f32 v20, v20, v22\n"
"v_add_f32 v21, v21, v23\n"
"v_smfmac_f32_16x16x64_bf16 v[24:27], v[12:15], a[48:55], v33 abid:1\n"
"ds_read_b128 v[236:239], v46 offset:35840\n"
"v_add_f32_dpp v22, v20, v21 quad_perm:[0,0,2,2] row_mask:0xf bank_mask:0xf\n"
"v_cndmask_b32 v49, v49, v22, s[18:19]\n"
"v_smfmac_f32_16x16x64_bf16 v[16:19], v[0:3], a[56:63], v33 abid:1\n"
"ds_read_b128 v[240:243], v46 offset:36864\n"
"v_smfmac_f32_16x16x64_bf16 v[16:19], v[4:7], a[152:159], v33 abid:1\n"
"s_waitcnt lgkmcnt(12)\n"
"ds_read_b128 v[244:247], v46 offset:37888\n"
"v_add_f32 v25, v25, v27\n"
"v_add_f32 v24, v24, v26\n"
"v_smfmac_f32_16x16x64_bf16 v[16:19], v[8:11], a[160:167], v33 abid:1\n"
"s_nop 0\n"
"v_add_f32_dpp v50, v25, v24 quad_perm:[1,1,3,3] row_mask:0xf bank_mask:0xf\n"
"v_smfmac_f32_16x16x64_bf16 v[16:19], v[12:15], a[168:175], v33 abid:1\n"
"s_nop 7\n"
"v_add_f32 v16, v16, v18\n"
"v_add_f32 v17, v17, v19\n"
"s_nop 1\n"
"v_add_f32_dpp v18, v16, v17 quad_perm:[0,0,2,2] row_mask:0xf bank_mask:0xf\n"
"v_cndmask_b32 v50, v50, v18, s[18:19]\n"
"v_max3_f32 v42, v42, v49, v50\n"
"v_cvt_pk_bf16_f32 v47, 0, v47\n"
"v_cvt_pk_bf16_f32 v48, 0, v48\n"
"v_max_f32_dpp v42, v42, v42 row_ror:8 row_mask:0xf bank_mask:0xf\n"
"v_cvt_pk_bf16_f32 v49, 0, v49\n"
"v_cvt_pk_bf16_f32 v50, 0, v50\n"
"v_max_f32_dpp v42, v42, v42 row_ror:4 row_mask:0xf bank_mask:0xf\n"
"v_cvt_pk_bf16_f32 v51, 0, v51\n"
"v_mul_f32 v47, 0x3db504f3, v47\n"
"v_max_f32_dpp v42, v42, v42 row_ror:2 row_mask:0xf bank_mask:0xf\n"
"v_mul_f32 v48, 0x3db504f3, v48\n"
"v_mul_f32 v49, 0x3db504f3, v49\n"
"v_max_f32_dpp v42, v42, v42 row_ror:1 row_mask:0xf bank_mask:0xf\n"
"v_mul_f32 v50, 0x3db504f3, v50\n"
"v_mul_f32 v51, 0x3db504f3, v51\n"
"v_max_f32_dpp v42, v42, v42 row_bcast:15 row_mask:0xa bank_mask:0xf\n"
"v_cvt_pk_bf16_f32 v47, 0, v47\n"
"v_cvt_pk_bf16_f32 v48, 0, v48\n"
"v_max_f32_dpp v42, v42, v42 row_bcast:31 row_mask:0xc bank_mask:0xf\n"
"v_cvt_pk_bf16_f32 v42, 0, v42\n"
"v_mul_f32 v42, 0x3db504f3, v42\n"
"v_cvt_pk_bf16_f32 v42, 0, v42\n"
"v_mul_f32 v42, 0x3fb8aa3b, v42\n"
"v_cvt_pk_bf16_f32 v49, 0, v49\n"
"v_readlane_b32 s88, v42, 63\n"
"v_cvt_pk_bf16_f32 v50, 0, v50\n"
"v_cvt_pk_bf16_f32 v51, 0, v51\n"
"v_fma_f32 v47, v47, v32, -s88\n"
"v_fma_f32 v48, v48, v32, -s88\n"
"v_exp_f32 v47, v47\n"
"v_exp_f32 v48, v48\n"
"v_cvt_pk_bf16_f32 v16, v47, 0\n"
"ds_write_b32 v38, v16 offset:0\n"
"ds_write_b32 v38, v16 offset:20\n"
"s_waitcnt lgkmcnt(12)\n"
"ds_read_b128 v[0:3], v28 offset:0\n"
"ds_read_b128 v[4:7], v28 offset:128\n"
"ds_read_b128 v[8:11], v28 offset:256\n"
"s_waitcnt lgkmcnt(12)\n"
"ds_read_b128 v[12:15], v28 offset:384\n"
"v_cvt_pk_bf16_f32 v17, v48, 0\n"
"ds_write_b32 v38, v17 offset:512\n"
"ds_write_b32 v38, v17 offset:532\n"
"v_fma_f32 v49, v49, v32, -s88\n"
"v_fma_f32 v50, v50, v32, -s88\n"
"v_fma_f32 v51, v51, v32, -s88\n"
"v_exp_f32 v49, v49\n"
"v_exp_f32 v50, v50\n"
"v_exp_f32 v51, v51\n"
"s_waitcnt lgkmcnt(12)\n"
"ds_read_b128 v[248:251], v28 offset:2064\n"
"ds_read_b128 v[252:255], v28 offset:2192\n"
"ds_read_b128 a[88:91], v28 offset:2320\n"
"s_waitcnt lgkmcnt(12)\n"
"ds_read_b128 a[92:95], v28 offset:2448\n"
"v_add_f32 v43, v47, v48\n"
"v_cvt_pk_bf16_f32 v18, v49, 0\n"
"v_cvt_pk_bf16_f32 v19, v50, 0\n"
"v_cvt_pk_bf16_f32 v20, v51, 0\n"
"v_add_f32 v40, v49, v50\n"
"v_add_f32 v43, v43, v51\n"
"v_add_f32 v43, v43, v40\n"
"s_waitcnt lgkmcnt(9)\n"
"v_smfmac_f32_16x16x64_bf16 v[52:55], v[0:3], v[104:111], v33\n"
"ds_write_b32 v38, v18 offset:1024\n"
"ds_read_b128 a[96:99], v28 offset:512\n"
"s_waitcnt lgkmcnt(10)\n"
"v_smfmac_f32_16x16x64_bf16 v[52:55], v[4:7], v[112:119], v33\n"
"ds_write_b32 v38, v18 offset:1044\n"
"ds_read_b128 a[100:103], v28 offset:640\n"
"s_waitcnt lgkmcnt(11)\n"
"v_smfmac_f32_16x16x64_bf16 v[52:55], v[8:11], v[128:135], v33\n"
"ds_write_b32 v38, v19 offset:1536\n"
"ds_read_b128 v[0:3], v28 offset:768\n"
"s_waitcnt lgkmcnt(12)\n"
"v_smfmac_f32_16x16x64_bf16 v[52:55], v[12:15], v[136:143], v33\n"
"ds_write_b32 v38, v19 offset:1556\n"
"ds_read_b128 v[4:7], v28 offset:896\n"
"s_waitcnt lgkmcnt(11)\n"
"v_smfmac_f32_16x16x64_bf16 v[52:55], v[248:251], v[144:151], v33\n"
"s_and_b64 exec, exec, s[84:85]\n"
"ds_write_b32 v29, v20 offset:0\n"
"ds_write_b32 v29, v20 offset:20\n"
"s_mov_b64 exec, -1\n"
"ds_read_b128 v[8:11], v28 offset:2576\n"
"s_waitcnt lgkmcnt(13)\n"
"v_smfmac_f32_16x16x64_bf16 v[52:55], v[252:255], v[152:159], v33\n"
"ds_read_b128 v[12:15], v28 offset:2704\n"
"s_waitcnt lgkmcnt(13)\n"
"v_smfmac_f32_16x16x64_bf16 v[52:55], a[88:91], v[160:167], v33\n"
"ds_read_b128 v[248:251], v28 offset:2832\n"
"s_waitcnt lgkmcnt(13)\n"
"v_smfmac_f32_16x16x64_bf16 v[52:55], a[92:95], a[64:71], v33\n"
"ds_read_b128 v[252:255], v28 offset:2960\n"
"s_waitcnt lgkmcnt(12)\n"
"v_smfmac_f32_16x16x64_bf16 v[52:55], a[96:99], a[72:79], v33\n"
"ds_read_b128 a[88:91], v28 offset:1024\n"
"s_waitcnt lgkmcnt(11)\n"
"v_smfmac_f32_16x16x64_bf16 v[52:55], a[100:103], a[80:87], v33\n"
"ds_read_b128 a[92:95], v28 offset:1152\n"
"s_waitcnt lgkmcnt(10)\n"
"v_smfmac_f32_16x16x64_bf16 v[52:55], v[0:3], v[176:183], v33\n"
"ds_read_b128 a[96:99], v28 offset:1280\n"
"s_waitcnt lgkmcnt(9)\n"
"v_smfmac_f32_16x16x64_bf16 v[52:55], v[4:7], v[184:191], v33\n"
"ds_read_b128 a[100:103], v28 offset:1408\n"
"s_waitcnt lgkmcnt(7)\n"
"v_smfmac_f32_16x16x64_bf16 v[52:55], v[8:11], v[192:199], v33\n"
"ds_read_b128 v[0:3], v28 offset:3088\n"
"s_waitcnt lgkmcnt(7)\n"
"v_smfmac_f32_16x16x64_bf16 v[52:55], v[12:15], v[200:207], v33\n"
"ds_read_b128 v[4:7], v28 offset:3216\n"
"s_waitcnt lgkmcnt(7)\n"
"v_smfmac_f32_16x16x64_bf16 v[52:55], v[248:251], v[208:215], v33\n"
"ds_read_b128 v[8:11], v28 offset:3344\n"
"s_waitcnt lgkmcnt(7)\n"
"v_smfmac_f32_16x16x64_bf16 v[52:55], v[252:255], v[216:223], v33\n"
"ds_read_b128 v[12:15], v28 offset:3472\n"
"s_waitcnt lgkmcnt(7)\n"
"v_smfmac_f32_16x16x64_bf16 v[52:55], a[88:91], v[224:231], v33\n"
"ds_read_b128 v[248:251], v28 offset:1536\n"
"s_waitcnt lgkmcnt(7)\n"
"v_smfmac_f32_16x16x64_bf16 v[52:55], a[92:95], v[232:239], v33\n"
"ds_read_b128 v[252:255], v28 offset:1664\n"
"s_waitcnt lgkmcnt(7)\n"
"v_smfmac_f32_16x16x64_bf16 v[52:55], a[96:99], v[240:247], v33\n"
"ds_read_b128 a[88:91], v28 offset:1792\n"
"s_waitcnt lgkmcnt(7)\n"
"v_smfmac_f32_16x16x64_bf16 v[52:55], a[100:103], v[120:127], v33\n"
"ds_read_b128 a[92:95], v28 offset:1920\n"
"s_waitcnt lgkmcnt(7)\n"
"v_smfmac_f32_16x16x64_bf16 v[52:55], v[0:3], v[168:175], v33\n"
"ds_read_b128 a[96:99], v28 offset:3600\n"
"s_waitcnt lgkmcnt(7)\n"
"v_smfmac_f32_16x16x64_bf16 v[52:55], v[4:7], a[176:183], v33\n"
"ds_read_b128 a[100:103], v28 offset:3728\n"
"s_waitcnt lgkmcnt(7)\n"
"v_smfmac_f32_16x16x64_bf16 v[52:55], v[8:11], a[184:191], v33\n"
"ds_read_b128 v[0:3], v28 offset:3856\n"
"s_waitcnt lgkmcnt(7)\n"
"v_smfmac_f32_16x16x64_bf16 v[52:55], v[12:15], a[192:199], v33\n"
"ds_read_b128 v[4:7], v28 offset:3984\n"
"s_waitcnt lgkmcnt(7)\n"
"v_smfmac_f32_16x16x64_bf16 v[52:55], v[248:251], a[200:207], v33\n"
"ds_read_b128 v[8:11], v30 offset:0\n"
"s_waitcnt lgkmcnt(7)\n"
"v_smfmac_f32_16x16x64_bf16 v[52:55], v[252:255], a[208:215], v33\n"
"ds_read_b128 v[12:15], v30 offset:128\n"
"s_waitcnt lgkmcnt(7)\n"
"v_smfmac_f32_16x16x64_bf16 v[52:55], a[88:91], a[216:223], v33\n"
"ds_read_b128 v[248:251], v30 offset:256\n"
"s_waitcnt lgkmcnt(7)\n"
"v_smfmac_f32_16x16x64_bf16 v[52:55], a[92:95], a[224:231], v33\n"
"ds_read_b128 v[252:255], v30 offset:384\n"
"s_waitcnt lgkmcnt(7)\n"
"v_smfmac_f32_16x16x64_bf16 v[52:55], a[96:99], a[232:239], v33\n"
"ds_read_b128 a[88:91], v30 offset:528\n"
"s_waitcnt lgkmcnt(7)\n"
"v_smfmac_f32_16x16x64_bf16 v[52:55], a[100:103], a[240:247], v33\n"
"s_waitcnt lgkmcnt(6)\n"
"v_add_f32_dpp v43, v43, v43 row_ror:8 row_mask:0xf bank_mask:0xf\n"
"v_smfmac_f32_16x16x64_bf16 v[52:55], v[0:3], a[248:255], v33\n"
"s_waitcnt lgkmcnt(5)\n"
"v_add_f32_dpp v43, v43, v43 row_ror:4 row_mask:0xf bank_mask:0xf\n"
"v_smfmac_f32_16x16x64_bf16 v[52:55], v[4:7], v[56:63], v33\n"
"s_waitcnt lgkmcnt(4)\n"
"v_add_f32_dpp v43, v43, v43 row_ror:2 row_mask:0xf bank_mask:0xf\n"
"v_smfmac_f32_16x16x64_bf16 v[52:55], v[8:11], v[64:71], v33\n"
"s_waitcnt lgkmcnt(3)\n"
"v_add_f32_dpp v43, v43, v43 row_ror:1 row_mask:0xf bank_mask:0xf\n"
"v_smfmac_f32_16x16x64_bf16 v[52:55], v[12:15], v[72:79], v33\n"
"s_waitcnt lgkmcnt(2)\n"
"v_add_f32_dpp v43, v43, v43 row_bcast:15 row_mask:0xa bank_mask:0xf\n"
"v_smfmac_f32_16x16x64_bf16 v[52:55], v[248:251], v[80:87], v33\n"
"s_waitcnt lgkmcnt(1)\n"
"v_add_f32_dpp v43, v43, v43 row_bcast:31 row_mask:0xc bank_mask:0xf\n"
"v_smfmac_f32_16x16x64_bf16 v[52:55], v[252:255], v[88:95], v33\n"
"s_mov_b32 exec_lo, 0\n"
"s_and_b32 exec_hi, exec_hi, 0x80000000\n"
"ds_write_b64 v31, v[42:43] offset:4704\n"
"s_mov_b64 exec, -1\n"
"s_waitcnt lgkmcnt(1)\n"
"v_smfmac_f32_16x16x64_bf16 v[52:55], a[88:91], v[96:103], v33\n"
"s_mov_b64 vcc, 2\n"
"s_mov_b32 exec_hi, 0\n"
"s_nop 5\n"
"ds_write_b128 v34, v[52:55] offset:2304\n"
"s_mov_b64 exec, -1\n"
"s_waitcnt lgkmcnt(0)\n"
"s_barrier\n"
"ds_read_b64 v[16:17], v36 offset:4704\n"
"ds_read_b64 v[18:19], v35 offset:2304\n"
"s_waitcnt lgkmcnt(1)\n"
"v_max_f32_dpp v20, v16, v16 quad_perm:[1,0,3,2] row_mask:0xf bank_mask:0xf\n"
"s_nop 1\n"
"v_max_f32_dpp v20, v20, v20 quad_perm:[2,3,0,1] row_mask:0xf bank_mask:0xf\n"
"v_sub_f32 v16, v16, v20\n"
"v_exp_f32 v16, v16\n"
"s_waitcnt lgkmcnt(0)\n"
"v_mul_f32 v18, v16, v18\n"
"v_mul_f32 v19, v16, v19\n"
"v_mul_f32 v21, v16, v17\n"
"v_add_f32_dpp v18, v18, v18 quad_perm:[1,0,3,2] row_mask:0xf bank_mask:0xf\n"
"v_add_f32_dpp v19, v19, v19 quad_perm:[1,0,3,2] row_mask:0xf bank_mask:0xf\n"
"v_add_f32_dpp v21, v21, v21 quad_perm:[1,0,3,2] row_mask:0xf bank_mask:0xf\n"
"v_add_f32_dpp v18, v18, v18 quad_perm:[2,3,0,1] row_mask:0xf bank_mask:0xf\n"
"v_add_f32_dpp v19, v19, v19 quad_perm:[2,3,0,1] row_mask:0xf bank_mask:0xf\n"
"v_add_f32_dpp v21, v21, v21 quad_perm:[2,3,0,1] row_mask:0xf bank_mask:0xf\n"
"v_cndmask_b32 v18, v18, v20, vcc\n"
"v_cndmask_b32 v19, v19, v21, vcc\n"
"v_min_u32 v18, -2, v18\n"
"v_min_u32 v19, -2, v19\n"
"s_mov_b64 exec, s[34:35]\n"
"buffer_store_dwordx2 v[18:19], v37, s[52:55], s90 offen sc1\n"
"s_mov_b64 exec, -1\n"
"s_branch .Lk_end\n"
".Lk_chain:\n"
"v_mov_b32 v224, v47\n"
"s_and_b32 s13, s14, 7\n"
"s_lshr_b32 s34, s14, 3\n"
"s_mov_b32 s96, 131072\n"
"s_mul_i32 s88, s15, 640\n"
"v_lshl_add_u32 v226, v224, 4, s88\n"
"v_lshrrev_b32 v227, 1, v226\n"
"v_lshlrev_b32 v242, 4, v224\n"
"v_lshrrev_b32 v254, 4, v224\n"
"v_and_b32 v255, 15, v224\n"
"s_and_b32 s88, s15, 1\n"
"s_mul_i32 s89, s88, 262144\n"
"v_mul_u32_u24 v246, 131072, v254\n"
"v_mad_u32_u24 v246, v255, 16, v246\n"
"v_add_u32 v246, s89, v246\n"
"s_xor_b32 s88, s88, 1\n"
"s_mul_i32 s89, s88, 1048576\n"
"v_mul_u32_u24 v247, 32768, v254\n"
"v_mad_u32_u24 v247, v255, 16, v247\n"
"v_add_u32 v247, s89, v247\n"
"s_and_b32 s88, s15, 1\n"
"s_mul_i32 s89, s15, 320\n"
"s_lshl_b32 s93, s88, 6\n"
"s_add_u32 s93, s93, s89\n"
"s_xor_b32 s88, s88, 1\n"
"s_lshl_b32 s88, s88, 8\n"
"s_add_u32 s88, s88, s89\n"
"v_and_b32 v104, 7, v224\n"
"v_lshrrev_b32 v105, 3, v224\n"
"v_lshlrev_b32 v105, 2, v105\n"
"v_lshl_add_u32 v105, v104, 5, v105\n"
"v_lshlrev_b32 v104, 3, v104\n"
"v_add_u32 v105, s93, v105\n"
"v_add_u32 v104, s88, v104\n"
"v_cmp_gt_u32 vcc, 32, v224\n"
"s_nop 1\n"
"v_cndmask_b32 v104, v104, v105, vcc\n"
"v_cndmask_b32 v105, 4, 16, vcc\n"
"v_add_u32 v105, v104, v105\n"
"s_waitcnt lgkmcnt(0)\n"
"s_mov_b32 s74, s94\n"
"s_mov_b32 s52, s8\n"
"s_and_b32 s53, s9, 0xffff\n"
"s_mov_b32 s54, 2097152\n"
"s_mov_b32 s55, 0x20000\n"
"s_mov_b32 s40, s16\n"
"s_and_b32 s41, s17, 0xffff\n"
"s_mov_b32 s42, 2560\n"
"s_mov_b32 s43, 0x20000\n"
"buffer_load_dwordx2 v[248:249], off, s[52:55], 0 sc1\n"
"s_mov_b32 exec_lo, -1\n"
"s_mov_b32 exec_hi, 0xff\n"
"buffer_load_dword v96, v104, s[40:43], 0 offen offset:0\n"
"buffer_load_dword v97, v105, s[40:43], 0 offen offset:0\n"
"buffer_load_dword v98, v104, s[40:43], 0 offen offset:1280\n"
"buffer_load_dword v99, v105, s[40:43], 0 offen offset:1280\n"
"v_mov_b32 v100, -1\n"
"v_mov_b32 v101, -1\n"
"v_mov_b32 v102, -1\n"
"v_mov_b32 v103, -1\n"
"s_mul_i32 s70, s15, 20480\n"
"s_mul_i32 s88, s13, 2560\n"
"s_add_u32 s70, s70, s88\n"
"s_mul_i32 s88, s34, 640\n"
"s_add_u32 s70, s70, s88\n"
"s_add_u32 s70, s70, 614400\n"
"buffer_store_dwordx4 v[100:103], v242, s[52:55], s70 offen sc1\n"
"s_lshl_b32 s71, s15, 13\n"
"s_lshl_b32 s88, s14, 8\n"
"s_add_u32 s71, s71, s88\n"
"s_add_u32 s71, s71, 8192\n"
"s_mov_b64 exec, 0xffff\n"
"buffer_store_dwordx4 v[100:103], v242, s[52:55], s71 offen sc1\n"
"s_mul_i32 s72, s15, 69120\n"
"s_mul_i32 s88, s14, 2560\n"
"s_add_u32 s72, s72, s88\n"
"s_add_u32 s72, s72, 696320\n"
"s_cmp_ge_u32 s14, 27\n"
"s_cmov_b32 s72, 972800\n"
"s_mov_b64 exec, -1\n"
"v_mov_b32 v100, -1\n"
"v_mov_b32 v101, -1\n"
"v_mov_b32 v102, -1\n"
"v_mov_b32 v103, -1\n"
"buffer_store_dwordx4 v[100:103], v242, s[52:55], s72 offen sc1\n"
"buffer_store_dwordx4 v[100:103], v242, s[52:55], s72 offen offset:1024 sc1\n"
"s_mov_b32 exec_hi, 0\n"
"buffer_store_dwordx4 v[100:103], v242, s[52:55], s72 offen offset:2048 sc1\n"
"s_mov_b64 exec, -1\n"
"s_getpc_b64 s[44:45]\n"
".Lk_pc_144:\n"
"s_and_b32 s45, s45, 0xffff\n"
"s_mov_b32 s46, .Lk_end-.Lk_pc_144\n"
"s_mov_b32 s47, 0x20000\n"
"s_lshl_b32 s73, s15, 13\n"
"v_lshlrev_b32 v250, 7, v224\n"
"buffer_load_dword v251, v250, s[44:47], s73 offen\n"
"s_add_u32 s73, s73, 32768\n"
"s_waitcnt vmcnt(0)\n"
"buffer_load_dword v251, v250, s[44:47], s73 offen\n"
"s_mul_i32 s88, s15, 1310720\n"
"s_lshl_b32 s89, s14, 8\n"
"s_add_u32 s88, s88, s89\n"
"s_sub_u32 s89, 5242880, s88\n"
"s_add_u32 s36, s22, s88\n"
"s_addc_u32 s37, s23, 0\n"
"s_and_b32 s37, s37, 0xffff\n"
"s_mov_b32 s38, s89\n"
"s_mov_b32 s39, 0x20000\n"
"s_mov_b32 s90, 0\n"
"buffer_load_dwordx4 v[0:3], v246, s[36:39], s90 offen nt\n"
"s_mov_b32 s90, 8192\n"
"buffer_load_dwordx4 v[4:7], v246, s[36:39], s90 offen nt\n"
"s_mov_b32 s90, 65536\n"
"buffer_load_dwordx4 v[8:11], v246, s[36:39], s90 offen nt\n"
"s_mov_b32 s90, 73728\n"
"buffer_load_dwordx4 v[12:15], v246, s[36:39], s90 offen nt\n"
"s_mov_b32 s90, 524288\n"
"buffer_load_dwordx4 v[16:19], v246, s[36:39], s90 offen nt\n"
"s_mov_b32 s90, 532480\n"
"buffer_load_dwordx4 v[20:23], v246, s[36:39], s90 offen nt\n"
"s_mov_b32 s90, 589824\n"
"buffer_load_dwordx4 v[24:27], v246, s[36:39], s90 offen nt\n"
"s_mov_b32 s90, 598016\n"
"buffer_load_dwordx4 v[28:31], v246, s[36:39], s90 offen nt\n"
"s_mov_b32 s90, 16384\n"
"buffer_load_dwordx4 v[32:35], v246, s[36:39], s90 offen nt\n"
"s_mov_b32 s90, 24576\n"
"buffer_load_dwordx4 v[36:39], v246, s[36:39], s90 offen nt\n"
"s_mov_b32 s90, 81920\n"
"buffer_load_dwordx4 v[40:43], v246, s[36:39], s90 offen nt\n"
".Lk_wg_145:\n"
"s_memrealtime s[92:93]\n"
"s_waitcnt lgkmcnt(0)\n"
"s_sub_u32 s93, s92, s74\n"
"s_cmp_ge_u32 s93, 215\n"
"s_cbranch_scc0 .Lk_wg_145\n"
"s_mov_b32 s90, 90112\n"
"buffer_load_dwordx4 v[44:47], v246, s[36:39], s90 offen nt\n"
"s_mov_b32 s90, 540672\n"
"buffer_load_dwordx4 v[48:51], v246, s[36:39], s90 offen nt\n"
"s_mov_b32 s90, 548864\n"
"buffer_load_dwordx4 v[52:55], v246, s[36:39], s90 offen nt\n"
"s_mov_b32 s90, 606208\n"
"buffer_load_dwordx4 v[56:59], v246, s[36:39], s90 offen nt\n"
".Lk_wg_146:\n"
"s_memrealtime s[92:93]\n"
"s_waitcnt lgkmcnt(0)\n"
"s_sub_u32 s93, s92, s74\n"
"s_cmp_ge_u32 s93, 275\n"
"s_cbranch_scc0 .Lk_wg_146\n"
"s_mov_b32 s90, 614400\n"
"buffer_load_dwordx4 v[60:63], v246, s[36:39], s90 offen nt\n"
"s_mov_b32 s90, 32768\n"
"buffer_load_dwordx4 v[64:67], v246, s[36:39], s90 offen nt\n"
"s_mov_b32 s90, 40960\n"
"buffer_load_dwordx4 v[68:71], v246, s[36:39], s90 offen nt\n"
"s_mov_b32 s90, 98304\n"
"buffer_load_dwordx4 v[72:75], v246, s[36:39], s90 offen nt\n"
".Lk_wg_147:\n"
"s_memrealtime s[92:93]\n"
"s_waitcnt lgkmcnt(0)\n"
"s_sub_u32 s93, s92, s74\n"
"s_cmp_ge_u32 s93, 335\n"
"s_cbranch_scc0 .Lk_wg_147\n"
"s_mov_b32 s90, 106496\n"
"buffer_load_dwordx4 v[76:79], v246, s[36:39], s90 offen nt\n"
"s_mov_b32 s90, 557056\n"
"buffer_load_dwordx4 v[80:83], v246, s[36:39], s90 offen nt\n"
"s_mov_b32 s90, 565248\n"
"buffer_load_dwordx4 v[84:87], v246, s[36:39], s90 offen nt\n"
"s_mov_b32 s90, 622592\n"
"buffer_load_dwordx4 v[88:91], v246, s[36:39], s90 offen nt\n"
".Lk_wg_148:\n"
"s_memrealtime s[92:93]\n"
"s_waitcnt lgkmcnt(0)\n"
"s_sub_u32 s93, s92, s74\n"
"s_cmp_ge_u32 s93, 395\n"
"s_cbranch_scc0 .Lk_wg_148\n"
"s_mov_b32 s90, 630784\n"
"buffer_load_dwordx4 v[92:95], v246, s[36:39], s90 offen nt\n"
"s_mov_b32 s90, 49152\n"
"buffer_load_dwordx4 v[144:147], v246, s[36:39], s90 offen nt\n"
"s_mov_b32 s90, 57344\n"
"buffer_load_dwordx4 v[148:151], v246, s[36:39], s90 offen nt\n"
"s_mov_b32 s90, 114688\n"
"buffer_load_dwordx4 v[152:155], v246, s[36:39], s90 offen nt\n"
".Lk_wg_149:\n"
"s_memrealtime s[92:93]\n"
"s_waitcnt lgkmcnt(0)\n"
"s_sub_u32 s93, s92, s74\n"
"s_cmp_ge_u32 s93, 455\n"
"s_cbranch_scc0 .Lk_wg_149\n"
"s_mov_b32 s90, 122880\n"
"buffer_load_dwordx4 v[156:159], v246, s[36:39], s90 offen nt\n"
"s_mov_b32 s90, 573440\n"
"buffer_load_dwordx4 v[160:163], v246, s[36:39], s90 offen nt\n"
"s_mov_b32 s90, 581632\n"
"buffer_load_dwordx4 v[164:167], v246, s[36:39], s90 offen nt\n"
"s_mov_b32 s90, 638976\n"
"buffer_load_dwordx4 v[168:171], v246, s[36:39], s90 offen nt\n"
".Lk_wg_150:\n"
"s_memrealtime s[92:93]\n"
"s_waitcnt lgkmcnt(0)\n"
"s_sub_u32 s93, s92, s74\n"
"s_cmp_ge_u32 s93, 515\n"
"s_cbranch_scc0 .Lk_wg_150\n"
"s_mov_b32 s90, 647168\n"
"buffer_load_dwordx4 v[172:175], v246, s[36:39], s90 offen nt\n"
"s_mov_b32 s90, 0\n"
"buffer_load_dwordx4 v[176:179], v247, s[36:39], s90 offen nt\n"
"s_mov_b32 s90, 8192\n"
"buffer_load_dwordx4 v[180:183], v247, s[36:39], s90 offen nt\n"
"s_mov_b32 s90, 16384\n"
"buffer_load_dwordx4 v[184:187], v247, s[36:39], s90 offen nt\n"
".Lk_wg_151:\n"
"s_memrealtime s[92:93]\n"
"s_waitcnt lgkmcnt(0)\n"
"s_sub_u32 s93, s92, s74\n"
"s_cmp_ge_u32 s93, 575\n"
"s_cbranch_scc0 .Lk_wg_151\n"
"s_mov_b32 s90, 24576\n"
"buffer_load_dwordx4 v[188:191], v247, s[36:39], s90 offen nt\n"
"s_mov_b32 s90, 131072\n"
"buffer_load_dwordx4 v[192:195], v247, s[36:39], s90 offen nt\n"
"s_mov_b32 s90, 139264\n"
"buffer_load_dwordx4 v[196:199], v247, s[36:39], s90 offen nt\n"
"s_mov_b32 s90, 147456\n"
"buffer_load_dwordx4 v[200:203], v247, s[36:39], s90 offen nt\n"
".Lk_wg_152:\n"
"s_memrealtime s[92:93]\n"
"s_waitcnt lgkmcnt(0)\n"
"s_sub_u32 s93, s92, s74\n"
"s_cmp_ge_u32 s93, 635\n"
"s_cbranch_scc0 .Lk_wg_152\n"
"s_mov_b32 s90, 155648\n"
"buffer_load_dwordx4 v[220:223], v247, s[36:39], s90 offen nt\n"
"v_lshrrev_b32 v252, 4, v224\n"
"v_and_b32 v253, 15, v224\n"
"v_lshrrev_b32 v254, 5, v224\n"
"v_and_b32 v255, 31, v224\n"
"v_bfe_u32 v234, v224, 2, 1\n"
"s_lshl_b32 s88, s15, 8\n"
"v_lshl_add_u32 v233, v252, 4, s88\n"
"v_mul_u32_u24 v228, 1280, v234\n"
"v_lshl_add_u32 v234, v234, 7, v233\n"
"s_mul_i32 s89, s15, 320\n"
"v_lshl_add_u32 v228, v252, 4, v228\n"
"v_add_u32 v228, s89, v228\n"
"v_lshl_add_u32 v232, v255, 3, s88\n"
"v_lshl_add_u32 v232, v254, 2, v232\n"
"v_lshl_add_u32 v238, v224, 2, s88\n"
"v_lshlrev_b32 v237, 3, v224\n"
"s_mul_i32 s89, s15, 640\n"
"v_lshl_add_u32 v239, v253, 3, s89\n"
"v_add_u32 v235, 9728, v239\n"
"v_mul_u32_u24 v241, 69120, v252\n"
"v_add_u32 v239, v239, v241\n"
"v_lshlrev_b32 v241, 13, v254\n"
"v_bfe_u32 v229, v224, 2, 1\n"
"v_lshl_add_u32 v241, v229, 3, v241\n"
"v_bfe_u32 v229, v224, 3, 2\n"
"v_lshl_add_u32 v241, v229, 6, v241\n"
"v_bfe_u32 v229, v224, 1, 1\n"
"v_lshl_add_u32 v241, v229, 4, v241\n"
"v_and_b32 v229, 1, v224\n"
"v_lshl_add_u32 v241, v229, 5, v241\n"
"v_lshlrev_b32 v253, 3, v253\n"
"s_lshl_b32 s89, s15, 9\n"
"s_add_u32 s89, s89, 12288\n"
"v_lshl_add_u32 v236, v252, 8, v253\n"
"v_add_u32 v236, s89, v236\n"
"s_lshl_b32 s89, s15, 10\n"
"s_add_u32 s89, s89, 14336\n"
"v_lshlrev_b32 v253, 2, v253\n"
"v_lshl_add_u32 v240, v252, 9, v253\n"
"v_add_u32 v240, s89, v240\n"
"s_lshl_b32 s89, s15, 3\n"
"v_mov_b32 v244, s89\n"
"v_mov_b32 v243, 0\n"
"s_add_u32 s89, s89, s13\n"
"s_mul_i32 s89, s89, 4480\n"
"v_lshl_add_u32 v230, v255, 4, s89\n"
"v_mul_u32_u24 v229, 640, v254\n"
"v_add_u32 v229, v229, v230\n"
"v_and_b32 v231, 7, v224\n"
"v_xor_b32 v231, v231, v254\n"
"v_cmp_eq_u32 s[76:77], 7, v231\n"
"v_min_u32 v231, 6, v231\n"
"v_mul_u32_u24 v231, 640, v231\n"
"s_add_u32 s89, s89, 512\n"
"v_add_u32 v231, s89, v231\n"
"v_and_b32 v225, 1, v224\n"
"v_mul_u32_u24 v225, 0x5555, v225\n"
"v_add_u32 v225, 0x8888, v225\n"
"v_readfirstlane_b32 s88, v248\n"
"v_readfirstlane_b32 s89, v249\n"
"s_mul_hi_u32 s93, s88, 0x7f4a7c15\n"
"s_mul_i32 s86, s88, 0x7f4a7c15\n"
"s_mul_i32 s94, s88, 0x9e3779b9\n"
"s_add_u32 s93, s93, s94\n"
"s_mul_i32 s94, s89, 0x7f4a7c15\n"
"s_add_u32 s93, s93, s94\n"
"s_add_u32 s86, s86, 0xd192ed03\n"
"s_addc_u32 s87, s93, 0xd1b54a32\n"
"s_barrier\n"
"v_mov_b32 v248, s86\n"
"v_mov_b32 v249, s87\n"
"s_lshl_b32 s88, s14, 3\n"
"s_add_u32 s88, s88, 4096\n"
"s_mov_b64 exec, 1\n"
"buffer_store_dwordx2 v[248:249], off, s[52:55], s88 sc1\n"
"s_mov_b64 exec, -1\n"
"v_mov_b32 v254, 0\n"
"v_mov_b32 v255, 0\n"
"s_mov_b32 exec_lo, -1\n"
"s_mov_b32 exec_hi, 0xff\n"
"v_lshlrev_b32 v250, 16, v96\n"
"s_waitcnt vmcnt(41)\n"
"v_and_b32 v251, 0xffff0000, v96\n"
"v_lshlrev_b32 v252, 16, v97\n"
"v_and_b32 v253, 0xffff0000, v97\n"
"v_add_f32 v254, v250, v251\n"
"v_mul_f32 v255, v250, v250\n"
"v_add_f32 v104, v252, v253\n"
"v_fmac_f32 v255, v251, v251\n"
"v_add_f32 v254, v254, v104\n"
"v_fmac_f32 v255, v252, v252\n"
"v_fmac_f32 v255, v253, v253\n"
"ds_write_b128 v226, v[250:253] offset:0\n"
"s_mov_b64 exec, -1\n"
"v_add_f32_dpp v254, v254, v254 row_ror:8 row_mask:0xf bank_mask:0xf\n"
"v_add_f32_dpp v255, v255, v255 row_ror:8 row_mask:0xf bank_mask:0xf\n"
"s_nop 0\n"
"v_add_f32_dpp v254, v254, v254 row_ror:4 row_mask:0xf bank_mask:0xf\n"
"v_add_f32_dpp v255, v255, v255 row_ror:4 row_mask:0xf bank_mask:0xf\n"
"s_nop 0\n"
"v_add_f32_dpp v254, v254, v254 row_ror:2 row_mask:0xf bank_mask:0xf\n"
"v_add_f32_dpp v255, v255, v255 row_ror:2 row_mask:0xf bank_mask:0xf\n"
"s_nop 0\n"
"v_add_f32_dpp v254, v254, v254 row_ror:1 row_mask:0xf bank_mask:0xf\n"
"v_add_f32_dpp v255, v255, v255 row_ror:1 row_mask:0xf bank_mask:0xf\n"
"s_nop 0\n"
"v_add_f32_dpp v254, v254, v254 row_bcast:15 row_mask:0xa bank_mask:0xf\n"
"v_add_f32_dpp v255, v255, v255 row_bcast:15 row_mask:0xa bank_mask:0xf\n"
"s_nop 0\n"
"v_add_f32_dpp v254, v254, v254 row_bcast:31 row_mask:0xc bank_mask:0xf\n"
"v_add_f32_dpp v255, v255, v255 row_bcast:31 row_mask:0xc bank_mask:0xf\n"
"s_mov_b32 exec_lo, 0\n"
"s_mov_b32 exec_hi, 0x80000000\n"
"ds_write_b64 v244, v[254:255] offset:18432\n"
"s_mov_b32 exec_lo, -1\n"
"s_mov_b32 exec_hi, 0xff\n"
"s_waitcnt lgkmcnt(0)\n"
"s_barrier\n"
"ds_read_b128 v[104:107], v243 offset:18432\n"
"ds_read_b128 v[108:111], v243 offset:18448\n"
"s_waitcnt lgkmcnt(1)\n"
"v_pk_add_f32 v[104:105], v[104:105], v[106:107]\n"
"s_waitcnt lgkmcnt(0)\n"
"v_pk_add_f32 v[108:109], v[108:109], v[110:111]\n"
"v_pk_add_f32 v[104:105], v[104:105], v[108:109]\n"
"v_mul_f32 v104, 0x3acccccd, v104\n"
"v_mul_f32 v105, 0x3acccccd, v105\n"
"v_cvt_pk_bf16_f32 v106, 0, v104\n"
"v_fma_f32 v105, -v104, v104, v105\n"
"v_sub_f32 v108, v250, v106\n"
"v_max_f32 v105, 0, v105\n"
"v_sub_f32 v109, v251, v106\n"
"v_cvt_pk_bf16_f32 v105, 0, v105\n"
"v_sub_f32 v110, v252, v106\n"
"v_add_f32 v105, 0x3727c5ac, v105\n"
"v_sub_f32 v111, v253, v106\n"
"v_cvt_pk_bf16_f32 v105, 0, v105\n"
"v_cvt_pk_bf16_f32 v108, 0, v108\n"
"v_sqrt_f32 v105, v105\n"
"v_cvt_pk_bf16_f32 v109, 0, v109\n"
"v_cvt_pk_bf16_f32 v105, 0, v105\n"
"v_cvt_pk_bf16_f32 v110, 0, v110\n"
"v_rcp_f32 v105, v105\n"
"v_cvt_pk_bf16_f32 v111, 0, v111\n"
"v_pk_mul_f32 v[108:109], v[108:109], v[104:105] op_sel:[0,1] op_sel_hi:[1,1]\n"
"v_pk_mul_f32 v[110:111], v[110:111], v[104:105] op_sel:[0,1] op_sel_hi:[1,1]\n"
"v_cvt_pk_bf16_f32 v108, v108, v109\n"
"v_cvt_pk_bf16_f32 v109, v110, v111\n"
"ds_write_b64 v227, v[108:109] offset:5120\n"
"s_mov_b64 exec, -1\n"
"v_mov_b32 v254, 0\n"
"v_mov_b32 v255, 0\n"
"s_mov_b32 exec_lo, -1\n"
"s_mov_b32 exec_hi, 0xff\n"
"v_lshlrev_b32 v250, 16, v98\n"
"v_and_b32 v251, 0xffff0000, v98\n"
"v_lshlrev_b32 v252, 16, v99\n"
"v_and_b32 v253, 0xffff0000, v99\n"
"v_add_f32 v254, v250, v251\n"
"v_mul_f32 v255, v250, v250\n"
"v_add_f32 v104, v252, v253\n"
"v_fmac_f32 v255, v251, v251\n"
"v_add_f32 v254, v254, v104\n"
"v_fmac_f32 v255, v252, v252\n"
"v_fmac_f32 v255, v253, v253\n"
"ds_write_b128 v226, v[250:253] offset:2560\n"
"s_mov_b64 exec, -1\n"
"v_add_f32_dpp v254, v254, v254 row_ror:8 row_mask:0xf bank_mask:0xf\n"
"v_add_f32_dpp v255, v255, v255 row_ror:8 row_mask:0xf bank_mask:0xf\n"
"s_nop 0\n"
"v_add_f32_dpp v254, v254, v254 row_ror:4 row_mask:0xf bank_mask:0xf\n"
"v_add_f32_dpp v255, v255, v255 row_ror:4 row_mask:0xf bank_mask:0xf\n"
"s_nop 0\n"
"v_add_f32_dpp v254, v254, v254 row_ror:2 row_mask:0xf bank_mask:0xf\n"
"v_add_f32_dpp v255, v255, v255 row_ror:2 row_mask:0xf bank_mask:0xf\n"
"s_nop 0\n"
"v_add_f32_dpp v254, v254, v254 row_ror:1 row_mask:0xf bank_mask:0xf\n"
"v_add_f32_dpp v255, v255, v255 row_ror:1 row_mask:0xf bank_mask:0xf\n"
"s_nop 0\n"
"v_add_f32_dpp v254, v254, v254 row_bcast:15 row_mask:0xa bank_mask:0xf\n"
"v_add_f32_dpp v255, v255, v255 row_bcast:15 row_mask:0xa bank_mask:0xf\n"
"s_nop 0\n"
"v_add_f32_dpp v254, v254, v254 row_bcast:31 row_mask:0xc bank_mask:0xf\n"
"v_add_f32_dpp v255, v255, v255 row_bcast:31 row_mask:0xc bank_mask:0xf\n"
"s_mov_b32 exec_lo, 0\n"
"s_mov_b32 exec_hi, 0x80000000\n"
"ds_write_b64 v244, v[254:255] offset:18464\n"
"s_mov_b32 exec_lo, -1\n"
"s_mov_b32 exec_hi, 0xff\n"
"s_waitcnt lgkmcnt(0)\n"
"s_barrier\n"
"ds_read_b128 v[104:107], v243 offset:18464\n"
"ds_read_b128 v[108:111], v243 offset:18480\n"
"s_waitcnt lgkmcnt(1)\n"
"v_pk_add_f32 v[104:105], v[104:105], v[106:107]\n"
"s_waitcnt lgkmcnt(0)\n"
"v_pk_add_f32 v[108:109], v[108:109], v[110:111]\n"
"v_pk_add_f32 v[104:105], v[104:105], v[108:109]\n"
"v_mul_f32 v104, 0x3acccccd, v104\n"
"v_mul_f32 v105, 0x3acccccd, v105\n"
"v_cvt_pk_bf16_f32 v106, 0, v104\n"
"v_fma_f32 v105, -v104, v104, v105\n"
"v_sub_f32 v108, v250, v106\n"
"v_max_f32 v105, 0, v105\n"
"v_sub_f32 v109, v251, v106\n"
"v_cvt_pk_bf16_f32 v105, 0, v105\n"
"v_sub_f32 v110, v252, v106\n"
"v_add_f32 v105, 0x3727c5ac, v105\n"
"v_sub_f32 v111, v253, v106\n"
"v_cvt_pk_bf16_f32 v105, 0, v105\n"
"v_cvt_pk_bf16_f32 v108, 0, v108\n"
"v_sqrt_f32 v105, v105\n"
"v_cvt_pk_bf16_f32 v109, 0, v109\n"
"v_cvt_pk_bf16_f32 v105, 0, v105\n"
"v_cvt_pk_bf16_f32 v110, 0, v110\n"
"v_rcp_f32 v105, v105\n"
"v_cvt_pk_bf16_f32 v111, 0, v111\n"
"v_pk_mul_f32 v[108:109], v[108:109], v[104:105] op_sel:[0,1] op_sel_hi:[1,1]\n"
"v_pk_mul_f32 v[110:111], v[110:111], v[104:105] op_sel:[0,1] op_sel_hi:[1,1]\n"
"v_cvt_pk_bf16_f32 v108, v108, v109\n"
"v_cvt_pk_bf16_f32 v109, v110, v111\n"
"ds_write_b64 v227, v[108:109] offset:6400\n"
"s_mov_b64 exec, -1\n"
"s_lshl_b32 s88, s15, 15\n"
"s_add_u32 s88, s88, 32768\n"
"v_add_u32 v242, s88, v242\n"
"s_waitcnt vmcnt(40)\n"
"v_accvgpr_write_b32 a0, v0\n"
"v_accvgpr_write_b32 a8, v1\n"
"v_accvgpr_write_b32 a16, v2\n"
"v_accvgpr_write_b32 a24, v3\n"
"s_waitcnt vmcnt(39)\n"
"v_accvgpr_write_b32 a1, v4\n"
"v_accvgpr_write_b32 a9, v5\n"
"v_accvgpr_write_b32 a17, v6\n"
"v_accvgpr_write_b32 a25, v7\n"
"s_waitcnt vmcnt(38)\n"
"v_accvgpr_write_b32 a2, v8\n"
"v_accvgpr_write_b32 a10, v9\n"
"v_accvgpr_write_b32 a18, v10\n"
"v_accvgpr_write_b32 a26, v11\n"
"s_waitcnt vmcnt(37)\n"
"v_accvgpr_write_b32 a3, v12\n"
"v_accvgpr_write_b32 a11, v13\n"
"v_accvgpr_write_b32 a19, v14\n"
"v_accvgpr_write_b32 a27, v15\n"
"s_waitcnt vmcnt(36)\n"
"v_accvgpr_write_b32 a4, v16\n"
"v_accvgpr_write_b32 a12, v17\n"
"v_accvgpr_write_b32 a20, v18\n"
"v_accvgpr_write_b32 a28, v19\n"
"s_waitcnt vmcnt(35)\n"
"v_accvgpr_write_b32 a5, v20\n"
"v_accvgpr_write_b32 a13, v21\n"
"v_accvgpr_write_b32 a21, v22\n"
"v_accvgpr_write_b32 a29, v23\n"
"s_waitcnt vmcnt(34)\n"
"v_accvgpr_write_b32 a6, v24\n"
"v_accvgpr_write_b32 a14, v25\n"
"v_accvgpr_write_b32 a22, v26\n"
"v_accvgpr_write_b32 a30, v27\n"
"s_waitcnt vmcnt(33)\n"
"v_accvgpr_write_b32 a7, v28\n"
"v_accvgpr_write_b32 a15, v29\n"
"v_accvgpr_write_b32 a23, v30\n"
"v_accvgpr_write_b32 a31, v31\n"
"s_waitcnt vmcnt(32)\n"
"v_accvgpr_write_b32 a32, v32\n"
"v_accvgpr_write_b32 a40, v33\n"
"v_accvgpr_write_b32 a48, v34\n"
"v_accvgpr_write_b32 a56, v35\n"
"s_waitcnt vmcnt(31)\n"
"v_accvgpr_write_b32 a33, v36\n"
"v_accvgpr_write_b32 a41, v37\n"
"v_accvgpr_write_b32 a49, v38\n"
"v_accvgpr_write_b32 a57, v39\n"
"s_waitcnt vmcnt(30)\n"
"v_accvgpr_write_b32 a34, v40\n"
"v_accvgpr_write_b32 a42, v41\n"
"v_accvgpr_write_b32 a50, v42\n"
"v_accvgpr_write_b32 a58, v43\n"
"s_waitcnt vmcnt(29)\n"
"v_accvgpr_write_b32 a35, v44\n"
"v_accvgpr_write_b32 a43, v45\n"
"v_accvgpr_write_b32 a51, v46\n"
"v_accvgpr_write_b32 a59, v47\n"
"s_waitcnt vmcnt(28)\n"
"v_accvgpr_write_b32 a36, v48\n"
"v_accvgpr_write_b32 a44, v49\n"
"v_accvgpr_write_b32 a52, v50\n"
"v_accvgpr_write_b32 a60, v51\n"
"s_waitcnt vmcnt(27)\n"
"v_accvgpr_write_b32 a37, v52\n"
"v_accvgpr_write_b32 a45, v53\n"
"v_accvgpr_write_b32 a53, v54\n"
"v_accvgpr_write_b32 a61, v55\n"
"s_waitcnt vmcnt(26)\n"
"v_accvgpr_write_b32 a38, v56\n"
"v_accvgpr_write_b32 a46, v57\n"
"v_accvgpr_write_b32 a54, v58\n"
"v_accvgpr_write_b32 a62, v59\n"
"s_waitcnt vmcnt(25)\n"
"v_accvgpr_write_b32 a39, v60\n"
"v_accvgpr_write_b32 a47, v61\n"
"v_accvgpr_write_b32 a55, v62\n"
"v_accvgpr_write_b32 a63, v63\n"
"s_waitcnt vmcnt(24)\n"
"v_accvgpr_write_b32 a64, v64\n"
"v_accvgpr_write_b32 a72, v65\n"
"v_accvgpr_write_b32 a80, v66\n"
"v_accvgpr_write_b32 a88, v67\n"
"s_waitcnt vmcnt(23)\n"
"v_accvgpr_write_b32 a65, v68\n"
"v_accvgpr_write_b32 a73, v69\n"
"v_accvgpr_write_b32 a81, v70\n"
"v_accvgpr_write_b32 a89, v71\n"
"s_waitcnt vmcnt(22)\n"
"v_accvgpr_write_b32 a66, v72\n"
"v_accvgpr_write_b32 a74, v73\n"
"v_accvgpr_write_b32 a82, v74\n"
"v_accvgpr_write_b32 a90, v75\n"
"s_waitcnt vmcnt(21)\n"
"v_accvgpr_write_b32 a67, v76\n"
"v_accvgpr_write_b32 a75, v77\n"
"v_accvgpr_write_b32 a83, v78\n"
"v_accvgpr_write_b32 a91, v79\n"
"s_waitcnt vmcnt(20)\n"
"v_accvgpr_write_b32 a68, v80\n"
"v_accvgpr_write_b32 a76, v81\n"
"v_accvgpr_write_b32 a84, v82\n"
"v_accvgpr_write_b32 a92, v83\n"
"s_waitcnt vmcnt(19)\n"
"v_accvgpr_write_b32 a69, v84\n"
"v_accvgpr_write_b32 a77, v85\n"
"v_accvgpr_write_b32 a85, v86\n"
"v_accvgpr_write_b32 a93, v87\n"
"s_waitcnt vmcnt(18)\n"
"v_accvgpr_write_b32 a70, v88\n"
"v_accvgpr_write_b32 a78, v89\n"
"v_accvgpr_write_b32 a86, v90\n"
"v_accvgpr_write_b32 a94, v91\n"
"s_waitcnt vmcnt(17)\n"
"v_accvgpr_write_b32 a71, v92\n"
"v_accvgpr_write_b32 a79, v93\n"
"v_accvgpr_write_b32 a87, v94\n"
"v_accvgpr_write_b32 a95, v95\n"
"s_waitcnt vmcnt(16)\n"
"v_accvgpr_write_b32 a96, v144\n"
"v_accvgpr_write_b32 a104, v145\n"
"v_accvgpr_write_b32 a112, v146\n"
"v_accvgpr_write_b32 a120, v147\n"
"s_waitcnt vmcnt(15)\n"
"v_accvgpr_write_b32 a97, v148\n"
"v_accvgpr_write_b32 a105, v149\n"
"v_accvgpr_write_b32 a113, v150\n"
"v_accvgpr_write_b32 a121, v151\n"
"s_waitcnt vmcnt(14)\n"
"v_accvgpr_write_b32 a98, v152\n"
"v_accvgpr_write_b32 a106, v153\n"
"v_accvgpr_write_b32 a114, v154\n"
"v_accvgpr_write_b32 a122, v155\n"
"s_waitcnt vmcnt(13)\n"
"v_accvgpr_write_b32 a99, v156\n"
"v_accvgpr_write_b32 a107, v157\n"
"v_accvgpr_write_b32 a115, v158\n"
"v_accvgpr_write_b32 a123, v159\n"
"s_waitcnt vmcnt(12)\n"
"v_accvgpr_write_b32 a100, v160\n"
"v_accvgpr_write_b32 a108, v161\n"
"v_accvgpr_write_b32 a116, v162\n"
"v_accvgpr_write_b32 a124, v163\n"
"s_waitcnt vmcnt(11)\n"
"v_accvgpr_write_b32 a101, v164\n"
"v_accvgpr_write_b32 a109, v165\n"
"v_accvgpr_write_b32 a117, v166\n"
"v_accvgpr_write_b32 a125, v167\n"
"s_waitcnt vmcnt(10)\n"
"v_accvgpr_write_b32 a102, v168\n"
"v_accvgpr_write_b32 a110, v169\n"
"v_accvgpr_write_b32 a118, v170\n"
"v_accvgpr_write_b32 a126, v171\n"
"s_waitcnt vmcnt(9)\n"
"v_accvgpr_write_b32 a103, v172\n"
"v_accvgpr_write_b32 a111, v173\n"
"v_accvgpr_write_b32 a119, v174\n"
"v_accvgpr_write_b32 a127, v175\n"
"s_waitcnt vmcnt(8)\n"
"v_accvgpr_write_b32 a128, v176\n"
"v_accvgpr_write_b32 a136, v177\n"
"v_accvgpr_write_b32 a144, v178\n"
"v_accvgpr_write_b32 a152, v179\n"
"s_waitcnt vmcnt(7)\n"
"v_accvgpr_write_b32 a129, v180\n"
"v_accvgpr_write_b32 a137, v181\n"
"v_accvgpr_write_b32 a145, v182\n"
"v_accvgpr_write_b32 a153, v183\n"
"s_waitcnt vmcnt(6)\n"
"v_accvgpr_write_b32 a130, v184\n"
"v_accvgpr_write_b32 a138, v185\n"
"v_accvgpr_write_b32 a146, v186\n"
"v_accvgpr_write_b32 a154, v187\n"
"s_waitcnt vmcnt(5)\n"
"v_accvgpr_write_b32 a131, v188\n"
"v_accvgpr_write_b32 a139, v189\n"
"v_accvgpr_write_b32 a147, v190\n"
"v_accvgpr_write_b32 a155, v191\n"
"s_waitcnt vmcnt(4)\n"
"v_accvgpr_write_b32 a132, v192\n"
"v_accvgpr_write_b32 a140, v193\n"
"v_accvgpr_write_b32 a148, v194\n"
"v_accvgpr_write_b32 a156, v195\n"
"s_waitcnt vmcnt(3)\n"
"v_accvgpr_write_b32 a133, v196\n"
"v_accvgpr_write_b32 a141, v197\n"
"v_accvgpr_write_b32 a149, v198\n"
"v_accvgpr_write_b32 a157, v199\n"
"s_waitcnt vmcnt(2)\n"
"v_accvgpr_write_b32 a134, v200\n"
"v_accvgpr_write_b32 a142, v201\n"
"v_accvgpr_write_b32 a150, v202\n"
"v_accvgpr_write_b32 a158, v203\n"
"s_waitcnt vmcnt(1)\n"
"v_accvgpr_write_b32 a135, v220\n"
"v_accvgpr_write_b32 a143, v221\n"
"v_accvgpr_write_b32 a151, v222\n"
"v_accvgpr_write_b32 a159, v223\n"
"s_lshl_b32 s88, s15, 3\n"
"s_add_u32 s88, s88, s13\n"
"s_mul_i32 s88, s88, 163840\n"
"s_mul_i32 s89, s34, 320\n"
"s_add_u32 s88, s88, s89\n"
"s_sub_u32 s89, 5242880, s88\n"
"s_add_u32 s36, s24, s88\n"
"s_addc_u32 s37, s25, 0\n"
"s_and_b32 s37, s37, 0xffff\n"
"s_mov_b32 s38, s89\n"
"s_mov_b32 s39, 0x20000\n"
"v_lshrrev_b32 v254, 4, v224\n"
"v_and_b32 v255, 15, v224\n"
"s_and_b32 s88, s34, 1\n"
"s_mul_i32 s89, s88, 64\n"
"v_mul_u32_u24 v246, 5120, v254\n"
"v_mad_u32_u24 v246, v255, 16, v246\n"
"v_add_u32 v246, s89, v246\n"
"s_xor_b32 s88, s88, 1\n"
"s_mul_i32 s89, s88, 256\n"
"v_mul_u32_u24 v247, 5120, v254\n"
"v_mad_u32_u24 v247, v255, 4, v247\n"
"v_add_u32 v247, s89, v247\n"
"s_mov_b32 s90, 0\n"
"buffer_load_dwordx4 v[144:147], v246, s[36:39], s90 offen nt\n"
"buffer_load_dword v192, v247, s[36:39], s90 offen nt\n"
"s_mov_b32 s90, 1280\n"
"buffer_load_dwordx4 v[148:151], v246, s[36:39], s90 offen nt\n"
"buffer_load_dword v193, v247, s[36:39], s90 offen nt\n"
"s_mov_b32 s90, 2560\n"
"buffer_load_dwordx4 v[152:155], v246, s[36:39], s90 offen nt\n"
"buffer_load_dword v194, v247, s[36:39], s90 offen nt\n"
"s_waitcnt lgkmcnt(0)\n"
".Lk_wg_153:\n"
"s_memrealtime s[92:93]\n"
"s_waitcnt lgkmcnt(0)\n"
"s_sub_u32 s93, s92, s74\n"
"s_cmp_ge_u32 s93, 706\n"
"s_cbranch_scc0 .Lk_wg_153\n"
"s_mov_b32 s90, 3840\n"
"buffer_load_dwordx4 v[156:159], v246, s[36:39], s90 offen nt\n"
"buffer_load_dword v195, v247, s[36:39], s90 offen nt\n"
"ds_read_b128 v[104:107], v228 offset:5120\n"
"ds_read_b128 v[108:111], v228 offset:5184\n"
"ds_read_b128 v[112:115], v228 offset:5248\n"
"ds_read_b128 v[116:119], v228 offset:5312\n"
"ds_read_b128 v[120:123], v228 offset:5376\n"
"s_lshl_b32 s91, s14, 8\n"
"s_add_u32 s91, s91, 8192\n"
"v_mov_b32 v204, 0\n"
"v_mov_b32 v205, 0\n"
"v_mov_b32 v208, 0\n"
"v_mov_b32 v209, 0\n"
"v_mov_b32 v212, 0\n"
"v_mov_b32 v213, 0\n"
"v_mov_b32 v216, 0\n"
"v_mov_b32 v217, 0\n"
"s_waitcnt lgkmcnt(4)\n"
"v_smfmac_f32_16x16x64_bf16 v[204:207], v[104:107], a[0:7], v225\n"
"v_smfmac_f32_16x16x64_bf16 v[208:211], v[104:107], a[8:15], v225\n"
"v_smfmac_f32_16x16x64_bf16 v[212:215], v[104:107], a[16:23], v225\n"
"v_smfmac_f32_16x16x64_bf16 v[216:219], v[104:107], a[24:31], v225\n"
"s_waitcnt lgkmcnt(3)\n"
"v_smfmac_f32_16x16x64_bf16 v[204:207], v[108:111], a[32:39], v225\n"
"v_smfmac_f32_16x16x64_bf16 v[208:211], v[108:111], a[40:47], v225\n"
"v_smfmac_f32_16x16x64_bf16 v[212:215], v[108:111], a[48:55], v225\n"
"v_smfmac_f32_16x16x64_bf16 v[216:219], v[108:111], a[56:63], v225\n"
"s_waitcnt lgkmcnt(2)\n"
"v_smfmac_f32_16x16x64_bf16 v[204:207], v[112:115], a[64:71], v225\n"
"v_smfmac_f32_16x16x64_bf16 v[208:211], v[112:115], a[72:79], v225\n"
"v_smfmac_f32_16x16x64_bf16 v[212:215], v[112:115], a[80:87], v225\n"
"v_smfmac_f32_16x16x64_bf16 v[216:219], v[112:115], a[88:95], v225\n"
"s_waitcnt lgkmcnt(1)\n"
"v_smfmac_f32_16x16x64_bf16 v[204:207], v[116:119], a[96:103], v225\n"
"v_smfmac_f32_16x16x64_bf16 v[208:211], v[116:119], a[104:111], v225\n"
"v_smfmac_f32_16x16x64_bf16 v[212:215], v[116:119], a[112:119], v225\n"
"v_smfmac_f32_16x16x64_bf16 v[216:219], v[116:119], a[120:127], v225\n"
"s_waitcnt lgkmcnt(0)\n"
"v_smfmac_f32_16x16x64_bf16 v[204:207], v[120:123], a[128:135], v225\n"
"v_smfmac_f32_16x16x64_bf16 v[208:211], v[120:123], a[136:143], v225\n"
"v_smfmac_f32_16x16x64_bf16 v[212:215], v[120:123], a[144:151], v225\n"
"v_smfmac_f32_16x16x64_bf16 v[216:219], v[120:123], a[152:159], v225\n"
"s_mov_b32 exec_lo, -1\n"
"s_mov_b32 exec_hi, 0\n"
"s_nop 3\n"
"ds_write2_b64 v240, v[204:205], v[208:209] offset0:0 offset1:2\n"
"s_nop 0\n"
"ds_write2_b64 v240, v[212:213], v[216:217] offset0:1 offset1:3\n"
"s_mov_b64 exec, -1\n"
"s_waitcnt lgkmcnt(0)\n"
"s_barrier\n"
"s_cmp_lg_u32 s15, 0\n"
"s_cbranch_scc1 .Lk_qs_154\n"
"ds_read_b128 v[128:131], v226 offset:14336\n"
"ds_read_b128 v[132:135], v226 offset:15360\n"
"ds_read_b128 v[136:139], v226 offset:16384\n"
"ds_read_b128 v[140:143], v226 offset:17408\n"
"s_waitcnt lgkmcnt(2)\n"
"v_pk_add_f32 v[128:129], v[128:129], v[132:133]\n"
"v_pk_add_f32 v[130:131], v[130:131], v[134:135]\n"
"s_waitcnt lgkmcnt(0)\n"
"v_pk_add_f32 v[136:137], v[136:137], v[140:141]\n"
"v_pk_add_f32 v[138:139], v[138:139], v[142:143]\n"
"v_pk_add_f32 v[128:129], v[128:129], v[136:137]\n"
"v_pk_add_f32 v[130:131], v[130:131], v[138:139]\n"
"v_cvt_pk_bf16_f32 v128, v128, v129\n"
"v_cvt_pk_bf16_f32 v129, v130, v131\n"
"v_min_u32 v128, -2, v128\n"
"v_min_u32 v129, -2, v129\n"
"s_mov_b32 exec_lo, -1\n"
"s_mov_b32 exec_hi, -1\n"
"buffer_store_dwordx2 v[128:129], v241, s[52:55], s91 offen sc1\n"
"s_mov_b64 exec, -1\n"
"s_nop 1\n"
".Lk_qs_154:\n"
"s_mov_b32 s90, 20480\n"
"buffer_load_dwordx4 v[160:163], v246, s[36:39], s90 offen nt\n"
"buffer_load_dword v196, v247, s[36:39], s90 offen nt\n"
"s_mov_b32 s90, 21760\n"
"buffer_load_dwordx4 v[164:167], v246, s[36:39], s90 offen nt\n"
"buffer_load_dword v197, v247, s[36:39], s90 offen nt\n"
"s_mov_b32 s90, 23040\n"
"buffer_load_dwordx4 v[168:171], v246, s[36:39], s90 offen nt\n"
"buffer_load_dword v198, v247, s[36:39], s90 offen nt\n"
".Lk_wg_155:\n"
"s_memrealtime s[92:93]\n"
"s_waitcnt lgkmcnt(0)\n"
"s_sub_u32 s93, s92, s74\n"
"s_cmp_ge_u32 s93, 781\n"
"s_cbranch_scc0 .Lk_wg_155\n"
"s_mov_b32 s90, 24320\n"
"buffer_load_dwordx4 v[172:175], v246, s[36:39], s90 offen nt\n"
"buffer_load_dword v199, v247, s[36:39], s90 offen nt\n"
"s_mov_b32 s90, 40960\n"
"buffer_load_dwordx4 v[176:179], v246, s[36:39], s90 offen nt\n"
"buffer_load_dword v200, v247, s[36:39], s90 offen nt\n"
"s_mov_b32 s90, 42240\n"
"buffer_load_dwordx4 v[180:183], v246, s[36:39], s90 offen nt\n"
"buffer_load_dword v201, v247, s[36:39], s90 offen nt\n"
"s_mov_b32 s90, 43520\n"
"buffer_load_dwordx4 v[184:187], v246, s[36:39], s90 offen nt\n"
"buffer_load_dword v202, v247, s[36:39], s90 offen nt\n"
".Lk_wg_156:\n"
"s_memrealtime s[92:93]\n"
"s_waitcnt lgkmcnt(0)\n"
"s_sub_u32 s93, s92, s74\n"
"s_cmp_ge_u32 s93, 856\n"
"s_cbranch_scc0 .Lk_wg_156\n"
"s_mov_b32 s90, 44800\n"
"buffer_load_dwordx4 v[188:191], v246, s[36:39], s90 offen nt\n"
"buffer_load_dword v203, v247, s[36:39], s90 offen nt\n"
"s_mov_b32 s90, 61440\n"
"buffer_load_dwordx4 v[96:99], v246, s[36:39], s90 offen nt\n"
"buffer_load_dword v204, v247, s[36:39], s90 offen nt\n"
"s_mov_b32 s90, 62720\n"
"buffer_load_dwordx4 v[100:103], v246, s[36:39], s90 offen nt\n"
"buffer_load_dword v205, v247, s[36:39], s90 offen nt\n"
"s_mov_b32 s90, 64000\n"
"buffer_load_dwordx4 v[104:107], v246, s[36:39], s90 offen nt\n"
"buffer_load_dword v206, v247, s[36:39], s90 offen nt\n"
".Lk_wg_157:\n"
"s_memrealtime s[92:93]\n"
"s_waitcnt lgkmcnt(0)\n"
"s_sub_u32 s93, s92, s74\n"
"s_cmp_ge_u32 s93, 931\n"
"s_cbranch_scc0 .Lk_wg_157\n"
"s_mov_b32 s90, 65280\n"
"buffer_load_dwordx4 v[108:111], v246, s[36:39], s90 offen nt\n"
"buffer_load_dword v207, v247, s[36:39], s90 offen nt\n"
"s_mov_b32 s90, 81920\n"
"buffer_load_dwordx4 v[112:115], v246, s[36:39], s90 offen nt\n"
"buffer_load_dword v208, v247, s[36:39], s90 offen nt\n"
"s_mov_b32 s90, 83200\n"
"buffer_load_dwordx4 v[116:119], v246, s[36:39], s90 offen nt\n"
"buffer_load_dword v209, v247, s[36:39], s90 offen nt\n"
"s_mov_b32 s90, 84480\n"
"buffer_load_dwordx4 v[120:123], v246, s[36:39], s90 offen nt\n"
"buffer_load_dword v210, v247, s[36:39], s90 offen nt\n"
".Lk_wg_158:\n"
"s_memrealtime s[92:93]\n"
"s_waitcnt lgkmcnt(0)\n"
"s_sub_u32 s93, s92, s74\n"
"s_cmp_ge_u32 s93, 1006\n"
"s_cbranch_scc0 .Lk_wg_158\n"
"s_mov_b32 s90, 85760\n"
"buffer_load_dwordx4 v[124:127], v246, s[36:39], s90 offen nt\n"
"buffer_load_dword v211, v247, s[36:39], s90 offen nt\n"
"s_mov_b32 s90, 102400\n"
"buffer_load_dwordx4 v[128:131], v246, s[36:39], s90 offen nt\n"
"buffer_load_dword v212, v247, s[36:39], s90 offen nt\n"
"s_mov_b32 s90, 103680\n"
"buffer_load_dwordx4 v[132:135], v246, s[36:39], s90 offen nt\n"
"buffer_load_dword v213, v247, s[36:39], s90 offen nt\n"
"s_mov_b32 s90, 104960\n"
"buffer_load_dwordx4 v[136:139], v246, s[36:39], s90 offen nt\n"
"buffer_load_dword v214, v247, s[36:39], s90 offen nt\n"
".Lk_wg_159:\n"
"s_memrealtime s[92:93]\n"
"s_waitcnt lgkmcnt(0)\n"
"s_sub_u32 s93, s92, s74\n"
"s_cmp_ge_u32 s93, 1081\n"
"s_cbranch_scc0 .Lk_wg_159\n"
"s_mov_b32 s90, 106240\n"
"buffer_load_dwordx4 v[140:143], v246, s[36:39], s90 offen nt\n"
"buffer_load_dword v215, v247, s[36:39], s90 offen nt\n"
"s_waitcnt vmcnt(47)\n"
"v_accvgpr_write_b32 a240, v144\n"
"v_accvgpr_write_b32 a248, v145\n"
"v_mov_b32 v0, v146\n"
"v_mov_b32 v8, v147\n"
"s_waitcnt vmcnt(46)\n"
"v_mov_b32 v16, v192\n"
"s_mov_b32 s90, 122880\n"
"buffer_load_dwordx4 v[144:147], v246, s[36:39], s90 offen nt\n"
"buffer_load_dword v192, v247, s[36:39], s90 offen nt\n"
"s_waitcnt vmcnt(47)\n"
"v_accvgpr_write_b32 a241, v148\n"
"v_accvgpr_write_b32 a249, v149\n"
"v_mov_b32 v1, v150\n"
"v_mov_b32 v9, v151\n"
"s_waitcnt vmcnt(46)\n"
"v_mov_b32 v17, v193\n"
"s_mov_b32 s90, 124160\n"
"buffer_load_dwordx4 v[148:151], v246, s[36:39], s90 offen nt\n"
"buffer_load_dword v193, v247, s[36:39], s90 offen nt\n"
"s_waitcnt vmcnt(47)\n"
"v_accvgpr_write_b32 a242, v152\n"
"v_accvgpr_write_b32 a250, v153\n"
"v_mov_b32 v2, v154\n"
"v_mov_b32 v10, v155\n"
"s_waitcnt vmcnt(46)\n"
"v_mov_b32 v18, v194\n"
"s_mov_b32 s90, 125440\n"
"buffer_load_dwordx4 v[152:155], v246, s[36:39], s90 offen nt\n"
"buffer_load_dword v194, v247, s[36:39], s90 offen nt\n"
"s_waitcnt vmcnt(47)\n"
"v_accvgpr_write_b32 a243, v156\n"
"v_accvgpr_write_b32 a251, v157\n"
"v_mov_b32 v3, v158\n"
"v_mov_b32 v11, v159\n"
"s_waitcnt vmcnt(46)\n"
"v_mov_b32 v19, v195\n"
".Lk_wg_160:\n"
"s_memrealtime s[92:93]\n"
"s_waitcnt lgkmcnt(0)\n"
"s_sub_u32 s93, s92, s74\n"
"s_cmp_ge_u32 s93, 1156\n"
"s_cbranch_scc0 .Lk_wg_160\n"
"s_mov_b32 s90, 126720\n"
"buffer_load_dwordx4 v[156:159], v246, s[36:39], s90 offen nt\n"
"buffer_load_dword v195, v247, s[36:39], s90 offen nt\n"
"s_waitcnt vmcnt(47)\n"
"v_accvgpr_write_b32 a244, v160\n"
"v_accvgpr_write_b32 a252, v161\n"
"v_mov_b32 v4, v162\n"
"v_mov_b32 v12, v163\n"
"s_waitcnt vmcnt(46)\n"
"v_mov_b32 v20, v196\n"
"s_mov_b32 s90, 143360\n"
"buffer_load_dwordx4 v[160:163], v246, s[36:39], s90 offen nt\n"
"buffer_load_dword v196, v247, s[36:39], s90 offen nt\n"
"s_waitcnt vmcnt(47)\n"
"v_accvgpr_write_b32 a245, v164\n"
"v_accvgpr_write_b32 a253, v165\n"
"v_mov_b32 v5, v166\n"
"v_mov_b32 v13, v167\n"
"s_waitcnt vmcnt(46)\n"
"v_mov_b32 v21, v197\n"
"s_mov_b32 s90, 144640\n"
"buffer_load_dwordx4 v[164:167], v246, s[36:39], s90 offen nt\n"
"buffer_load_dword v197, v247, s[36:39], s90 offen nt\n"
"s_waitcnt vmcnt(47)\n"
"v_accvgpr_write_b32 a246, v168\n"
"v_accvgpr_write_b32 a254, v169\n"
"v_mov_b32 v6, v170\n"
"v_mov_b32 v14, v171\n"
"s_waitcnt vmcnt(46)\n"
"v_mov_b32 v22, v198\n"
"s_mov_b32 s90, 145920\n"
"buffer_load_dwordx4 v[168:171], v246, s[36:39], s90 offen nt\n"
"buffer_load_dword v198, v247, s[36:39], s90 offen nt\n"
"s_waitcnt vmcnt(47)\n"
"v_accvgpr_write_b32 a247, v172\n"
"v_accvgpr_write_b32 a255, v173\n"
"v_mov_b32 v7, v174\n"
"v_mov_b32 v15, v175\n"
"s_waitcnt vmcnt(46)\n"
"v_mov_b32 v23, v199\n"
".Lk_wg_161:\n"
"s_memrealtime s[92:93]\n"
"s_waitcnt lgkmcnt(0)\n"
"s_sub_u32 s93, s92, s74\n"
"s_cmp_ge_u32 s93, 1231\n"
"s_cbranch_scc0 .Lk_wg_161\n"
"s_mov_b32 s90, 147200\n"
"buffer_load_dwordx4 v[172:175], v246, s[36:39], s90 offen nt\n"
"buffer_load_dword v199, v247, s[36:39], s90 offen nt\n"
"s_lshl_b32 s73, s15, 9\n"
"s_add_u32 s73, s73, 4096\n"
"buffer_load_dwordx2 v[248:249], v237, s[52:55], s73 offen sc1\n"
"s_cmp_ge_u32 s14, 27\n"
"s_cbranch_scc1 .Lk_w12_162\n"
"s_mul_i32 s88, s15, 552960\n"
"s_lshl_b32 s89, s14, 7\n"
"s_add_u32 s88, s88, s89\n"
"s_sub_u32 s89, 2211840, s88\n"
"s_add_u32 s36, s26, s88\n"
"s_addc_u32 s37, s27, 0\n"
"s_and_b32 s37, s37, 0xffff\n"
"s_mov_b32 s38, s89\n"
"s_mov_b32 s39, 0x20000\n"
"v_lshrrev_b32 v254, 4, v224\n"
"v_and_b32 v255, 15, v224\n"
"s_and_b32 s88, s15, 1\n"
"s_mul_i32 s89, s88, 110592\n"
"v_mul_u32_u24 v246, 55296, v254\n"
"v_mad_u32_u24 v246, v255, 8, v246\n"
"v_add_u32 v246, s89, v246\n"
"s_xor_b32 s88, s88, 1\n"
"s_mul_i32 s89, s88, 442368\n"
"v_mul_u32_u24 v247, 13824, v254\n"
"v_mad_u32_u24 v247, v255, 8, v247\n"
"v_add_u32 v247, s89, v247\n"
"s_waitcnt vmcnt(48)\n"
"v_mov_b32 v24, v176\n"
"v_mov_b32 v32, v177\n"
"v_mov_b32 v40, v178\n"
"v_mov_b32 v48, v179\n"
"s_waitcnt vmcnt(47)\n"
"v_mov_b32 v56, v200\n"
"s_mov_b32 s90, 0\n"
"buffer_load_dwordx2 v[176:177], v246, s[36:39], s90 offen nt\n"
"s_waitcnt vmcnt(47)\n"
"v_mov_b32 v25, v180\n"
"v_mov_b32 v33, v181\n"
"v_mov_b32 v41, v182\n"
"v_mov_b32 v49, v183\n"
"s_waitcnt vmcnt(46)\n"
"v_mov_b32 v57, v201\n"
"s_mov_b32 s90, 3456\n"
"buffer_load_dwordx2 v[180:181], v246, s[36:39], s90 offen nt\n"
"s_waitcnt vmcnt(46)\n"
"v_mov_b32 v26, v184\n"
"v_mov_b32 v34, v185\n"
"v_mov_b32 v42, v186\n"
"v_mov_b32 v50, v187\n"
"s_waitcnt vmcnt(45)\n"
"v_mov_b32 v58, v202\n"
"s_mov_b32 s90, 27648\n"
"buffer_load_dwordx2 v[184:185], v246, s[36:39], s90 offen nt\n"
"s_waitcnt vmcnt(45)\n"
"v_mov_b32 v27, v188\n"
"v_mov_b32 v35, v189\n"
"v_mov_b32 v43, v190\n"
"v_mov_b32 v51, v191\n"
"s_waitcnt vmcnt(44)\n"
"v_mov_b32 v59, v203\n"
".Lk_wg_164:\n"
"s_memrealtime s[92:93]\n"
"s_waitcnt lgkmcnt(0)\n"
"s_sub_u32 s93, s92, s74\n"
"s_cmp_ge_u32 s93, 1272\n"
"s_cbranch_scc0 .Lk_wg_164\n"
"s_mov_b32 s90, 31104\n"
"buffer_load_dwordx2 v[188:189], v246, s[36:39], s90 offen nt\n"
"s_waitcnt vmcnt(44)\n"
"v_mov_b32 v28, v96\n"
"v_mov_b32 v36, v97\n"
"v_mov_b32 v44, v98\n"
"v_mov_b32 v52, v99\n"
"s_waitcnt vmcnt(43)\n"
"v_mov_b32 v60, v204\n"
"s_mov_b32 s90, 221184\n"
"buffer_load_dwordx2 v[96:97], v246, s[36:39], s90 offen nt\n"
"s_waitcnt vmcnt(43)\n"
"v_mov_b32 v29, v100\n"
"v_mov_b32 v37, v101\n"
"v_mov_b32 v45, v102\n"
"v_mov_b32 v53, v103\n"
"s_waitcnt vmcnt(42)\n"
"v_mov_b32 v61, v205\n"
"s_mov_b32 s90, 224640\n"
"buffer_load_dwordx2 v[100:101], v246, s[36:39], s90 offen nt\n"
"s_waitcnt vmcnt(42)\n"
"v_mov_b32 v30, v104\n"
"v_mov_b32 v38, v105\n"
"v_mov_b32 v46, v106\n"
"v_mov_b32 v54, v107\n"
"s_waitcnt vmcnt(41)\n"
"v_mov_b32 v62, v206\n"
"s_mov_b32 s90, 248832\n"
"buffer_load_dwordx2 v[104:105], v246, s[36:39], s90 offen nt\n"
"s_waitcnt vmcnt(41)\n"
"v_mov_b32 v31, v108\n"
"v_mov_b32 v39, v109\n"
"v_mov_b32 v47, v110\n"
"v_mov_b32 v55, v111\n"
"s_waitcnt vmcnt(40)\n"
"v_mov_b32 v63, v207\n"
".Lk_wg_165:\n"
"s_memrealtime s[92:93]\n"
"s_waitcnt lgkmcnt(0)\n"
"s_sub_u32 s93, s92, s74\n"
"s_cmp_ge_u32 s93, 1302\n"
"s_cbranch_scc0 .Lk_wg_165\n"
"s_mov_b32 s90, 252288\n"
"buffer_load_dwordx2 v[108:109], v246, s[36:39], s90 offen nt\n"
"s_waitcnt vmcnt(40)\n"
"v_mov_b32 v64, v112\n"
"v_mov_b32 v72, v113\n"
"v_mov_b32 v80, v114\n"
"v_mov_b32 v88, v115\n"
"s_waitcnt vmcnt(39)\n"
"ds_write_b32 v242, v208 offset:0\n"
"s_mov_b32 s90, 6912\n"
"buffer_load_dwordx2 v[112:113], v246, s[36:39], s90 offen nt\n"
"s_waitcnt vmcnt(39)\n"
"v_mov_b32 v65, v116\n"
"v_mov_b32 v73, v117\n"
"v_mov_b32 v81, v118\n"
"v_mov_b32 v89, v119\n"
"s_waitcnt vmcnt(38)\n"
"ds_write_b32 v242, v209 offset:4\n"
"s_mov_b32 s90, 10368\n"
"buffer_load_dwordx2 v[116:117], v246, s[36:39], s90 offen nt\n"
"s_waitcnt vmcnt(38)\n"
"v_mov_b32 v66, v120\n"
"v_mov_b32 v74, v121\n"
"v_mov_b32 v82, v122\n"
"v_mov_b32 v90, v123\n"
"s_waitcnt vmcnt(37)\n"
"ds_write_b32 v242, v210 offset:8\n"
"s_mov_b32 s90, 34560\n"
"buffer_load_dwordx2 v[120:121], v246, s[36:39], s90 offen nt\n"
"s_waitcnt vmcnt(37)\n"
"v_mov_b32 v67, v124\n"
"v_mov_b32 v75, v125\n"
"v_mov_b32 v83, v126\n"
"v_mov_b32 v91, v127\n"
"s_waitcnt vmcnt(36)\n"
"ds_write_b32 v242, v211 offset:12\n"
"s_waitcnt lgkmcnt(0)\n"
".Lk_wg_166:\n"
"s_memrealtime s[92:93]\n"
"s_waitcnt lgkmcnt(0)\n"
"s_sub_u32 s93, s92, s74\n"
"s_cmp_ge_u32 s93, 1332\n"
"s_cbranch_scc0 .Lk_wg_166\n"
"s_mov_b32 s90, 38016\n"
"buffer_load_dwordx2 v[124:125], v246, s[36:39], s90 offen nt\n"
"s_waitcnt vmcnt(36)\n"
"v_mov_b32 v68, v128\n"
"v_mov_b32 v76, v129\n"
"v_mov_b32 v84, v130\n"
"v_mov_b32 v92, v131\n"
"s_waitcnt vmcnt(35)\n"
"ds_write_b32 v242, v212 offset:1024\n"
"s_mov_b32 s90, 228096\n"
"buffer_load_dwordx2 v[128:129], v246, s[36:39], s90 offen nt\n"
"s_waitcnt vmcnt(35)\n"
"v_mov_b32 v69, v132\n"
"v_mov_b32 v77, v133\n"
"v_mov_b32 v85, v134\n"
"v_mov_b32 v93, v135\n"
"s_waitcnt vmcnt(34)\n"
"ds_write_b32 v242, v213 offset:1028\n"
"s_mov_b32 s90, 231552\n"
"buffer_load_dwordx2 v[132:133], v246, s[36:39], s90 offen nt\n"
"s_waitcnt vmcnt(34)\n"
"v_mov_b32 v70, v136\n"
"v_mov_b32 v78, v137\n"
"v_mov_b32 v86, v138\n"
"v_mov_b32 v94, v139\n"
"s_waitcnt vmcnt(33)\n"
"ds_write_b32 v242, v214 offset:1032\n"
"s_mov_b32 s90, 255744\n"
"buffer_load_dwordx2 v[136:137], v246, s[36:39], s90 offen nt\n"
"s_waitcnt vmcnt(33)\n"
"v_mov_b32 v71, v140\n"
"v_mov_b32 v79, v141\n"
"v_mov_b32 v87, v142\n"
"v_mov_b32 v95, v143\n"
"s_waitcnt vmcnt(32)\n"
"ds_write_b32 v242, v215 offset:1036\n"
"s_waitcnt lgkmcnt(0)\n"
".Lk_wg_167:\n"
"s_memrealtime s[92:93]\n"
"s_waitcnt lgkmcnt(0)\n"
"s_sub_u32 s93, s92, s74\n"
"s_cmp_ge_u32 s93, 1362\n"
"s_cbranch_scc0 .Lk_wg_167\n"
"s_mov_b32 s90, 259200\n"
"buffer_load_dwordx2 v[140:141], v246, s[36:39], s90 offen nt\n"
"s_waitcnt vmcnt(32)\n"
"ds_write_b32 v242, v144 offset:2048\n"
"ds_write_b32 v242, v145 offset:4096\n"
"ds_write_b32 v242, v146 offset:6144\n"
"ds_write_b32 v242, v147 offset:8192\n"
"s_waitcnt vmcnt(31)\n"
"ds_write_b32 v242, v192 offset:10240\n"
"s_mov_b32 s90, 13824\n"
"buffer_load_dwordx2 v[144:145], v246, s[36:39], s90 offen nt\n"
"s_waitcnt vmcnt(31)\n"
"ds_write_b32 v242, v148 offset:2052\n"
"ds_write_b32 v242, v149 offset:4100\n"
"ds_write_b32 v242, v150 offset:6148\n"
"ds_write_b32 v242, v151 offset:8196\n"
"s_waitcnt vmcnt(30)\n"
"ds_write_b32 v242, v193 offset:10244\n"
"s_mov_b32 s90, 17280\n"
"buffer_load_dwordx2 v[148:149], v246, s[36:39], s90 offen nt\n"
"s_waitcnt vmcnt(30)\n"
"ds_write_b32 v242, v152 offset:2056\n"
"ds_write_b32 v242, v153 offset:4104\n"
"ds_write_b32 v242, v154 offset:6152\n"
"ds_write_b32 v242, v155 offset:8200\n"
"s_waitcnt vmcnt(29)\n"
"ds_write_b32 v242, v194 offset:10248\n"
"s_mov_b32 s90, 41472\n"
"buffer_load_dwordx2 v[152:153], v246, s[36:39], s90 offen nt\n"
"s_waitcnt vmcnt(29)\n"
"s_waitcnt lgkmcnt(12)\n"
"ds_write_b32 v242, v156 offset:2060\n"
"ds_write_b32 v242, v157 offset:4108\n"
"ds_write_b32 v242, v158 offset:6156\n"
"s_waitcnt lgkmcnt(12)\n"
"ds_write_b32 v242, v159 offset:8204\n"
"s_waitcnt vmcnt(28)\n"
"ds_write_b32 v242, v195 offset:10252\n"
"s_waitcnt lgkmcnt(0)\n"
".Lk_wg_168:\n"
"s_memrealtime s[92:93]\n"
"s_waitcnt lgkmcnt(0)\n"
"s_sub_u32 s93, s92, s74\n"
"s_cmp_ge_u32 s93, 1392\n"
"s_cbranch_scc0 .Lk_wg_168\n"
"s_mov_b32 s90, 44928\n"
"buffer_load_dwordx2 v[156:157], v246, s[36:39], s90 offen nt\n"
"s_waitcnt vmcnt(28)\n"
"ds_write_b32 v242, v160 offset:3072\n"
"ds_write_b32 v242, v161 offset:5120\n"
"ds_write_b32 v242, v162 offset:7168\n"
"ds_write_b32 v242, v163 offset:9216\n"
"s_waitcnt vmcnt(27)\n"
"ds_write_b32 v242, v196 offset:11264\n"
"s_mov_b32 s90, 235008\n"
"buffer_load_dwordx2 v[160:161], v246, s[36:39], s90 offen nt\n"
"s_waitcnt vmcnt(27)\n"
"ds_write_b32 v242, v164 offset:3076\n"
"ds_write_b32 v242, v165 offset:5124\n"
"ds_write_b32 v242, v166 offset:7172\n"
"ds_write_b32 v242, v167 offset:9220\n"
"s_waitcnt vmcnt(26)\n"
"ds_write_b32 v242, v197 offset:11268\n"
"s_mov_b32 s90, 238464\n"
"buffer_load_dwordx2 v[164:165], v246, s[36:39], s90 offen nt\n"
"s_waitcnt vmcnt(26)\n"
"ds_write_b32 v242, v168 offset:3080\n"
"ds_write_b32 v242, v169 offset:5128\n"
"ds_write_b32 v242, v170 offset:7176\n"
"ds_write_b32 v242, v171 offset:9224\n"
"s_waitcnt vmcnt(25)\n"
"ds_write_b32 v242, v198 offset:11272\n"
"s_mov_b32 s90, 262656\n"
"buffer_load_dwordx2 v[168:169], v246, s[36:39], s90 offen nt\n"
"s_waitcnt vmcnt(25)\n"
"s_waitcnt lgkmcnt(12)\n"
"ds_write_b32 v242, v172 offset:3084\n"
"ds_write_b32 v242, v173 offset:5132\n"
"ds_write_b32 v242, v174 offset:7180\n"
"s_waitcnt lgkmcnt(12)\n"
"ds_write_b32 v242, v175 offset:9228\n"
"s_waitcnt vmcnt(24)\n"
"ds_write_b32 v242, v199 offset:11276\n"
"s_waitcnt lgkmcnt(0)\n"
".Lk_wg_169:\n"
"s_memrealtime s[92:93]\n"
"s_waitcnt lgkmcnt(0)\n"
"s_sub_u32 s93, s92, s74\n"
"s_cmp_ge_u32 s93, 1422\n"
"s_cbranch_scc0 .Lk_wg_169\n"
"s_mov_b32 s90, 266112\n"
"buffer_load_dwordx2 v[172:173], v246, s[36:39], s90 offen nt\n"
"s_waitcnt vmcnt(23)\n"
"ds_write_b32 v242, v176 offset:12288\n"
"ds_write_b32 v242, v177 offset:14336\n"
"s_mov_b32 s90, 20736\n"
"buffer_load_dwordx2 v[176:177], v246, s[36:39], s90 offen nt\n"
"s_waitcnt vmcnt(23)\n"
"ds_write_b32 v242, v180 offset:12292\n"
"ds_write_b32 v242, v181 offset:14340\n"
"s_mov_b32 s90, 24192\n"
"buffer_load_dwordx2 v[180:181], v246, s[36:39], s90 offen nt\n"
"s_waitcnt vmcnt(23)\n"
"ds_write_b32 v242, v184 offset:12296\n"
"ds_write_b32 v242, v185 offset:14344\n"
"s_mov_b32 s90, 48384\n"
"buffer_load_dwordx2 v[184:185], v246, s[36:39], s90 offen nt\n"
"s_waitcnt vmcnt(23)\n"
"ds_write_b32 v242, v188 offset:12300\n"
"ds_write_b32 v242, v189 offset:14348\n"
"s_waitcnt lgkmcnt(0)\n"
".Lk_wg_170:\n"
"s_memrealtime s[92:93]\n"
"s_waitcnt lgkmcnt(0)\n"
"s_sub_u32 s93, s92, s74\n"
"s_cmp_ge_u32 s93, 1452\n"
"s_cbranch_scc0 .Lk_wg_170\n"
"s_mov_b32 s90, 51840\n"
"buffer_load_dwordx2 v[188:189], v246, s[36:39], s90 offen nt\n"
"s_waitcnt vmcnt(23)\n"
"ds_write_b32 v242, v96 offset:13312\n"
"ds_write_b32 v242, v97 offset:15360\n"
"s_mov_b32 s90, 241920\n"
"buffer_load_dwordx2 v[96:97], v246, s[36:39], s90 offen nt\n"
"s_waitcnt vmcnt(23)\n"
"ds_write_b32 v242, v100 offset:13316\n"
"ds_write_b32 v242, v101 offset:15364\n"
"s_mov_b32 s90, 245376\n"
"buffer_load_dwordx2 v[100:101], v246, s[36:39], s90 offen nt\n"
"s_waitcnt vmcnt(23)\n"
"ds_write_b32 v242, v104 offset:13320\n"
"ds_write_b32 v242, v105 offset:15368\n"
"s_mov_b32 s90, 269568\n"
"buffer_load_dwordx2 v[104:105], v246, s[36:39], s90 offen nt\n"
"s_waitcnt vmcnt(23)\n"
"ds_write_b32 v242, v108 offset:13324\n"
"ds_write_b32 v242, v109 offset:15372\n"
"s_waitcnt lgkmcnt(0)\n"
".Lk_wg_171:\n"
"s_memrealtime s[92:93]\n"
"s_waitcnt lgkmcnt(0)\n"
"s_sub_u32 s93, s92, s74\n"
"s_cmp_ge_u32 s93, 1482\n"
"s_cbranch_scc0 .Lk_wg_171\n"
"s_mov_b32 s90, 273024\n"
"buffer_load_dwordx2 v[108:109], v246, s[36:39], s90 offen nt\n"
"s_waitcnt vmcnt(23)\n"
"ds_write_b32 v242, v112 offset:16384\n"
"ds_write_b32 v242, v113 offset:18432\n"
"s_mov_b32 s90, 0\n"
"buffer_load_dwordx2 v[112:113], v247, s[36:39], s90 offen nt\n"
"s_waitcnt vmcnt(23)\n"
"ds_write_b32 v242, v116 offset:16388\n"
"ds_write_b32 v242, v117 offset:18436\n"
"s_mov_b32 s90, 3456\n"
"buffer_load_dwordx2 v[116:117], v247, s[36:39], s90 offen nt\n"
"s_waitcnt vmcnt(23)\n"
"ds_write_b32 v242, v120 offset:16392\n"
"ds_write_b32 v242, v121 offset:18440\n"
"s_mov_b32 s90, 6912\n"
"buffer_load_dwordx2 v[120:121], v247, s[36:39], s90 offen nt\n"
"s_waitcnt vmcnt(23)\n"
"ds_write_b32 v242, v124 offset:16396\n"
"ds_write_b32 v242, v125 offset:18444\n"
"s_waitcnt lgkmcnt(0)\n"
".Lk_wg_172:\n"
"s_memrealtime s[92:93]\n"
"s_waitcnt lgkmcnt(0)\n"
"s_sub_u32 s93, s92, s74\n"
"s_cmp_ge_u32 s93, 1512\n"
"s_cbranch_scc0 .Lk_wg_172\n"
"s_mov_b32 s90, 10368\n"
"buffer_load_dwordx2 v[124:125], v247, s[36:39], s90 offen nt\n"
"s_waitcnt vmcnt(23)\n"
"ds_write_b32 v242, v128 offset:17408\n"
"ds_write_b32 v242, v129 offset:19456\n"
"s_mov_b32 s90, 55296\n"
"buffer_load_dwordx2 v[128:129], v247, s[36:39], s90 offen nt\n"
"s_waitcnt vmcnt(23)\n"
"ds_write_b32 v242, v132 offset:17412\n"
"ds_write_b32 v242, v133 offset:19460\n"
"s_mov_b32 s90, 58752\n"
"buffer_load_dwordx2 v[132:133], v247, s[36:39], s90 offen nt\n"
"s_waitcnt vmcnt(23)\n"
"ds_write_b32 v242, v136 offset:17416\n"
"ds_write_b32 v242, v137 offset:19464\n"
"s_mov_b32 s90, 62208\n"
"buffer_load_dwordx2 v[136:137], v247, s[36:39], s90 offen nt\n"
"s_waitcnt vmcnt(23)\n"
"ds_write_b32 v242, v140 offset:17420\n"
"ds_write_b32 v242, v141 offset:19468\n"
"s_waitcnt lgkmcnt(0)\n"
".Lk_wg_173:\n"
"s_memrealtime s[92:93]\n"
"s_waitcnt lgkmcnt(0)\n"
"s_sub_u32 s93, s92, s74\n"
"s_cmp_ge_u32 s93, 1542\n"
"s_cbranch_scc0 .Lk_wg_173\n"
"s_mov_b32 s90, 65664\n"
"buffer_load_dwordx2 v[140:141], v247, s[36:39], s90 offen nt\n"
"s_mul_i32 s88, s14, 81920\n"
"s_mul_i32 s89, s15, 320\n"
"s_add_u32 s88, s88, s89\n"
"s_sub_u32 s89, 2211840, s88\n"
"s_add_u32 s36, s28, s88\n"
"s_addc_u32 s37, s29, 0\n"
"s_and_b32 s37, s37, 0xffff\n"
"s_mov_b32 s38, s89\n"
"s_mov_b32 s39, 0x20000\n"
"v_lshrrev_b32 v254, 4, v224\n"
"v_and_b32 v255, 15, v224\n"
"s_and_b32 s88, s15, 1\n"
"s_mul_i32 s89, s88, 64\n"
"v_mul_u32_u24 v246, 10240, v254\n"
"v_mad_u32_u24 v246, v255, 16, v246\n"
"v_add_u32 v246, s89, v246\n"
"s_xor_b32 s88, s88, 1\n"
"s_mul_i32 s89, s88, 256\n"
"v_mul_u32_u24 v247, 10240, v254\n"
"v_mad_u32_u24 v247, v255, 4, v247\n"
"v_add_u32 v247, s89, v247\n"
"s_waitcnt vmcnt(23)\n"
"ds_write_b32 v242, v144 offset:20480\n"
"ds_write_b32 v242, v145 offset:22528\n"
"s_mov_b32 s90, 0\n"
"buffer_load_dwordx4 v[144:147], v246, s[36:39], s90 offen nt\n"
"buffer_load_dword v200, v247, s[36:39], s90 offen nt\n"
"s_waitcnt vmcnt(24)\n"
"ds_write_b32 v242, v148 offset:20484\n"
"ds_write_b32 v242, v149 offset:22532\n"
"s_mov_b32 s90, 1280\n"
"buffer_load_dwordx4 v[148:151], v246, s[36:39], s90 offen nt\n"
"buffer_load_dword v201, v247, s[36:39], s90 offen nt\n"
"s_waitcnt vmcnt(25)\n"
"ds_write_b32 v242, v152 offset:20488\n"
"ds_write_b32 v242, v153 offset:22536\n"
"s_mov_b32 s90, 5120\n"
"buffer_load_dwordx4 v[152:155], v246, s[36:39], s90 offen nt\n"
"buffer_load_dword v202, v247, s[36:39], s90 offen nt\n"
"s_waitcnt vmcnt(26)\n"
"ds_write_b32 v242, v156 offset:20492\n"
"ds_write_b32 v242, v157 offset:22540\n"
"s_waitcnt lgkmcnt(0)\n"
".Lk_wg_174:\n"
"s_memrealtime s[92:93]\n"
"s_waitcnt lgkmcnt(0)\n"
"s_sub_u32 s93, s92, s74\n"
"s_cmp_ge_u32 s93, 1606\n"
"s_cbranch_scc0 .Lk_wg_174\n"
"s_mov_b32 s90, 6400\n"
"buffer_load_dwordx4 v[156:159], v246, s[36:39], s90 offen nt\n"
"buffer_load_dword v203, v247, s[36:39], s90 offen nt\n"
"s_waitcnt vmcnt(27)\n"
"ds_write_b32 v242, v160 offset:21504\n"
"ds_write_b32 v242, v161 offset:23552\n"
"s_mov_b32 s90, 40960\n"
"buffer_load_dwordx4 v[160:163], v246, s[36:39], s90 offen nt\n"
"buffer_load_dword v204, v247, s[36:39], s90 offen nt\n"
"s_waitcnt vmcnt(28)\n"
"ds_write_b32 v242, v164 offset:21508\n"
"ds_write_b32 v242, v165 offset:23556\n"
"s_mov_b32 s90, 42240\n"
"buffer_load_dwordx4 v[164:167], v246, s[36:39], s90 offen nt\n"
"buffer_load_dword v205, v247, s[36:39], s90 offen nt\n"
"s_waitcnt vmcnt(29)\n"
"ds_write_b32 v242, v168 offset:21512\n"
"ds_write_b32 v242, v169 offset:23560\n"
"s_mov_b32 s90, 46080\n"
"buffer_load_dwordx4 v[168:171], v246, s[36:39], s90 offen nt\n"
"buffer_load_dword v206, v247, s[36:39], s90 offen nt\n"
"s_waitcnt vmcnt(30)\n"
"ds_write_b32 v242, v172 offset:21516\n"
"ds_write_b32 v242, v173 offset:23564\n"
"s_waitcnt lgkmcnt(0)\n"
".Lk_wg_175:\n"
"s_memrealtime s[92:93]\n"
"s_waitcnt lgkmcnt(0)\n"
"s_sub_u32 s93, s92, s74\n"
"s_cmp_ge_u32 s93, 1681\n"
"s_cbranch_scc0 .Lk_wg_175\n"
"s_mov_b32 s90, 47360\n"
"buffer_load_dwordx4 v[172:175], v246, s[36:39], s90 offen nt\n"
"buffer_load_dword v207, v247, s[36:39], s90 offen nt\n"
"s_waitcnt vmcnt(31)\n"
"ds_write_b32 v242, v176 offset:24576\n"
"ds_write_b32 v242, v177 offset:26624\n"
"s_mov_b32 s90, 2560\n"
"buffer_load_dwordx4 v[176:179], v246, s[36:39], s90 offen nt\n"
"buffer_load_dword v208, v247, s[36:39], s90 offen nt\n"
"s_waitcnt vmcnt(32)\n"
"ds_write_b32 v242, v180 offset:24580\n"
"ds_write_b32 v242, v181 offset:26628\n"
"s_mov_b32 s90, 3840\n"
"buffer_load_dwordx4 v[180:183], v246, s[36:39], s90 offen nt\n"
"buffer_load_dword v209, v247, s[36:39], s90 offen nt\n"
"s_waitcnt vmcnt(33)\n"
"ds_write_b32 v242, v184 offset:24584\n"
"ds_write_b32 v242, v185 offset:26632\n"
"s_mov_b32 s90, 7680\n"
"buffer_load_dwordx4 v[184:187], v246, s[36:39], s90 offen nt\n"
"buffer_load_dword v210, v247, s[36:39], s90 offen nt\n"
"s_waitcnt vmcnt(34)\n"
"ds_write_b32 v242, v188 offset:24588\n"
"ds_write_b32 v242, v189 offset:26636\n"
"s_waitcnt lgkmcnt(0)\n"
".Lk_wg_176:\n"
"s_memrealtime s[92:93]\n"
"s_waitcnt lgkmcnt(0)\n"
"s_sub_u32 s93, s92, s74\n"
"s_cmp_ge_u32 s93, 1756\n"
"s_cbranch_scc0 .Lk_wg_176\n"
"s_mov_b32 s90, 8960\n"
"buffer_load_dwordx4 v[188:191], v246, s[36:39], s90 offen nt\n"
"buffer_load_dword v211, v247, s[36:39], s90 offen nt\n"
"s_waitcnt vmcnt(35)\n"
"ds_write_b32 v242, v96 offset:25600\n"
"ds_write_b32 v242, v97 offset:27648\n"
"s_mov_b32 s90, 43520\n"
"buffer_load_dwordx4 v[96:99], v246, s[36:39], s90 offen nt\n"
"buffer_load_dword v212, v247, s[36:39], s90 offen nt\n"
"s_waitcnt vmcnt(36)\n"
"ds_write_b32 v242, v100 offset:25604\n"
"ds_write_b32 v242, v101 offset:27652\n"
"s_mov_b32 s90, 44800\n"
"buffer_load_dwordx4 v[100:103], v246, s[36:39], s90 offen nt\n"
"buffer_load_dword v213, v247, s[36:39], s90 offen nt\n"
"s_waitcnt vmcnt(37)\n"
"ds_write_b32 v242, v104 offset:25608\n"
"ds_write_b32 v242, v105 offset:27656\n"
"s_mov_b32 s90, 48640\n"
"buffer_load_dwordx4 v[104:107], v246, s[36:39], s90 offen nt\n"
"buffer_load_dword v214, v247, s[36:39], s90 offen nt\n"
"s_waitcnt vmcnt(38)\n"
"ds_write_b32 v242, v108 offset:25612\n"
"ds_write_b32 v242, v109 offset:27660\n"
"s_waitcnt lgkmcnt(0)\n"
".Lk_wg_177:\n"
"s_memrealtime s[92:93]\n"
"s_waitcnt lgkmcnt(0)\n"
"s_sub_u32 s93, s92, s74\n"
"s_cmp_ge_u32 s93, 1831\n"
"s_cbranch_scc0 .Lk_wg_177\n"
"s_mov_b32 s90, 49920\n"
"buffer_load_dwordx4 v[108:111], v246, s[36:39], s90 offen nt\n"
"buffer_load_dword v215, v247, s[36:39], s90 offen nt\n"
"s_waitcnt vmcnt(39)\n"
"ds_write_b32 v242, v112 offset:28672\n"
"ds_write_b32 v242, v113 offset:30720\n"
"s_waitcnt vmcnt(38)\n"
"ds_write_b32 v242, v116 offset:28676\n"
"ds_write_b32 v242, v117 offset:30724\n"
"s_waitcnt vmcnt(37)\n"
"ds_write_b32 v242, v120 offset:28680\n"
"ds_write_b32 v242, v121 offset:30728\n"
"s_waitcnt vmcnt(36)\n"
"ds_write_b32 v242, v124 offset:28684\n"
"ds_write_b32 v242, v125 offset:30732\n"
"s_waitcnt vmcnt(35)\n"
"ds_write_b32 v242, v128 offset:29696\n"
"ds_write_b32 v242, v129 offset:31744\n"
"s_waitcnt vmcnt(34)\n"
"ds_write_b32 v242, v132 offset:29700\n"
"ds_write_b32 v242, v133 offset:31748\n"
"s_waitcnt vmcnt(33)\n"
"ds_write_b32 v242, v136 offset:29704\n"
"ds_write_b32 v242, v137 offset:31752\n"
"s_waitcnt vmcnt(32)\n"
"ds_write_b32 v242, v140 offset:29708\n"
"s_waitcnt lgkmcnt(12)\n"
"ds_write_b32 v242, v141 offset:31756\n"
"s_waitcnt vmcnt(31)\n"
"v_accvgpr_write_b32 a160, v144\n"
"v_accvgpr_write_b32 a168, v145\n"
"v_accvgpr_write_b32 a176, v146\n"
"v_accvgpr_write_b32 a184, v147\n"
"s_waitcnt vmcnt(30)\n"
"v_accvgpr_write_b32 a192, v200\n"
"s_waitcnt vmcnt(29)\n"
"v_accvgpr_write_b32 a161, v148\n"
"v_accvgpr_write_b32 a169, v149\n"
"v_accvgpr_write_b32 a177, v150\n"
"v_accvgpr_write_b32 a185, v151\n"
"s_waitcnt vmcnt(28)\n"
"v_accvgpr_write_b32 a193, v201\n"
"s_waitcnt vmcnt(27)\n"
"v_accvgpr_write_b32 a162, v152\n"
"v_accvgpr_write_b32 a170, v153\n"
"v_accvgpr_write_b32 a178, v154\n"
"v_accvgpr_write_b32 a186, v155\n"
"s_waitcnt vmcnt(26)\n"
"v_accvgpr_write_b32 a194, v202\n"
"s_waitcnt vmcnt(25)\n"
"v_accvgpr_write_b32 a163, v156\n"
"v_accvgpr_write_b32 a171, v157\n"
"v_accvgpr_write_b32 a179, v158\n"
"v_accvgpr_write_b32 a187, v159\n"
"s_waitcnt vmcnt(24)\n"
"v_accvgpr_write_b32 a195, v203\n"
"s_waitcnt vmcnt(23)\n"
"v_accvgpr_write_b32 a164, v160\n"
"v_accvgpr_write_b32 a172, v161\n"
"v_accvgpr_write_b32 a180, v162\n"
"v_accvgpr_write_b32 a188, v163\n"
"s_waitcnt vmcnt(22)\n"
"v_accvgpr_write_b32 a196, v204\n"
"s_waitcnt vmcnt(21)\n"
"v_accvgpr_write_b32 a165, v164\n"
"v_accvgpr_write_b32 a173, v165\n"
"v_accvgpr_write_b32 a181, v166\n"
"v_accvgpr_write_b32 a189, v167\n"
"s_waitcnt vmcnt(20)\n"
"v_accvgpr_write_b32 a197, v205\n"
"s_waitcnt vmcnt(19)\n"
"v_accvgpr_write_b32 a166, v168\n"
"v_accvgpr_write_b32 a174, v169\n"
"v_accvgpr_write_b32 a182, v170\n"
"v_accvgpr_write_b32 a190, v171\n"
"s_waitcnt vmcnt(18)\n"
"v_accvgpr_write_b32 a198, v206\n"
"s_waitcnt vmcnt(17)\n"
"v_accvgpr_write_b32 a167, v172\n"
"v_accvgpr_write_b32 a175, v173\n"
"v_accvgpr_write_b32 a183, v174\n"
"v_accvgpr_write_b32 a191, v175\n"
"s_waitcnt vmcnt(16)\n"
"v_accvgpr_write_b32 a199, v207\n"
"s_waitcnt vmcnt(15)\n"
"v_accvgpr_write_b32 a200, v176\n"
"v_accvgpr_write_b32 a208, v177\n"
"v_accvgpr_write_b32 a216, v178\n"
"v_accvgpr_write_b32 a224, v179\n"
"s_waitcnt vmcnt(14)\n"
"v_accvgpr_write_b32 a232, v208\n"
"s_waitcnt vmcnt(13)\n"
"v_accvgpr_write_b32 a201, v180\n"
"v_accvgpr_write_b32 a209, v181\n"
"v_accvgpr_write_b32 a217, v182\n"
"v_accvgpr_write_b32 a225, v183\n"
"s_waitcnt vmcnt(12)\n"
"v_accvgpr_write_b32 a233, v209\n"
"s_waitcnt vmcnt(11)\n"
"v_accvgpr_write_b32 a202, v184\n"
"v_accvgpr_write_b32 a210, v185\n"
"v_accvgpr_write_b32 a218, v186\n"
"v_accvgpr_write_b32 a226, v187\n"
"s_waitcnt vmcnt(10)\n"
"v_accvgpr_write_b32 a234, v210\n"
"s_waitcnt vmcnt(9)\n"
"v_accvgpr_write_b32 a203, v188\n"
"v_accvgpr_write_b32 a211, v189\n"
"v_accvgpr_write_b32 a219, v190\n"
"v_accvgpr_write_b32 a227, v191\n"
"s_waitcnt vmcnt(8)\n"
"v_accvgpr_write_b32 a235, v211\n"
"s_waitcnt vmcnt(7)\n"
"v_accvgpr_write_b32 a204, v96\n"
"v_accvgpr_write_b32 a212, v97\n"
"v_accvgpr_write_b32 a220, v98\n"
"v_accvgpr_write_b32 a228, v99\n"
"s_waitcnt vmcnt(6)\n"
"v_accvgpr_write_b32 a236, v212\n"
"s_waitcnt vmcnt(5)\n"
"v_accvgpr_write_b32 a205, v100\n"
"v_accvgpr_write_b32 a213, v101\n"
"v_accvgpr_write_b32 a221, v102\n"
"v_accvgpr_write_b32 a229, v103\n"
"s_waitcnt vmcnt(4)\n"
"v_accvgpr_write_b32 a237, v213\n"
"s_waitcnt vmcnt(3)\n"
"v_accvgpr_write_b32 a206, v104\n"
"v_accvgpr_write_b32 a214, v105\n"
"v_accvgpr_write_b32 a222, v106\n"
"v_accvgpr_write_b32 a230, v107\n"
"s_waitcnt vmcnt(2)\n"
"v_accvgpr_write_b32 a238, v214\n"
"s_waitcnt vmcnt(1)\n"
"v_accvgpr_write_b32 a207, v108\n"
"v_accvgpr_write_b32 a215, v109\n"
"v_accvgpr_write_b32 a223, v110\n"
"v_accvgpr_write_b32 a231, v111\n"
"s_waitcnt vmcnt(0)\n"
"v_accvgpr_write_b32 a239, v215\n"
"s_waitcnt lgkmcnt(0)\n"
"s_branch .Lk_w12j_163\n"
".Lk_w12_162:\n"
"s_waitcnt vmcnt(48)\n"
"v_mov_b32 v24, v176\n"
"v_mov_b32 v32, v177\n"
"v_mov_b32 v40, v178\n"
"v_mov_b32 v48, v179\n"
"s_waitcnt vmcnt(47)\n"
"v_mov_b32 v56, v200\n"
"s_waitcnt vmcnt(46)\n"
"v_mov_b32 v25, v180\n"
"v_mov_b32 v33, v181\n"
"v_mov_b32 v41, v182\n"
"v_mov_b32 v49, v183\n"
"s_waitcnt vmcnt(45)\n"
"v_mov_b32 v57, v201\n"
"s_waitcnt vmcnt(44)\n"
"v_mov_b32 v26, v184\n"
"v_mov_b32 v34, v185\n"
"v_mov_b32 v42, v186\n"
"v_mov_b32 v50, v187\n"
"s_waitcnt vmcnt(43)\n"
"v_mov_b32 v58, v202\n"
"s_waitcnt vmcnt(42)\n"
"v_mov_b32 v27, v188\n"
"v_mov_b32 v35, v189\n"
"v_mov_b32 v43, v190\n"
"v_mov_b32 v51, v191\n"
"s_waitcnt vmcnt(41)\n"
"v_mov_b32 v59, v203\n"
"s_waitcnt vmcnt(40)\n"
"v_mov_b32 v28, v96\n"
"v_mov_b32 v36, v97\n"
"v_mov_b32 v44, v98\n"
"v_mov_b32 v52, v99\n"
"s_waitcnt vmcnt(39)\n"
"v_mov_b32 v60, v204\n"
"s_waitcnt vmcnt(38)\n"
"v_mov_b32 v29, v100\n"
"v_mov_b32 v37, v101\n"
"v_mov_b32 v45, v102\n"
"v_mov_b32 v53, v103\n"
"s_waitcnt vmcnt(37)\n"
"v_mov_b32 v61, v205\n"
"s_waitcnt vmcnt(36)\n"
"v_mov_b32 v30, v104\n"
"v_mov_b32 v38, v105\n"
"v_mov_b32 v46, v106\n"
"v_mov_b32 v54, v107\n"
"s_waitcnt vmcnt(35)\n"
"v_mov_b32 v62, v206\n"
"s_waitcnt vmcnt(34)\n"
"v_mov_b32 v31, v108\n"
"v_mov_b32 v39, v109\n"
"v_mov_b32 v47, v110\n"
"v_mov_b32 v55, v111\n"
"s_waitcnt vmcnt(33)\n"
"v_mov_b32 v63, v207\n"
"s_waitcnt vmcnt(32)\n"
"v_mov_b32 v64, v112\n"
"v_mov_b32 v72, v113\n"
"v_mov_b32 v80, v114\n"
"v_mov_b32 v88, v115\n"
"s_waitcnt vmcnt(31)\n"
"ds_write_b32 v242, v208 offset:0\n"
"s_waitcnt vmcnt(30)\n"
"v_mov_b32 v65, v116\n"
"v_mov_b32 v73, v117\n"
"v_mov_b32 v81, v118\n"
"v_mov_b32 v89, v119\n"
"s_waitcnt vmcnt(29)\n"
"ds_write_b32 v242, v209 offset:4\n"
"s_waitcnt vmcnt(28)\n"
"v_mov_b32 v66, v120\n"
"v_mov_b32 v74, v121\n"
"v_mov_b32 v82, v122\n"
"v_mov_b32 v90, v123\n"
"s_waitcnt vmcnt(27)\n"
"ds_write_b32 v242, v210 offset:8\n"
"s_waitcnt vmcnt(26)\n"
"v_mov_b32 v67, v124\n"
"v_mov_b32 v75, v125\n"
"v_mov_b32 v83, v126\n"
"v_mov_b32 v91, v127\n"
"s_waitcnt vmcnt(25)\n"
"ds_write_b32 v242, v211 offset:12\n"
"s_waitcnt vmcnt(24)\n"
"v_mov_b32 v68, v128\n"
"v_mov_b32 v76, v129\n"
"v_mov_b32 v84, v130\n"
"v_mov_b32 v92, v131\n"
"s_waitcnt vmcnt(23)\n"
"ds_write_b32 v242, v212 offset:1024\n"
"s_waitcnt vmcnt(22)\n"
"v_mov_b32 v69, v132\n"
"v_mov_b32 v77, v133\n"
"v_mov_b32 v85, v134\n"
"v_mov_b32 v93, v135\n"
"s_waitcnt vmcnt(21)\n"
"ds_write_b32 v242, v213 offset:1028\n"
"s_waitcnt vmcnt(20)\n"
"v_mov_b32 v70, v136\n"
"v_mov_b32 v78, v137\n"
"v_mov_b32 v86, v138\n"
"v_mov_b32 v94, v139\n"
"s_waitcnt vmcnt(19)\n"
"ds_write_b32 v242, v214 offset:1032\n"
"s_waitcnt vmcnt(18)\n"
"v_mov_b32 v71, v140\n"
"v_mov_b32 v79, v141\n"
"v_mov_b32 v87, v142\n"
"v_mov_b32 v95, v143\n"
"s_waitcnt vmcnt(17)\n"
"ds_write_b32 v242, v215 offset:1036\n"
"s_waitcnt vmcnt(16)\n"
"ds_write_b32 v242, v144 offset:2048\n"
"ds_write_b32 v242, v145 offset:4096\n"
"ds_write_b32 v242, v146 offset:6144\n"
"ds_write_b32 v242, v147 offset:8192\n"
"s_waitcnt vmcnt(15)\n"
"ds_write_b32 v242, v192 offset:10240\n"
"s_waitcnt vmcnt(14)\n"
"ds_write_b32 v242, v148 offset:2052\n"
"ds_write_b32 v242, v149 offset:4100\n"
"s_waitcnt lgkmcnt(12)\n"
"ds_write_b32 v242, v150 offset:6148\n"
"ds_write_b32 v242, v151 offset:8196\n"
"s_waitcnt vmcnt(13)\n"
"ds_write_b32 v242, v193 offset:10244\n"
"s_waitcnt vmcnt(12)\n"
"s_waitcnt lgkmcnt(12)\n"
"ds_write_b32 v242, v152 offset:2056\n"
"ds_write_b32 v242, v153 offset:4104\n"
"ds_write_b32 v242, v154 offset:6152\n"
"s_waitcnt lgkmcnt(12)\n"
"ds_write_b32 v242, v155 offset:8200\n"
"s_waitcnt vmcnt(11)\n"
"ds_write_b32 v242, v194 offset:10248\n"
"s_waitcnt vmcnt(10)\n"
"ds_write_b32 v242, v156 offset:2060\n"
"s_waitcnt lgkmcnt(12)\n"
"ds_write_b32 v242, v157 offset:4108\n"
"ds_write_b32 v242, v158 offset:6156\n"
"ds_write_b32 v242, v159 offset:8204\n"
"s_waitcnt vmcnt(9)\n"
"s_waitcnt lgkmcnt(12)\n"
"ds_write_b32 v242, v195 offset:10252\n"
"s_waitcnt vmcnt(8)\n"
"ds_write_b32 v242, v160 offset:3072\n"
"ds_write_b32 v242, v161 offset:5120\n"
"s_waitcnt lgkmcnt(12)\n"
"ds_write_b32 v242, v162 offset:7168\n"
"ds_write_b32 v242, v163 offset:9216\n"
"s_waitcnt vmcnt(7)\n"
"ds_write_b32 v242, v196 offset:11264\n"
"s_waitcnt vmcnt(6)\n"
"s_waitcnt lgkmcnt(12)\n"
"ds_write_b32 v242, v164 offset:3076\n"
"ds_write_b32 v242, v165 offset:5124\n"
"ds_write_b32 v242, v166 offset:7172\n"
"s_waitcnt lgkmcnt(12)\n"
"ds_write_b32 v242, v167 offset:9220\n"
"s_waitcnt vmcnt(5)\n"
"ds_write_b32 v242, v197 offset:11268\n"
"s_waitcnt vmcnt(4)\n"
"ds_write_b32 v242, v168 offset:3080\n"
"s_waitcnt lgkmcnt(12)\n"
"ds_write_b32 v242, v169 offset:5128\n"
"ds_write_b32 v242, v170 offset:7176\n"
"ds_write_b32 v242, v171 offset:9224\n"
"s_waitcnt vmcnt(3)\n"
"s_waitcnt lgkmcnt(12)\n"
"ds_write_b32 v242, v198 offset:11272\n"
"s_waitcnt vmcnt(2)\n"
"ds_write_b32 v242, v172 offset:3084\n"
"ds_write_b32 v242, v173 offset:5132\n"
"s_waitcnt lgkmcnt(12)\n"
"ds_write_b32 v242, v174 offset:7180\n"
"ds_write_b32 v242, v175 offset:9228\n"
"s_waitcnt vmcnt(1)\n"
"ds_write_b32 v242, v199 offset:11276\n"
"s_waitcnt vmcnt(0)\n"
"s_waitcnt lgkmcnt(0)\n"
".Lk_w12j_163:\n"
"s_nop 1\n"
"v_cmp_ne_u64 vcc, s[86:87], v[248:249]\n"
"s_cbranch_vccz .Lk_arrok_179\n"
".Lk_arr_178:\n"
"s_cmp_eq_u32 s96, 0\n"
"s_cbranch_scc1 .Lk_arrok_179\n"
"s_sub_u32 s96, s96, 1\n"
"buffer_load_dwordx2 v[248:249], v237, s[52:55], s73 offen sc1\n"
"s_waitcnt vmcnt(0)\n"
"v_cmp_ne_u64 vcc, s[86:87], v[248:249]\n"
"s_cbranch_vccnz .Lk_arr_178\n"
".Lk_arrok_179:\n"
"s_barrier\n"
"ds_read_b128 v[96:99], v242 offset:0\n"
"ds_read_b128 v[100:103], v242 offset:1024\n"
"ds_read_b128 v[104:107], v242 offset:2048\n"
"ds_read_b128 v[108:111], v242 offset:3072\n"
"ds_read_b128 v[112:115], v242 offset:4096\n"
"ds_read_b128 v[116:119], v242 offset:5120\n"
"ds_read_b128 v[120:123], v242 offset:6144\n"
"ds_read_b128 v[124:127], v242 offset:7168\n"
"ds_read_b128 v[128:131], v242 offset:8192\n"
"ds_read_b128 v[132:135], v242 offset:9216\n"
"ds_read_b128 v[136:139], v242 offset:10240\n"
"ds_read_b128 v[140:143], v242 offset:11264\n"
"v_mov_b32 v188, 0\n"
"v_mov_b32 v189, 0\n"
"v_mov_b32 v192, 0\n"
"v_mov_b32 v193, 0\n"
"v_mov_b32 v196, 0\n"
"v_mov_b32 v197, 0\n"
"v_mov_b32 v200, 0\n"
"v_mov_b32 v201, 0\n"
"v_mov_b32 v204, 0\n"
"v_mov_b32 v205, 0\n"
"s_mov_b32 s90, 40960\n"
"s_mul_i32 s91, s13, 2560\n"
"s_mul_i32 s92, s34, 640\n"
"s_add_u32 s91, s91, s92\n"
"s_add_u32 s91, s91, 614400\n"
"s_waitcnt lgkmcnt(0)\n"
"s_nop 1\n"
"s_branch .Lk_o2_182\n"
".Lk_o_180:\n"
".Lk_o2_182:\n"
"s_cmp_eq_u32 s96, 0\n"
"s_cbranch_scc1 .Lk_ook_181\n"
"s_sub_u32 s96, s96, 1\n"
"buffer_load_dwordx2 v[160:161], v231, s[52:55], s90 offen sc1\n"
"buffer_load_dwordx4 v[144:147], v229, s[52:55], s90 offen offset:0 sc1\n"
"buffer_load_dwordx4 v[148:151], v229, s[52:55], s90 offen offset:1280 sc1\n"
"buffer_load_dwordx4 v[152:155], v229, s[52:55], s90 offen offset:2560 sc1\n"
"buffer_load_dwordx4 v[156:159], v230, s[52:55], s90 offen offset:3840 sc1\n"
"s_waitcnt vmcnt(2)\n"
"v_max3_u32 v246, v160, v144, v148\n"
"s_waitcnt vmcnt(0)\n"
"v_max3_u32 v246, v246, v152, v156\n"
"v_cmp_eq_u32 vcc, -1, v246\n"
"s_cbranch_vccnz .Lk_o_180\n"
"v_max_f32_dpp v246, v160, v160 quad_perm:[1,0,3,2] row_mask:0xf bank_mask:0xf\n"
"s_nop 1\n"
"v_max_f32_dpp v246, v246, v246 quad_perm:[2,3,0,1] row_mask:0xf bank_mask:0xf\n"
"s_nop 1\n"
"v_max_f32_dpp v246, v246, v246 row_half_mirror row_mask:0xf bank_mask:0xf\n"
"v_sub_f32 v246, v160, v246\n"
"v_exp_f32 v246, v246\n"
"s_nop 0\n"
"v_cndmask_b32 v246, v246, 0, s[76:77]\n"
"v_mul_f32 v247, v246, v161\n"
"s_nop 1\n"
"v_add_f32_dpp v247, v247, v247 quad_perm:[1,0,3,2] row_mask:0xf bank_mask:0xf\n"
"s_nop 1\n"
"v_add_f32_dpp v247, v247, v247 quad_perm:[2,3,0,1] row_mask:0xf bank_mask:0xf\n"
"s_nop 1\n"
"v_add_f32_dpp v247, v247, v247 row_half_mirror row_mask:0xf bank_mask:0xf\n"
"v_rcp_f32 v247, v247\n"
"s_nop 0\n"
"v_mul_f32 v246, v246, v247\n"
"s_nop 1\n"
"v_mov_b32_dpp v162, v246 row_newbcast:0 row_mask:0xf bank_mask:0xf\n"
"v_mov_b32_dpp v163, v246 row_newbcast:2 row_mask:0xf bank_mask:0xf\n"
"v_mov_b32_dpp v164, v246 row_newbcast:4 row_mask:0xf bank_mask:0xf\n"
"v_mov_b32_dpp v165, v246 row_newbcast:6 row_mask:0xf bank_mask:0xf\n"
"v_pk_mul_f32 v[166:167], v[144:145], v[162:163] op_sel:[0,0] op_sel_hi:[1,0]\n"
"v_pk_mul_f32 v[168:169], v[146:147], v[162:163] op_sel:[0,0] op_sel_hi:[1,0]\n"
"v_pk_fma_f32 v[166:167], v[148:149], v[162:163], v[166:167] op_sel:[0,1,0] op_sel_hi:[1,1,1]\n"
"v_pk_fma_f32 v[168:169], v[150:151], v[162:163], v[168:169] op_sel:[0,1,0] op_sel_hi:[1,1,1]\n"
"v_pk_fma_f32 v[166:167], v[152:153], v[164:165], v[166:167] op_sel:[0,0,0] op_sel_hi:[1,0,1]\n"
"v_pk_fma_f32 v[168:169], v[154:155], v[164:165], v[168:169] op_sel:[0,0,0] op_sel_hi:[1,0,1]\n"
"v_pk_fma_f32 v[166:167], v[156:157], v[164:165], v[166:167] op_sel:[0,1,0] op_sel_hi:[1,1,1]\n"
"v_pk_fma_f32 v[168:169], v[158:159], v[164:165], v[168:169] op_sel:[0,1,0] op_sel_hi:[1,1,1]\n"
"s_nop 1\n"
"v_permlane32_swap_b32 v166, v168\n"
"v_permlane32_swap_b32 v167, v169\n"
"v_pk_add_f32 v[166:167], v[166:167], v[168:169]\n"
"v_cmp_u_f32 vcc, v166, v167\n"
"v_cvt_pk_bf16_f32 v166, v166, v167\n"
"s_cbranch_vccnz .Lk_o_180\n"
".Lk_ook_181:\n"
"ds_write_b32 v232, v166 offset:7680\n"
"ds_read_b128 v[172:175], v233 offset:7680\n"
"ds_read_b128 v[176:179], v233 offset:7744\n"
"ds_read_b128 v[180:183], v233 offset:7808\n"
"ds_read_b128 v[184:187], v233 offset:7872\n"
"s_waitcnt lgkmcnt(3)\n"
"v_smfmac_f32_16x16x64_bf16 v[188:191], v[172:175], a[240:247], v225\n"
"v_smfmac_f32_16x16x64_bf16 v[192:195], v[172:175], a[248:255], v225\n"
"v_smfmac_f32_16x16x64_bf16 v[196:199], v[172:175], v[0:7], v225\n"
"v_smfmac_f32_16x16x64_bf16 v[200:203], v[172:175], v[8:15], v225\n"
"v_smfmac_f32_16x16x64_bf16 v[204:207], v[172:175], v[16:23], v225\n"
"s_waitcnt lgkmcnt(2)\n"
"v_smfmac_f32_16x16x64_bf16 v[188:191], v[176:179], v[24:31], v225\n"
"v_smfmac_f32_16x16x64_bf16 v[192:195], v[176:179], v[32:39], v225\n"
"v_smfmac_f32_16x16x64_bf16 v[196:199], v[176:179], v[40:47], v225\n"
"v_smfmac_f32_16x16x64_bf16 v[200:203], v[176:179], v[48:55], v225\n"
"v_smfmac_f32_16x16x64_bf16 v[204:207], v[176:179], v[56:63], v225\n"
"s_waitcnt lgkmcnt(1)\n"
"v_smfmac_f32_16x16x64_bf16 v[188:191], v[180:183], v[64:71], v225\n"
"v_smfmac_f32_16x16x64_bf16 v[192:195], v[180:183], v[72:79], v225\n"
"v_smfmac_f32_16x16x64_bf16 v[196:199], v[180:183], v[80:87], v225\n"
"v_smfmac_f32_16x16x64_bf16 v[200:203], v[180:183], v[88:95], v225\n"
"v_smfmac_f32_16x16x64_bf16 v[204:207], v[180:183], v[96:103], v225\n"
"s_waitcnt lgkmcnt(0)\n"
"v_smfmac_f32_16x16x64_bf16 v[188:191], v[184:187], v[104:111], v225\n"
"v_smfmac_f32_16x16x64_bf16 v[192:195], v[184:187], v[112:119], v225\n"
"v_smfmac_f32_16x16x64_bf16 v[196:199], v[184:187], v[120:127], v225\n"
"v_smfmac_f32_16x16x64_bf16 v[200:203], v[184:187], v[128:135], v225\n"
"v_smfmac_f32_16x16x64_bf16 v[204:207], v[184:187], v[136:143], v225\n"
"s_mov_b64 exec, 0xffff\n"
"s_nop 3\n"
"ds_write2_b64 v235, v[188:189], v[192:193] offset0:0 offset1:16\n"
"s_nop 0\n"
"ds_write2_b64 v235, v[196:197], v[200:201] offset0:32 offset1:48\n"
"ds_write_b64 v235, v[204:205] offset:512\n"
"s_mov_b32 exec_lo, -1\n"
"s_mov_b32 exec_hi, 0xff\n"
"s_waitcnt lgkmcnt(0)\n"
"s_barrier\n"
"s_cmp_lg_u32 s15, 0\n"
"s_cbranch_scc1 .Lk_os_183\n"
"ds_read_b128 v[144:147], v226 offset:9728\n"
"ds_read_b128 v[148:151], v226 offset:10368\n"
"ds_read_b128 v[152:155], v226 offset:11008\n"
"ds_read_b128 v[156:159], v226 offset:11648\n"
"s_waitcnt lgkmcnt(2)\n"
"v_pk_add_f32 v[144:145], v[144:145], v[148:149]\n"
"v_pk_add_f32 v[146:147], v[146:147], v[150:151]\n"
"s_waitcnt lgkmcnt(0)\n"
"v_pk_add_f32 v[152:153], v[152:153], v[156:157]\n"
"v_pk_add_f32 v[154:155], v[154:155], v[158:159]\n"
"v_pk_add_f32 v[144:145], v[144:145], v[152:153]\n"
"v_pk_add_f32 v[146:147], v[146:147], v[154:155]\n"
"v_min_u32 v144, -2, v144\n"
"v_min_u32 v145, -2, v145\n"
"v_min_u32 v146, -2, v146\n"
"v_min_u32 v147, -2, v147\n"
"buffer_store_dwordx4 v[144:147], v226, s[52:55], s91 offen sc1\n"
"s_branch .Lk_oj_184\n"
".Lk_os_183:\n"
"s_sleep 14\n"
"s_nop 1\n"
".Lk_oj_184:\n"
"s_mov_b64 exec, -1\n"
"ds_read_b128 v[96:99], v242 offset:12288\n"
"ds_read_b128 v[100:103], v242 offset:13312\n"
"ds_read_b128 v[104:107], v242 offset:14336\n"
"ds_read_b128 v[108:111], v242 offset:15360\n"
"ds_read_b128 v[112:115], v242 offset:16384\n"
"ds_read_b128 v[116:119], v242 offset:17408\n"
"ds_read_b128 v[120:123], v242 offset:18432\n"
"ds_read_b128 v[124:127], v242 offset:19456\n"
"ds_read_b128 v[128:131], v242 offset:20480\n"
"ds_read_b128 v[132:135], v242 offset:21504\n"
"ds_read_b128 v[136:139], v242 offset:22528\n"
"ds_read_b128 v[140:143], v242 offset:23552\n"
"ds_read_b128 v[144:147], v242 offset:24576\n"
"ds_read_b128 v[148:151], v242 offset:25600\n"
"ds_read_b128 v[152:155], v242 offset:26624\n"
"s_waitcnt lgkmcnt(12)\n"
"ds_read_b128 v[156:159], v242 offset:27648\n"
"ds_read_b128 v[160:163], v242 offset:28672\n"
"ds_read_b128 v[164:167], v242 offset:29696\n"
"s_waitcnt lgkmcnt(12)\n"
"ds_read_b128 v[168:171], v242 offset:30720\n"
"ds_read_b128 v[172:175], v242 offset:31744\n"
"v_mov_b32 v208, 0\n"
"v_mov_b32 v209, 0\n"
"v_mov_b32 v212, 0\n"
"v_mov_b32 v213, 0\n"
"v_mov_b32 v254, 0\n"
"v_mov_b32 v255, 0\n"
"s_mov_b32 exec_lo, -1\n"
"s_mov_b32 exec_hi, 0xff\n"
"ds_read_b128 v[250:253], v226 offset:0\n"
"s_mov_b32 s90, 614400\n"
"s_add_u32 s56, s90, 0\n"
"s_add_u32 s57, s90, 5120\n"
"s_add_u32 s58, s90, 10240\n"
"s_add_u32 s59, s90, 15360\n"
"s_nop 1\n"
".Lk_g_185:\n"
"s_cmp_eq_u32 s96, 0\n"
"s_cbranch_scc1 .Lk_gok_186\n"
"s_sub_u32 s96, s96, 1\n"
"buffer_load_dwordx4 v[176:179], v226, s[52:55], s56 offen offset:0 sc1\n"
"buffer_load_dwordx4 v[180:183], v226, s[52:55], s56 offen offset:2560 sc1\n"
"buffer_load_dwordx4 v[184:187], v226, s[52:55], s57 offen offset:0 sc1\n"
"buffer_load_dwordx4 v[188:191], v226, s[52:55], s57 offen offset:2560 sc1\n"
"buffer_load_dwordx4 v[192:195], v226, s[52:55], s58 offen offset:0 sc1\n"
"buffer_load_dwordx4 v[196:199], v226, s[52:55], s58 offen offset:2560 sc1\n"
"buffer_load_dwordx4 v[200:203], v226, s[52:55], s59 offen offset:0 sc1\n"
"buffer_load_dwordx4 v[204:207], v226, s[52:55], s59 offen offset:2560 sc1\n"
"s_waitcnt vmcnt(6)\n"
"v_pk_add_f32 v[246:247], v[176:177], v[180:181]\n"
"v_pk_add_f32 v[248:249], v[178:179], v[182:183]\n"
"s_waitcnt vmcnt(5)\n"
"v_pk_add_f32 v[246:247], v[246:247], v[184:185]\n"
"v_pk_add_f32 v[248:249], v[248:249], v[186:187]\n"
"s_waitcnt vmcnt(4)\n"
"v_pk_add_f32 v[246:247], v[246:247], v[188:189]\n"
"v_pk_add_f32 v[248:249], v[248:249], v[190:191]\n"
"s_waitcnt vmcnt(3)\n"
"v_pk_add_f32 v[246:247], v[246:247], v[192:193]\n"
"v_pk_add_f32 v[248:249], v[248:249], v[194:195]\n"
"s_waitcnt vmcnt(2)\n"
"v_pk_add_f32 v[246:247], v[246:247], v[196:197]\n"
"v_pk_add_f32 v[248:249], v[248:249], v[198:199]\n"
"s_waitcnt vmcnt(1)\n"
"v_pk_add_f32 v[246:247], v[246:247], v[200:201]\n"
"v_pk_add_f32 v[248:249], v[248:249], v[202:203]\n"
"s_waitcnt vmcnt(0)\n"
"v_pk_add_f32 v[246:247], v[246:247], v[204:205]\n"
"v_pk_add_f32 v[248:249], v[248:249], v[206:207]\n"
"v_cmp_u_f32 vcc, v246, v247\n"
"v_cmp_u_f32 s[88:89], v248, v249\n"
"s_or_b64 s[88:89], s[88:89], vcc\n"
"s_cbranch_scc1 .Lk_g_185\n"
".Lk_gok_186:\n"
"v_cvt_pk_bf16_f32 v246, 0, v246\n"
"v_cvt_pk_bf16_f32 v247, 0, v247\n"
"v_cvt_pk_bf16_f32 v248, 0, v248\n"
"v_cvt_pk_bf16_f32 v249, 0, v249\n"
"s_waitcnt lgkmcnt(0)\n"
"v_pk_add_f32 v[250:251], v[250:251], v[246:247]\n"
"v_pk_add_f32 v[252:253], v[252:253], v[248:249]\n"
"v_cvt_pk_bf16_f32 v250, 0, v250\n"
"v_cvt_pk_bf16_f32 v251, 0, v251\n"
"v_cvt_pk_bf16_f32 v252, 0, v252\n"
"v_cvt_pk_bf16_f32 v253, 0, v253\n"
"v_add_f32 v254, v250, v251\n"
"v_mul_f32 v255, v250, v250\n"
"v_add_f32 v216, v252, v253\n"
"v_fmac_f32 v255, v251, v251\n"
"v_add_f32 v254, v254, v216\n"
"v_fmac_f32 v255, v252, v252\n"
"v_fmac_f32 v255, v253, v253\n"
"ds_write_b128 v226, v[250:253] offset:0\n"
"s_mov_b64 exec, -1\n"
"v_add_f32_dpp v254, v254, v254 row_ror:8 row_mask:0xf bank_mask:0xf\n"
"v_add_f32_dpp v255, v255, v255 row_ror:8 row_mask:0xf bank_mask:0xf\n"
"s_nop 0\n"
"v_add_f32_dpp v254, v254, v254 row_ror:4 row_mask:0xf bank_mask:0xf\n"
"v_add_f32_dpp v255, v255, v255 row_ror:4 row_mask:0xf bank_mask:0xf\n"
"s_nop 0\n"
"v_add_f32_dpp v254, v254, v254 row_ror:2 row_mask:0xf bank_mask:0xf\n"
"v_add_f32_dpp v255, v255, v255 row_ror:2 row_mask:0xf bank_mask:0xf\n"
"s_nop 0\n"
"v_add_f32_dpp v254, v254, v254 row_ror:1 row_mask:0xf bank_mask:0xf\n"
"v_add_f32_dpp v255, v255, v255 row_ror:1 row_mask:0xf bank_mask:0xf\n"
"s_nop 0\n"
"v_add_f32_dpp v254, v254, v254 row_bcast:15 row_mask:0xa bank_mask:0xf\n"
"v_add_f32_dpp v255, v255, v255 row_bcast:15 row_mask:0xa bank_mask:0xf\n"
"s_nop 0\n"
"v_add_f32_dpp v254, v254, v254 row_bcast:31 row_mask:0xc bank_mask:0xf\n"
"v_add_f32_dpp v255, v255, v255 row_bcast:31 row_mask:0xc bank_mask:0xf\n"
"s_mov_b32 exec_lo, 0\n"
"s_mov_b32 exec_hi, 0x80000000\n"
"ds_write_b64 v244, v[254:255] offset:18432\n"
"s_mov_b32 exec_lo, -1\n"
"s_mov_b32 exec_hi, 0xff\n"
"s_waitcnt lgkmcnt(0)\n"
"s_barrier\n"
"ds_read_b128 v[216:219], v243 offset:18432\n"
"ds_read_b128 v[220:223], v243 offset:18448\n"
"s_waitcnt lgkmcnt(1)\n"
"v_pk_add_f32 v[216:217], v[216:217], v[218:219]\n"
"s_waitcnt lgkmcnt(0)\n"
"v_pk_add_f32 v[220:221], v[220:221], v[222:223]\n"
"v_pk_add_f32 v[216:217], v[216:217], v[220:221]\n"
"v_mul_f32 v216, 0x3acccccd, v216\n"
"v_mul_f32 v217, 0x3acccccd, v217\n"
"v_cvt_pk_bf16_f32 v218, 0, v216\n"
"v_fma_f32 v217, -v216, v216, v217\n"
"v_sub_f32 v220, v250, v218\n"
"v_max_f32 v217, 0, v217\n"
"v_sub_f32 v221, v251, v218\n"
"v_cvt_pk_bf16_f32 v217, 0, v217\n"
"v_sub_f32 v222, v252, v218\n"
"v_add_f32 v217, 0x3727c5ac, v217\n"
"v_sub_f32 v223, v253, v218\n"
"v_cvt_pk_bf16_f32 v217, 0, v217\n"
"v_cvt_pk_bf16_f32 v220, 0, v220\n"
"v_sqrt_f32 v217, v217\n"
"v_cvt_pk_bf16_f32 v221, 0, v221\n"
"v_cvt_pk_bf16_f32 v217, 0, v217\n"
"v_cvt_pk_bf16_f32 v222, 0, v222\n"
"v_rcp_f32 v217, v217\n"
"v_cvt_pk_bf16_f32 v223, 0, v223\n"
"v_pk_mul_f32 v[220:221], v[220:221], v[216:217] op_sel:[0,1] op_sel_hi:[1,1]\n"
"v_pk_mul_f32 v[222:223], v[222:223], v[216:217] op_sel:[0,1] op_sel_hi:[1,1]\n"
"v_cvt_pk_bf16_f32 v220, v220, v221\n"
"v_cvt_pk_bf16_f32 v221, v222, v223\n"
"ds_write_b64 v227, v[220:221] offset:5120\n"
"s_mov_b64 exec, -1\n"
"s_cmp_ge_u32 s14, 27\n"
"s_cbranch_scc1 .Lk_ms_187\n"
"ds_read_b128 v[184:187], v228 offset:5120\n"
"ds_read_b128 v[188:191], v228 offset:5184\n"
"ds_read_b128 v[192:195], v228 offset:5248\n"
"ds_read_b128 v[196:199], v228 offset:5312\n"
"ds_read_b128 v[200:203], v228 offset:5376\n"
"s_mul_i32 s91, s14, 2560\n"
"s_add_u32 s91, s91, 696320\n"
"s_waitcnt lgkmcnt(4)\n"
"v_smfmac_f32_16x16x64_bf16 v[208:211], v[184:187], v[96:103], v225\n"
"v_smfmac_f32_16x16x64_bf16 v[212:215], v[184:187], v[104:111], v225\n"
"s_waitcnt lgkmcnt(3)\n"
"v_smfmac_f32_16x16x64_bf16 v[208:211], v[188:191], v[112:119], v225\n"
"v_smfmac_f32_16x16x64_bf16 v[212:215], v[188:191], v[120:127], v225\n"
"s_waitcnt lgkmcnt(2)\n"
"v_smfmac_f32_16x16x64_bf16 v[208:211], v[192:195], v[128:135], v225\n"
"v_smfmac_f32_16x16x64_bf16 v[212:215], v[192:195], v[136:143], v225\n"
"s_waitcnt lgkmcnt(1)\n"
"v_smfmac_f32_16x16x64_bf16 v[208:211], v[196:199], v[144:151], v225\n"
"v_smfmac_f32_16x16x64_bf16 v[212:215], v[196:199], v[152:159], v225\n"
"s_waitcnt lgkmcnt(0)\n"
"v_smfmac_f32_16x16x64_bf16 v[208:211], v[200:203], v[160:167], v225\n"
"v_smfmac_f32_16x16x64_bf16 v[212:215], v[200:203], v[168:175], v225\n"
"v_mov_b32 v184, 0\n"
"v_mov_b32 v185, 0\n"
"v_mov_b32 v188, 0\n"
"v_mov_b32 v189, 0\n"
"v_mov_b32 v192, 0\n"
"v_mov_b32 v193, 0\n"
"v_mov_b32 v196, 0\n"
"v_mov_b32 v197, 0\n"
"v_mov_b32 v200, 0\n"
"v_mov_b32 v201, 0\n"
"s_mov_b32 exec_lo, -1\n"
"s_mov_b32 exec_hi, 0\n"
"ds_write2_b64 v236, v[208:209], v[212:213] offset0:0 offset1:16\n"
"s_mov_b64 exec, -1\n"
"s_waitcnt lgkmcnt(0)\n"
"s_barrier\n"
"ds_read2st64_b64 v[216:219], v237 offset0:24 offset1:25\n"
"ds_read2st64_b64 v[220:223], v237 offset0:26 offset1:27\n"
"s_waitcnt lgkmcnt(1)\n"
"v_pk_add_f32 v[216:217], v[216:217], v[218:219]\n"
"s_waitcnt lgkmcnt(0)\n"
"v_pk_add_f32 v[220:221], v[220:221], v[222:223]\n"
"v_pk_add_f32 v[216:217], v[216:217], v[220:221]\n"
"v_cvt_pk_bf16_f32 v216, 0, v216\n"
"v_cvt_pk_bf16_f32 v217, 0, v217\n"
"v_mul_f32 v218, 0xbfb8aa3b, v216\n"
"v_mul_f32 v219, 0xbfb8aa3b, v217\n"
"v_exp_f32 v218, v218\n"
"v_exp_f32 v219, v219\n"
"v_add_f32 v218, 1.0, v218\n"
"v_add_f32 v219, 1.0, v219\n"
"v_rcp_f32 v218, v218\n"
"v_rcp_f32 v219, v219\n"
"s_nop 0\n"
"v_pk_mul_f32 v[216:217], v[216:217], v[218:219]\n"
"v_cvt_pk_bf16_f32 v216, v216, v217\n"
"ds_write_b32 v238, v216 offset:8704\n"
"ds_read_b128 v[176:179], v234 offset:8704\n"
"ds_read_b128 v[180:183], v234 offset:8768\n"
"s_waitcnt lgkmcnt(1)\n"
"v_smfmac_f32_16x16x64_bf16 v[184:187], v[176:179], a[160:167], v225\n"
"v_smfmac_f32_16x16x64_bf16 v[188:191], v[176:179], a[168:175], v225\n"
"v_smfmac_f32_16x16x64_bf16 v[192:195], v[176:179], a[176:183], v225\n"
"v_smfmac_f32_16x16x64_bf16 v[196:199], v[176:179], a[184:191], v225\n"
"v_smfmac_f32_16x16x64_bf16 v[200:203], v[176:179], a[192:199], v225\n"
"s_waitcnt lgkmcnt(0)\n"
"v_smfmac_f32_16x16x64_bf16 v[184:187], v[180:183], a[200:207], v225\n"
"v_smfmac_f32_16x16x64_bf16 v[188:191], v[180:183], a[208:215], v225\n"
"v_smfmac_f32_16x16x64_bf16 v[192:195], v[180:183], a[216:223], v225\n"
"v_smfmac_f32_16x16x64_bf16 v[196:199], v[180:183], a[224:231], v225\n"
"v_smfmac_f32_16x16x64_bf16 v[200:203], v[180:183], a[232:239], v225\n"
"s_mov_b32 exec_lo, 0xffff\n"
"s_mov_b32 exec_hi, 0\n"
"s_nop 1\n"
"v_min_u32 v184, -2, v184\n"
"v_min_u32 v185, -2, v185\n"
"buffer_store_dwordx2 v[184:185], v239, s[52:55], s91 offen offset:0 sc1\n"
"v_min_u32 v188, -2, v188\n"
"v_min_u32 v189, -2, v189\n"
"buffer_store_dwordx2 v[188:189], v239, s[52:55], s91 offen offset:128 sc1\n"
"v_min_u32 v192, -2, v192\n"
"v_min_u32 v193, -2, v193\n"
"buffer_store_dwordx2 v[192:193], v239, s[52:55], s91 offen offset:256 sc1\n"
"v_min_u32 v196, -2, v196\n"
"v_min_u32 v197, -2, v197\n"
"buffer_store_dwordx2 v[196:197], v239, s[52:55], s91 offen offset:384 sc1\n"
"v_min_u32 v200, -2, v200\n"
"v_min_u32 v201, -2, v201\n"
"buffer_store_dwordx2 v[200:201], v239, s[52:55], s91 offen offset:512 sc1\n"
"s_mov_b64 exec, -1\n"
"s_branch .Lk_mj_188\n"
".Lk_ms_187:\n"
"s_sleep 24\n"
"s_nop 1\n"
".Lk_mj_188:\n"
"s_mul_i32 s88, s15, 640\n"
"v_and_b32 v170, 15, v224\n"
"v_lshrrev_b32 v171, 4, v224\n"
"v_lshl_add_u32 v170, v170, 4, s88\n"
"v_mul_u32_u24 v168, 2560, v171\n"
"v_add_u32 v168, v168, v170\n"
"v_lshrrev_b32 v169, 3, v224\n"
"v_mul_u32_u24 v169, 2560, v169\n"
"v_and_b32 v254, 7, v224\n"
"v_lshl_add_u32 v169, v254, 4, v169\n"
"v_add_u32 v169, s88, v169\n"
"v_and_b32 v254, 1, v171\n"
"v_lshrrev_b32 v171, 1, v171\n"
"v_lshl_add_u32 v170, v254, 9, v170\n"
"v_lshl_add_u32 v170, v171, 8, v170\n"
"v_lshrrev_b32 v171, 1, v170\n"
"v_mov_b32 v254, 0\n"
"v_mov_b32 v255, 0\n"
"s_mov_b32 s56, 696320\n"
"s_mov_b32 s57, 706560\n"
"s_mov_b32 s58, 716800\n"
"s_mov_b32 s59, 727040\n"
"s_mov_b32 s60, 737280\n"
"s_mov_b32 s61, 747520\n"
"s_mov_b32 s62, 757760\n"
"v_mov_b64 v[108:109], 0\n"
"v_mov_b64 v[110:111], 0\n"
"v_mov_b64 v[136:137], 0\n"
"v_mov_b64 v[138:139], 0\n"
"v_mov_b64 v[164:165], 0\n"
"v_mov_b64 v[166:167], 0\n"
"s_mov_b32 exec_lo, 0x00ffffff\n"
"s_mov_b32 exec_hi, 0xffff\n"
"ds_read_b128 v[250:253], v170 offset:0\n"
"s_waitcnt lgkmcnt(0)\n"
"s_sleep 6\n"
"s_nop 1\n"
".Lk_g_189:\n"
"s_cmp_eq_u32 s96, 0\n"
"s_cbranch_scc1 .Lk_gok_190\n"
"s_sub_u32 s96, s96, 1\n"
"s_mov_b64 exec, -1\n"
"s_mov_b32 exec_lo, 0xffffff\n"
"s_mov_b32 exec_hi, 0x0\n"
"buffer_load_dwordx4 v[108:111], v169, s[52:55], s62 offen offset:512 sc1\n"
"s_mov_b32 exec_lo, 0xffffffff\n"
"s_mov_b32 exec_hi, 0xffff\n"
"buffer_load_dwordx4 v[136:139], v168, s[52:55], s62 offen offset:0 sc1\n"
"buffer_load_dwordx4 v[164:167], v168, s[52:55], s62 offen offset:256 sc1\n"
"s_mov_b64 exec, -1\n"
"buffer_load_dwordx4 v[96:99], v169, s[52:55], s56 offen offset:512 sc1\n"
"buffer_load_dwordx4 v[100:103], v169, s[52:55], s58 offen offset:512 sc1\n"
"buffer_load_dwordx4 v[104:107], v169, s[52:55], s60 offen offset:512 sc1\n"
"buffer_load_dwordx4 v[112:115], v168, s[52:55], s56 offen offset:0 sc1\n"
"buffer_load_dwordx4 v[140:143], v168, s[52:55], s56 offen offset:256 sc1\n"
"buffer_load_dwordx4 v[116:119], v168, s[52:55], s57 offen offset:0 sc1\n"
"buffer_load_dwordx4 v[144:147], v168, s[52:55], s57 offen offset:256 sc1\n"
"buffer_load_dwordx4 v[120:123], v168, s[52:55], s58 offen offset:0 sc1\n"
"buffer_load_dwordx4 v[148:151], v168, s[52:55], s58 offen offset:256 sc1\n"
"buffer_load_dwordx4 v[124:127], v168, s[52:55], s59 offen offset:0 sc1\n"
"buffer_load_dwordx4 v[152:155], v168, s[52:55], s59 offen offset:256 sc1\n"
"buffer_load_dwordx4 v[128:131], v168, s[52:55], s60 offen offset:0 sc1\n"
"buffer_load_dwordx4 v[156:159], v168, s[52:55], s60 offen offset:256 sc1\n"
"buffer_load_dwordx4 v[132:135], v168, s[52:55], s61 offen offset:0 sc1\n"
"buffer_load_dwordx4 v[160:163], v168, s[52:55], s61 offen offset:256 sc1\n"
"s_waitcnt vmcnt(14)\n"
"v_pk_add_f32 v[172:173], v[108:109], v[96:97]\n"
"v_pk_add_f32 v[174:175], v[110:111], v[98:99]\n"
"s_waitcnt vmcnt(13)\n"
"v_pk_add_f32 v[172:173], v[172:173], v[100:101]\n"
"v_pk_add_f32 v[174:175], v[174:175], v[102:103]\n"
"s_waitcnt vmcnt(12)\n"
"v_pk_add_f32 v[172:173], v[172:173], v[104:105]\n"
"v_pk_add_f32 v[174:175], v[174:175], v[106:107]\n"
"s_nop 0\n"
"v_add_f32_dpp v184, v172, v172 row_ror:8 row_mask:0xf bank_mask:0xf\n"
"v_add_f32_dpp v185, v173, v173 row_ror:8 row_mask:0xf bank_mask:0xf\n"
"v_add_f32_dpp v186, v174, v174 row_ror:8 row_mask:0xf bank_mask:0xf\n"
"v_add_f32_dpp v187, v175, v175 row_ror:8 row_mask:0xf bank_mask:0xf\n"
"v_permlane32_swap_b32 v184, v172\n"
"v_permlane32_swap_b32 v185, v173\n"
"v_permlane32_swap_b32 v186, v174\n"
"v_permlane32_swap_b32 v187, v175\n"
"v_pk_add_f32 v[172:173], v[184:185], v[172:173]\n"
"v_pk_add_f32 v[174:175], v[186:187], v[174:175]\n"
"s_waitcnt vmcnt(11)\n"
"v_pk_add_f32 v[176:177], v[136:137], v[112:113]\n"
"v_pk_add_f32 v[178:179], v[138:139], v[114:115]\n"
"s_waitcnt vmcnt(10)\n"
"v_pk_add_f32 v[180:181], v[164:165], v[140:141]\n"
"v_pk_add_f32 v[182:183], v[166:167], v[142:143]\n"
"s_waitcnt vmcnt(9)\n"
"v_pk_add_f32 v[176:177], v[176:177], v[116:117]\n"
"v_pk_add_f32 v[178:179], v[178:179], v[118:119]\n"
"s_waitcnt vmcnt(8)\n"
"v_pk_add_f32 v[180:181], v[180:181], v[144:145]\n"
"v_pk_add_f32 v[182:183], v[182:183], v[146:147]\n"
"s_waitcnt vmcnt(7)\n"
"v_pk_add_f32 v[176:177], v[176:177], v[120:121]\n"
"v_pk_add_f32 v[178:179], v[178:179], v[122:123]\n"
"s_waitcnt vmcnt(6)\n"
"v_pk_add_f32 v[180:181], v[180:181], v[148:149]\n"
"v_pk_add_f32 v[182:183], v[182:183], v[150:151]\n"
"s_waitcnt vmcnt(5)\n"
"v_pk_add_f32 v[176:177], v[176:177], v[124:125]\n"
"v_pk_add_f32 v[178:179], v[178:179], v[126:127]\n"
"s_waitcnt vmcnt(4)\n"
"v_pk_add_f32 v[180:181], v[180:181], v[152:153]\n"
"v_pk_add_f32 v[182:183], v[182:183], v[154:155]\n"
"s_waitcnt vmcnt(3)\n"
"v_pk_add_f32 v[176:177], v[176:177], v[128:129]\n"
"v_pk_add_f32 v[178:179], v[178:179], v[130:131]\n"
"s_waitcnt vmcnt(2)\n"
"v_pk_add_f32 v[180:181], v[180:181], v[156:157]\n"
"v_pk_add_f32 v[182:183], v[182:183], v[158:159]\n"
"s_waitcnt vmcnt(1)\n"
"v_pk_add_f32 v[176:177], v[176:177], v[132:133]\n"
"v_pk_add_f32 v[178:179], v[178:179], v[134:135]\n"
"s_waitcnt vmcnt(0)\n"
"v_pk_add_f32 v[180:181], v[180:181], v[160:161]\n"
"v_pk_add_f32 v[182:183], v[182:183], v[162:163]\n"
"s_nop 0\n"
"v_permlane32_swap_b32 v176, v180\n"
"v_permlane32_swap_b32 v177, v181\n"
"v_permlane32_swap_b32 v178, v182\n"
"v_permlane32_swap_b32 v179, v183\n"
"v_pk_add_f32 v[176:177], v[176:177], v[180:181]\n"
"v_pk_add_f32 v[178:179], v[178:179], v[182:183]\n"
"s_nop 0\n"
"v_permlane16_swap_b32 v176, v172\n"
"v_permlane16_swap_b32 v177, v173\n"
"v_permlane16_swap_b32 v178, v174\n"
"v_permlane16_swap_b32 v179, v175\n"
"s_mov_b32 exec_lo, 0x00ffffff\n"
"s_mov_b32 exec_hi, 0xffff\n"
"v_pk_add_f32 v[246:247], v[176:177], v[172:173]\n"
"v_pk_add_f32 v[248:249], v[178:179], v[174:175]\n"
"v_cmp_u_f32 vcc, v246, v247\n"
"v_cmp_u_f32 s[88:89], v248, v249\n"
"s_or_b64 s[88:89], s[88:89], vcc\n"
"s_cbranch_scc1 .Lk_g_189\n"
".Lk_gok_190:\n"
"v_cvt_pk_bf16_f32 v246, 0, v246\n"
"v_cvt_pk_bf16_f32 v247, 0, v247\n"
"v_cvt_pk_bf16_f32 v248, 0, v248\n"
"v_cvt_pk_bf16_f32 v249, 0, v249\n"
"v_pk_add_f32 v[250:251], v[250:251], v[246:247]\n"
"v_pk_add_f32 v[252:253], v[252:253], v[248:249]\n"
"v_cvt_pk_bf16_f32 v250, 0, v250\n"
"v_cvt_pk_bf16_f32 v251, 0, v251\n"
"v_cvt_pk_bf16_f32 v252, 0, v252\n"
"v_cvt_pk_bf16_f32 v253, 0, v253\n"
"v_add_f32 v254, v250, v251\n"
"v_mul_f32 v255, v250, v250\n"
"v_add_f32 v212, v252, v253\n"
"v_fmac_f32 v255, v251, v251\n"
"v_add_f32 v254, v254, v212\n"
"v_fmac_f32 v255, v252, v252\n"
"v_fmac_f32 v255, v253, v253\n"
"ds_write_b128 v170, v[250:253] offset:0\n"
"s_mov_b64 exec, -1\n"
"v_add_f32_dpp v254, v254, v254 row_ror:8 row_mask:0xf bank_mask:0xf\n"
"v_add_f32_dpp v255, v255, v255 row_ror:8 row_mask:0xf bank_mask:0xf\n"
"s_nop 0\n"
"v_add_f32_dpp v254, v254, v254 row_ror:4 row_mask:0xf bank_mask:0xf\n"
"v_add_f32_dpp v255, v255, v255 row_ror:4 row_mask:0xf bank_mask:0xf\n"
"s_nop 0\n"
"v_add_f32_dpp v254, v254, v254 row_ror:2 row_mask:0xf bank_mask:0xf\n"
"v_add_f32_dpp v255, v255, v255 row_ror:2 row_mask:0xf bank_mask:0xf\n"
"s_nop 0\n"
"v_add_f32_dpp v254, v254, v254 row_ror:1 row_mask:0xf bank_mask:0xf\n"
"v_add_f32_dpp v255, v255, v255 row_ror:1 row_mask:0xf bank_mask:0xf\n"
"s_nop 0\n"
"v_add_f32_dpp v254, v254, v254 row_bcast:15 row_mask:0xa bank_mask:0xf\n"
"v_add_f32_dpp v255, v255, v255 row_bcast:15 row_mask:0xa bank_mask:0xf\n"
"s_nop 0\n"
"v_add_f32_dpp v254, v254, v254 row_bcast:31 row_mask:0xc bank_mask:0xf\n"
"v_add_f32_dpp v255, v255, v255 row_bcast:31 row_mask:0xc bank_mask:0xf\n"
"s_mov_b32 exec_lo, 0\n"
"s_mov_b32 exec_hi, 0x80000000\n"
"ds_write_b64 v244, v[254:255] offset:18464\n"
"s_mov_b32 exec_lo, 0x00ffffff\n"
"s_mov_b32 exec_hi, 0xffff\n"
"s_waitcnt lgkmcnt(0)\n"
"s_barrier\n"
"ds_read_b128 v[212:215], v243 offset:18464\n"
"ds_read_b128 v[216:219], v243 offset:18480\n"
"s_waitcnt lgkmcnt(1)\n"
"v_pk_add_f32 v[212:213], v[212:213], v[214:215]\n"
"s_waitcnt lgkmcnt(0)\n"
"v_pk_add_f32 v[216:217], v[216:217], v[218:219]\n"
"v_pk_add_f32 v[212:213], v[212:213], v[216:217]\n"
"v_mul_f32 v212, 0x3acccccd, v212\n"
"v_mul_f32 v213, 0x3acccccd, v213\n"
"v_cvt_pk_bf16_f32 v214, 0, v212\n"
"v_fma_f32 v213, -v212, v212, v213\n"
"v_sub_f32 v216, v250, v214\n"
"v_max_f32 v213, 0, v213\n"
"v_sub_f32 v217, v251, v214\n"
"v_cvt_pk_bf16_f32 v213, 0, v213\n"
"v_sub_f32 v218, v252, v214\n"
"v_add_f32 v213, 0x3727c5ac, v213\n"
"v_sub_f32 v219, v253, v214\n"
"v_cvt_pk_bf16_f32 v213, 0, v213\n"
"v_cvt_pk_bf16_f32 v216, 0, v216\n"
"v_sqrt_f32 v213, v213\n"
"v_cvt_pk_bf16_f32 v217, 0, v217\n"
"v_cvt_pk_bf16_f32 v213, 0, v213\n"
"v_cvt_pk_bf16_f32 v218, 0, v218\n"
"v_rcp_f32 v213, v213\n"
"v_cvt_pk_bf16_f32 v219, 0, v219\n"
"v_pk_mul_f32 v[216:217], v[216:217], v[212:213] op_sel:[0,1] op_sel_hi:[1,1]\n"
"v_pk_mul_f32 v[218:219], v[218:219], v[212:213] op_sel:[0,1] op_sel_hi:[1,1]\n"
"v_cvt_pk_bf16_f32 v216, v216, v217\n"
"v_cvt_pk_bf16_f32 v217, v218, v219\n"
"ds_write_b64 v171, v[216:217] offset:5120\n"
"s_mov_b64 exec, -1\n"
"ds_read_b128 v[104:107], v228 offset:5120\n"
"ds_read_b128 v[108:111], v228 offset:5184\n"
"ds_read_b128 v[112:115], v228 offset:5248\n"
"ds_read_b128 v[116:119], v228 offset:5312\n"
"ds_read_b128 v[120:123], v228 offset:5376\n"
"s_lshl_b32 s91, s14, 8\n"
"s_add_u32 s91, s91, 24576\n"
"v_mov_b32 v204, 0\n"
"v_mov_b32 v205, 0\n"
"v_mov_b32 v208, 0\n"
"v_mov_b32 v209, 0\n"
"v_mov_b32 v212, 0\n"
"v_mov_b32 v213, 0\n"
"v_mov_b32 v216, 0\n"
"v_mov_b32 v217, 0\n"
"s_waitcnt lgkmcnt(4)\n"
"v_smfmac_f32_16x16x64_bf16 v[204:207], v[104:107], a[0:7], v225\n"
"v_smfmac_f32_16x16x64_bf16 v[208:211], v[104:107], a[8:15], v225\n"
"v_smfmac_f32_16x16x64_bf16 v[212:215], v[104:107], a[16:23], v225\n"
"v_smfmac_f32_16x16x64_bf16 v[216:219], v[104:107], a[24:31], v225\n"
"s_waitcnt lgkmcnt(3)\n"
"v_smfmac_f32_16x16x64_bf16 v[204:207], v[108:111], a[32:39], v225\n"
"v_smfmac_f32_16x16x64_bf16 v[208:211], v[108:111], a[40:47], v225\n"
"v_smfmac_f32_16x16x64_bf16 v[212:215], v[108:111], a[48:55], v225\n"
"v_smfmac_f32_16x16x64_bf16 v[216:219], v[108:111], a[56:63], v225\n"
"s_waitcnt lgkmcnt(2)\n"
"v_smfmac_f32_16x16x64_bf16 v[204:207], v[112:115], a[64:71], v225\n"
"v_smfmac_f32_16x16x64_bf16 v[208:211], v[112:115], a[72:79], v225\n"
"v_smfmac_f32_16x16x64_bf16 v[212:215], v[112:115], a[80:87], v225\n"
"v_smfmac_f32_16x16x64_bf16 v[216:219], v[112:115], a[88:95], v225\n"
"s_waitcnt lgkmcnt(1)\n"
"v_smfmac_f32_16x16x64_bf16 v[204:207], v[116:119], a[96:103], v225\n"
"v_smfmac_f32_16x16x64_bf16 v[208:211], v[116:119], a[104:111], v225\n"
"v_smfmac_f32_16x16x64_bf16 v[212:215], v[116:119], a[112:119], v225\n"
"v_smfmac_f32_16x16x64_bf16 v[216:219], v[116:119], a[120:127], v225\n"
"s_waitcnt lgkmcnt(0)\n"
"v_smfmac_f32_16x16x64_bf16 v[204:207], v[120:123], a[128:135], v225\n"
"v_smfmac_f32_16x16x64_bf16 v[208:211], v[120:123], a[136:143], v225\n"
"v_smfmac_f32_16x16x64_bf16 v[212:215], v[120:123], a[144:151], v225\n"
"v_smfmac_f32_16x16x64_bf16 v[216:219], v[120:123], a[152:159], v225\n"
"s_mov_b32 exec_lo, -1\n"
"s_mov_b32 exec_hi, 0\n"
"s_nop 3\n"
"ds_write2_b64 v240, v[204:205], v[208:209] offset0:0 offset1:2\n"
"s_nop 0\n"
"ds_write2_b64 v240, v[212:213], v[216:217] offset0:1 offset1:3\n"
"s_mov_b64 exec, -1\n"
"s_waitcnt lgkmcnt(0)\n"
"s_barrier\n"
"s_cmp_lg_u32 s15, 0\n"
"s_cbranch_scc1 .Lk_qs_191\n"
"ds_read_b128 v[128:131], v226 offset:14336\n"
"ds_read_b128 v[132:135], v226 offset:15360\n"
"ds_read_b128 v[136:139], v226 offset:16384\n"
"ds_read_b128 v[140:143], v226 offset:17408\n"
"s_waitcnt lgkmcnt(2)\n"
"v_pk_add_f32 v[128:129], v[128:129], v[132:133]\n"
"v_pk_add_f32 v[130:131], v[130:131], v[134:135]\n"
"s_waitcnt lgkmcnt(0)\n"
"v_pk_add_f32 v[136:137], v[136:137], v[140:141]\n"
"v_pk_add_f32 v[138:139], v[138:139], v[142:143]\n"
"v_pk_add_f32 v[128:129], v[128:129], v[136:137]\n"
"v_pk_add_f32 v[130:131], v[130:131], v[138:139]\n"
"v_cvt_pk_bf16_f32 v128, v128, v129\n"
"v_cvt_pk_bf16_f32 v129, v130, v131\n"
"v_min_u32 v128, -2, v128\n"
"v_min_u32 v129, -2, v129\n"
"s_mov_b32 exec_lo, -1\n"
"s_mov_b32 exec_hi, 0\n"
"buffer_store_dwordx2 v[128:129], v241, s[52:55], s91 offen sc1\n"
"s_mov_b64 exec, -1\n"
"s_nop 1\n"
".Lk_qs_191:\n"
"ds_read_b128 v[96:99], v242 offset:0\n"
"ds_read_b128 v[100:103], v242 offset:1024\n"
"ds_read_b128 v[104:107], v242 offset:2048\n"
"ds_read_b128 v[108:111], v242 offset:3072\n"
"ds_read_b128 v[112:115], v242 offset:4096\n"
"ds_read_b128 v[116:119], v242 offset:5120\n"
"ds_read_b128 v[120:123], v242 offset:6144\n"
"ds_read_b128 v[124:127], v242 offset:7168\n"
"ds_read_b128 v[128:131], v242 offset:8192\n"
"ds_read_b128 v[132:135], v242 offset:9216\n"
"ds_read_b128 v[136:139], v242 offset:10240\n"
"ds_read_b128 v[140:143], v242 offset:11264\n"
"v_mov_b32 v188, 0\n"
"v_mov_b32 v189, 0\n"
"v_mov_b32 v192, 0\n"
"v_mov_b32 v193, 0\n"
"v_mov_b32 v196, 0\n"
"v_mov_b32 v197, 0\n"
"v_mov_b32 v200, 0\n"
"v_mov_b32 v201, 0\n"
"v_mov_b32 v204, 0\n"
"v_mov_b32 v205, 0\n"
"s_mov_b32 s90, 184320\n"
"s_mul_i32 s91, s13, 2560\n"
"s_mul_i32 s92, s34, 640\n"
"s_add_u32 s91, s91, s92\n"
"s_add_u32 s91, s91, 634880\n"
"s_waitcnt lgkmcnt(0)\n"
"s_nop 1\n"
"s_branch .Lk_o2_194\n"
".Lk_o_192:\n"
".Lk_o2_194:\n"
"s_cmp_eq_u32 s96, 0\n"
"s_cbranch_scc1 .Lk_ook_193\n"
"s_sub_u32 s96, s96, 1\n"
"buffer_load_dwordx2 v[160:161], v231, s[52:55], s90 offen sc1\n"
"buffer_load_dwordx4 v[144:147], v229, s[52:55], s90 offen offset:0 sc1\n"
"buffer_load_dwordx4 v[148:151], v229, s[52:55], s90 offen offset:1280 sc1\n"
"buffer_load_dwordx4 v[152:155], v229, s[52:55], s90 offen offset:2560 sc1\n"
"buffer_load_dwordx4 v[156:159], v230, s[52:55], s90 offen offset:3840 sc1\n"
"s_waitcnt vmcnt(2)\n"
"v_max3_u32 v246, v160, v144, v148\n"
"s_waitcnt vmcnt(0)\n"
"v_max3_u32 v246, v246, v152, v156\n"
"v_cmp_eq_u32 vcc, -1, v246\n"
"s_cbranch_vccnz .Lk_o_192\n"
"v_max_f32_dpp v246, v160, v160 quad_perm:[1,0,3,2] row_mask:0xf bank_mask:0xf\n"
"s_nop 1\n"
"v_max_f32_dpp v246, v246, v246 quad_perm:[2,3,0,1] row_mask:0xf bank_mask:0xf\n"
"s_nop 1\n"
"v_max_f32_dpp v246, v246, v246 row_half_mirror row_mask:0xf bank_mask:0xf\n"
"v_sub_f32 v246, v160, v246\n"
"v_exp_f32 v246, v246\n"
"s_nop 0\n"
"v_cndmask_b32 v246, v246, 0, s[76:77]\n"
"v_mul_f32 v247, v246, v161\n"
"s_nop 1\n"
"v_add_f32_dpp v247, v247, v247 quad_perm:[1,0,3,2] row_mask:0xf bank_mask:0xf\n"
"s_nop 1\n"
"v_add_f32_dpp v247, v247, v247 quad_perm:[2,3,0,1] row_mask:0xf bank_mask:0xf\n"
"s_nop 1\n"
"v_add_f32_dpp v247, v247, v247 row_half_mirror row_mask:0xf bank_mask:0xf\n"
"v_rcp_f32 v247, v247\n"
"s_nop 0\n"
"v_mul_f32 v246, v246, v247\n"
"s_nop 1\n"
"v_mov_b32_dpp v162, v246 row_newbcast:0 row_mask:0xf bank_mask:0xf\n"
"v_mov_b32_dpp v163, v246 row_newbcast:2 row_mask:0xf bank_mask:0xf\n"
"v_mov_b32_dpp v164, v246 row_newbcast:4 row_mask:0xf bank_mask:0xf\n"
"v_mov_b32_dpp v165, v246 row_newbcast:6 row_mask:0xf bank_mask:0xf\n"
"v_pk_mul_f32 v[166:167], v[144:145], v[162:163] op_sel:[0,0] op_sel_hi:[1,0]\n"
"v_pk_mul_f32 v[168:169], v[146:147], v[162:163] op_sel:[0,0] op_sel_hi:[1,0]\n"
"v_pk_fma_f32 v[166:167], v[148:149], v[162:163], v[166:167] op_sel:[0,1,0] op_sel_hi:[1,1,1]\n"
"v_pk_fma_f32 v[168:169], v[150:151], v[162:163], v[168:169] op_sel:[0,1,0] op_sel_hi:[1,1,1]\n"
"v_pk_fma_f32 v[166:167], v[152:153], v[164:165], v[166:167] op_sel:[0,0,0] op_sel_hi:[1,0,1]\n"
"v_pk_fma_f32 v[168:169], v[154:155], v[164:165], v[168:169] op_sel:[0,0,0] op_sel_hi:[1,0,1]\n"
"v_pk_fma_f32 v[166:167], v[156:157], v[164:165], v[166:167] op_sel:[0,1,0] op_sel_hi:[1,1,1]\n"
"v_pk_fma_f32 v[168:169], v[158:159], v[164:165], v[168:169] op_sel:[0,1,0] op_sel_hi:[1,1,1]\n"
"s_nop 1\n"
"v_permlane32_swap_b32 v166, v168\n"
"v_permlane32_swap_b32 v167, v169\n"
"v_pk_add_f32 v[166:167], v[166:167], v[168:169]\n"
"v_cmp_u_f32 vcc, v166, v167\n"
"v_cvt_pk_bf16_f32 v166, v166, v167\n"
"s_cbranch_vccnz .Lk_o_192\n"
".Lk_ook_193:\n"
"ds_write_b32 v232, v166 offset:7680\n"
"ds_read_b128 v[172:175], v233 offset:7680\n"
"ds_read_b128 v[176:179], v233 offset:7744\n"
"ds_read_b128 v[180:183], v233 offset:7808\n"
"ds_read_b128 v[184:187], v233 offset:7872\n"
"s_waitcnt lgkmcnt(3)\n"
"v_smfmac_f32_16x16x64_bf16 v[188:191], v[172:175], a[240:247], v225\n"
"v_smfmac_f32_16x16x64_bf16 v[192:195], v[172:175], a[248:255], v225\n"
"v_smfmac_f32_16x16x64_bf16 v[196:199], v[172:175], v[0:7], v225\n"
"v_smfmac_f32_16x16x64_bf16 v[200:203], v[172:175], v[8:15], v225\n"
"v_smfmac_f32_16x16x64_bf16 v[204:207], v[172:175], v[16:23], v225\n"
"s_waitcnt lgkmcnt(2)\n"
"v_smfmac_f32_16x16x64_bf16 v[188:191], v[176:179], v[24:31], v225\n"
"v_smfmac_f32_16x16x64_bf16 v[192:195], v[176:179], v[32:39], v225\n"
"v_smfmac_f32_16x16x64_bf16 v[196:199], v[176:179], v[40:47], v225\n"
"v_smfmac_f32_16x16x64_bf16 v[200:203], v[176:179], v[48:55], v225\n"
"v_smfmac_f32_16x16x64_bf16 v[204:207], v[176:179], v[56:63], v225\n"
"s_waitcnt lgkmcnt(1)\n"
"v_smfmac_f32_16x16x64_bf16 v[188:191], v[180:183], v[64:71], v225\n"
"v_smfmac_f32_16x16x64_bf16 v[192:195], v[180:183], v[72:79], v225\n"
"v_smfmac_f32_16x16x64_bf16 v[196:199], v[180:183], v[80:87], v225\n"
"v_smfmac_f32_16x16x64_bf16 v[200:203], v[180:183], v[88:95], v225\n"
"v_smfmac_f32_16x16x64_bf16 v[204:207], v[180:183], v[96:103], v225\n"
"s_waitcnt lgkmcnt(0)\n"
"v_smfmac_f32_16x16x64_bf16 v[188:191], v[184:187], v[104:111], v225\n"
"v_smfmac_f32_16x16x64_bf16 v[192:195], v[184:187], v[112:119], v225\n"
"v_smfmac_f32_16x16x64_bf16 v[196:199], v[184:187], v[120:127], v225\n"
"v_smfmac_f32_16x16x64_bf16 v[200:203], v[184:187], v[128:135], v225\n"
"v_smfmac_f32_16x16x64_bf16 v[204:207], v[184:187], v[136:143], v225\n"
"s_mov_b64 exec, 0xffff\n"
"s_nop 3\n"
"ds_write2_b64 v235, v[188:189], v[192:193] offset0:0 offset1:16\n"
"s_nop 0\n"
"ds_write2_b64 v235, v[196:197], v[200:201] offset0:32 offset1:48\n"
"ds_write_b64 v235, v[204:205] offset:512\n"
"s_mov_b32 exec_lo, -1\n"
"s_mov_b32 exec_hi, 0xff\n"
"s_waitcnt lgkmcnt(0)\n"
"s_barrier\n"
"s_cmp_lg_u32 s15, 0\n"
"s_cbranch_scc1 .Lk_os_195\n"
"ds_read_b128 v[144:147], v226 offset:9728\n"
"ds_read_b128 v[148:151], v226 offset:10368\n"
"ds_read_b128 v[152:155], v226 offset:11008\n"
"ds_read_b128 v[156:159], v226 offset:11648\n"
"s_waitcnt lgkmcnt(2)\n"
"v_pk_add_f32 v[144:145], v[144:145], v[148:149]\n"
"v_pk_add_f32 v[146:147], v[146:147], v[150:151]\n"
"s_waitcnt lgkmcnt(0)\n"
"v_pk_add_f32 v[152:153], v[152:153], v[156:157]\n"
"v_pk_add_f32 v[154:155], v[154:155], v[158:159]\n"
"v_pk_add_f32 v[144:145], v[144:145], v[152:153]\n"
"v_pk_add_f32 v[146:147], v[146:147], v[154:155]\n"
"v_min_u32 v144, -2, v144\n"
"v_min_u32 v145, -2, v145\n"
"v_min_u32 v146, -2, v146\n"
"v_min_u32 v147, -2, v147\n"
"buffer_store_dwordx4 v[144:147], v226, s[52:55], s91 offen sc1\n"
"s_branch .Lk_oj_196\n"
".Lk_os_195:\n"
"s_sleep 14\n"
"s_nop 1\n"
".Lk_oj_196:\n"
"s_mov_b64 exec, -1\n"
"ds_read_b128 v[96:99], v242 offset:12288\n"
"ds_read_b128 v[100:103], v242 offset:13312\n"
"ds_read_b128 v[104:107], v242 offset:14336\n"
"ds_read_b128 v[108:111], v242 offset:15360\n"
"ds_read_b128 v[112:115], v242 offset:16384\n"
"ds_read_b128 v[116:119], v242 offset:17408\n"
"ds_read_b128 v[120:123], v242 offset:18432\n"
"ds_read_b128 v[124:127], v242 offset:19456\n"
"ds_read_b128 v[128:131], v242 offset:20480\n"
"ds_read_b128 v[132:135], v242 offset:21504\n"
"ds_read_b128 v[136:139], v242 offset:22528\n"
"ds_read_b128 v[140:143], v242 offset:23552\n"
"ds_read_b128 v[144:147], v242 offset:24576\n"
"ds_read_b128 v[148:151], v242 offset:25600\n"
"ds_read_b128 v[152:155], v242 offset:26624\n"
"s_waitcnt lgkmcnt(12)\n"
"ds_read_b128 v[156:159], v242 offset:27648\n"
"ds_read_b128 v[160:163], v242 offset:28672\n"
"ds_read_b128 v[164:167], v242 offset:29696\n"
"s_waitcnt lgkmcnt(12)\n"
"ds_read_b128 v[168:171], v242 offset:30720\n"
"ds_read_b128 v[172:175], v242 offset:31744\n"
"v_mov_b32 v208, 0\n"
"v_mov_b32 v209, 0\n"
"v_mov_b32 v212, 0\n"
"v_mov_b32 v213, 0\n"
"v_mov_b32 v254, 0\n"
"v_mov_b32 v255, 0\n"
"s_mov_b32 exec_lo, -1\n"
"s_mov_b32 exec_hi, 0xff\n"
"ds_read_b128 v[250:253], v226 offset:2560\n"
"s_mov_b32 s90, 634880\n"
"s_add_u32 s56, s90, 0\n"
"s_add_u32 s57, s90, 5120\n"
"s_add_u32 s58, s90, 10240\n"
"s_add_u32 s59, s90, 15360\n"
"s_nop 1\n"
".Lk_g_197:\n"
"s_cmp_eq_u32 s96, 0\n"
"s_cbranch_scc1 .Lk_gok_198\n"
"s_sub_u32 s96, s96, 1\n"
"buffer_load_dwordx4 v[176:179], v226, s[52:55], s56 offen offset:0 sc1\n"
"buffer_load_dwordx4 v[180:183], v226, s[52:55], s56 offen offset:2560 sc1\n"
"buffer_load_dwordx4 v[184:187], v226, s[52:55], s57 offen offset:0 sc1\n"
"buffer_load_dwordx4 v[188:191], v226, s[52:55], s57 offen offset:2560 sc1\n"
"buffer_load_dwordx4 v[192:195], v226, s[52:55], s58 offen offset:0 sc1\n"
"buffer_load_dwordx4 v[196:199], v226, s[52:55], s58 offen offset:2560 sc1\n"
"buffer_load_dwordx4 v[200:203], v226, s[52:55], s59 offen offset:0 sc1\n"
"buffer_load_dwordx4 v[204:207], v226, s[52:55], s59 offen offset:2560 sc1\n"
"s_waitcnt vmcnt(6)\n"
"v_pk_add_f32 v[246:247], v[176:177], v[180:181]\n"
"v_pk_add_f32 v[248:249], v[178:179], v[182:183]\n"
"s_waitcnt vmcnt(5)\n"
"v_pk_add_f32 v[246:247], v[246:247], v[184:185]\n"
"v_pk_add_f32 v[248:249], v[248:249], v[186:187]\n"
"s_waitcnt vmcnt(4)\n"
"v_pk_add_f32 v[246:247], v[246:247], v[188:189]\n"
"v_pk_add_f32 v[248:249], v[248:249], v[190:191]\n"
"s_waitcnt vmcnt(3)\n"
"v_pk_add_f32 v[246:247], v[246:247], v[192:193]\n"
"v_pk_add_f32 v[248:249], v[248:249], v[194:195]\n"
"s_waitcnt vmcnt(2)\n"
"v_pk_add_f32 v[246:247], v[246:247], v[196:197]\n"
"v_pk_add_f32 v[248:249], v[248:249], v[198:199]\n"
"s_waitcnt vmcnt(1)\n"
"v_pk_add_f32 v[246:247], v[246:247], v[200:201]\n"
"v_pk_add_f32 v[248:249], v[248:249], v[202:203]\n"
"s_waitcnt vmcnt(0)\n"
"v_pk_add_f32 v[246:247], v[246:247], v[204:205]\n"
"v_pk_add_f32 v[248:249], v[248:249], v[206:207]\n"
"v_cmp_u_f32 vcc, v246, v247\n"
"v_cmp_u_f32 s[88:89], v248, v249\n"
"s_or_b64 s[88:89], s[88:89], vcc\n"
"s_cbranch_scc1 .Lk_g_197\n"
".Lk_gok_198:\n"
"v_cvt_pk_bf16_f32 v246, 0, v246\n"
"v_cvt_pk_bf16_f32 v247, 0, v247\n"
"v_cvt_pk_bf16_f32 v248, 0, v248\n"
"v_cvt_pk_bf16_f32 v249, 0, v249\n"
"s_waitcnt lgkmcnt(0)\n"
"v_pk_add_f32 v[250:251], v[250:251], v[246:247]\n"
"v_pk_add_f32 v[252:253], v[252:253], v[248:249]\n"
"v_cvt_pk_bf16_f32 v250, 0, v250\n"
"v_cvt_pk_bf16_f32 v251, 0, v251\n"
"v_cvt_pk_bf16_f32 v252, 0, v252\n"
"v_cvt_pk_bf16_f32 v253, 0, v253\n"
"v_add_f32 v254, v250, v251\n"
"v_mul_f32 v255, v250, v250\n"
"v_add_f32 v216, v252, v253\n"
"v_fmac_f32 v255, v251, v251\n"
"v_add_f32 v254, v254, v216\n"
"v_fmac_f32 v255, v252, v252\n"
"v_fmac_f32 v255, v253, v253\n"
"ds_write_b128 v226, v[250:253] offset:2560\n"
"s_mov_b64 exec, -1\n"
"v_add_f32_dpp v254, v254, v254 row_ror:8 row_mask:0xf bank_mask:0xf\n"
"v_add_f32_dpp v255, v255, v255 row_ror:8 row_mask:0xf bank_mask:0xf\n"
"s_nop 0\n"
"v_add_f32_dpp v254, v254, v254 row_ror:4 row_mask:0xf bank_mask:0xf\n"
"v_add_f32_dpp v255, v255, v255 row_ror:4 row_mask:0xf bank_mask:0xf\n"
"s_nop 0\n"
"v_add_f32_dpp v254, v254, v254 row_ror:2 row_mask:0xf bank_mask:0xf\n"
"v_add_f32_dpp v255, v255, v255 row_ror:2 row_mask:0xf bank_mask:0xf\n"
"s_nop 0\n"
"v_add_f32_dpp v254, v254, v254 row_ror:1 row_mask:0xf bank_mask:0xf\n"
"v_add_f32_dpp v255, v255, v255 row_ror:1 row_mask:0xf bank_mask:0xf\n"
"s_nop 0\n"
"v_add_f32_dpp v254, v254, v254 row_bcast:15 row_mask:0xa bank_mask:0xf\n"
"v_add_f32_dpp v255, v255, v255 row_bcast:15 row_mask:0xa bank_mask:0xf\n"
"s_nop 0\n"
"v_add_f32_dpp v254, v254, v254 row_bcast:31 row_mask:0xc bank_mask:0xf\n"
"v_add_f32_dpp v255, v255, v255 row_bcast:31 row_mask:0xc bank_mask:0xf\n"
"s_mov_b32 exec_lo, 0\n"
"s_mov_b32 exec_hi, 0x80000000\n"
"ds_write_b64 v244, v[254:255] offset:18432\n"
"s_mov_b32 exec_lo, -1\n"
"s_mov_b32 exec_hi, 0xff\n"
"s_waitcnt lgkmcnt(0)\n"
"s_barrier\n"
"ds_read_b128 v[216:219], v243 offset:18432\n"
"ds_read_b128 v[220:223], v243 offset:18448\n"
"s_waitcnt lgkmcnt(1)\n"
"v_pk_add_f32 v[216:217], v[216:217], v[218:219]\n"
"s_waitcnt lgkmcnt(0)\n"
"v_pk_add_f32 v[220:221], v[220:221], v[222:223]\n"
"v_pk_add_f32 v[216:217], v[216:217], v[220:221]\n"
"v_mul_f32 v216, 0x3acccccd, v216\n"
"v_mul_f32 v217, 0x3acccccd, v217\n"
"v_cvt_pk_bf16_f32 v218, 0, v216\n"
"v_fma_f32 v217, -v216, v216, v217\n"
"v_sub_f32 v220, v250, v218\n"
"v_max_f32 v217, 0, v217\n"
"v_sub_f32 v221, v251, v218\n"
"v_cvt_pk_bf16_f32 v217, 0, v217\n"
"v_sub_f32 v222, v252, v218\n"
"v_add_f32 v217, 0x3727c5ac, v217\n"
"v_sub_f32 v223, v253, v218\n"
"v_cvt_pk_bf16_f32 v217, 0, v217\n"
"v_cvt_pk_bf16_f32 v220, 0, v220\n"
"v_sqrt_f32 v217, v217\n"
"v_cvt_pk_bf16_f32 v221, 0, v221\n"
"v_cvt_pk_bf16_f32 v217, 0, v217\n"
"v_cvt_pk_bf16_f32 v222, 0, v222\n"
"v_rcp_f32 v217, v217\n"
"v_cvt_pk_bf16_f32 v223, 0, v223\n"
"v_pk_mul_f32 v[220:221], v[220:221], v[216:217] op_sel:[0,1] op_sel_hi:[1,1]\n"
"v_pk_mul_f32 v[222:223], v[222:223], v[216:217] op_sel:[0,1] op_sel_hi:[1,1]\n"
"v_cvt_pk_bf16_f32 v220, v220, v221\n"
"v_cvt_pk_bf16_f32 v221, v222, v223\n"
"ds_write_b64 v227, v[220:221] offset:6400\n"
"s_mov_b64 exec, -1\n"
"s_cmp_ge_u32 s14, 27\n"
"s_cbranch_scc1 .Lk_ms_199\n"
"ds_read_b128 v[184:187], v228 offset:5120\n"
"ds_read_b128 v[188:191], v228 offset:5184\n"
"ds_read_b128 v[192:195], v228 offset:5248\n"
"ds_read_b128 v[196:199], v228 offset:5312\n"
"ds_read_b128 v[200:203], v228 offset:5376\n"
"s_mul_i32 s91, s14, 2560\n"
"s_add_u32 s91, s91, 696320\n"
"s_waitcnt lgkmcnt(4)\n"
"v_smfmac_f32_16x16x64_bf16 v[208:211], v[184:187], v[96:103], v225\n"
"v_smfmac_f32_16x16x64_bf16 v[212:215], v[184:187], v[104:111], v225\n"
"s_waitcnt lgkmcnt(3)\n"
"v_smfmac_f32_16x16x64_bf16 v[208:211], v[188:191], v[112:119], v225\n"
"v_smfmac_f32_16x16x64_bf16 v[212:215], v[188:191], v[120:127], v225\n"
"s_waitcnt lgkmcnt(2)\n"
"v_smfmac_f32_16x16x64_bf16 v[208:211], v[192:195], v[128:135], v225\n"
"v_smfmac_f32_16x16x64_bf16 v[212:215], v[192:195], v[136:143], v225\n"
"s_waitcnt lgkmcnt(1)\n"
"v_smfmac_f32_16x16x64_bf16 v[208:211], v[196:199], v[144:151], v225\n"
"v_smfmac_f32_16x16x64_bf16 v[212:215], v[196:199], v[152:159], v225\n"
"s_waitcnt lgkmcnt(0)\n"
"v_smfmac_f32_16x16x64_bf16 v[208:211], v[200:203], v[160:167], v225\n"
"v_smfmac_f32_16x16x64_bf16 v[212:215], v[200:203], v[168:175], v225\n"
"v_mov_b32 v184, 0\n"
"v_mov_b32 v185, 0\n"
"v_mov_b32 v188, 0\n"
"v_mov_b32 v189, 0\n"
"v_mov_b32 v192, 0\n"
"v_mov_b32 v193, 0\n"
"v_mov_b32 v196, 0\n"
"v_mov_b32 v197, 0\n"
"v_mov_b32 v200, 0\n"
"v_mov_b32 v201, 0\n"
"s_mov_b32 exec_lo, -1\n"
"s_mov_b32 exec_hi, 0\n"
"ds_write2_b64 v236, v[208:209], v[212:213] offset0:0 offset1:16\n"
"s_mov_b64 exec, -1\n"
"s_waitcnt lgkmcnt(0)\n"
"s_barrier\n"
"ds_read2st64_b64 v[216:219], v237 offset0:24 offset1:25\n"
"ds_read2st64_b64 v[220:223], v237 offset0:26 offset1:27\n"
"s_waitcnt lgkmcnt(1)\n"
"v_pk_add_f32 v[216:217], v[216:217], v[218:219]\n"
"s_waitcnt lgkmcnt(0)\n"
"v_pk_add_f32 v[220:221], v[220:221], v[222:223]\n"
"v_pk_add_f32 v[216:217], v[216:217], v[220:221]\n"
"v_cvt_pk_bf16_f32 v216, 0, v216\n"
"v_cvt_pk_bf16_f32 v217, 0, v217\n"
"v_mul_f32 v218, 0xbfb8aa3b, v216\n"
"v_mul_f32 v219, 0xbfb8aa3b, v217\n"
"v_exp_f32 v218, v218\n"
"v_exp_f32 v219, v219\n"
"v_add_f32 v218, 1.0, v218\n"
"v_add_f32 v219, 1.0, v219\n"
"v_rcp_f32 v218, v218\n"
"v_rcp_f32 v219, v219\n"
"s_nop 0\n"
"v_pk_mul_f32 v[216:217], v[216:217], v[218:219]\n"
"v_cvt_pk_bf16_f32 v216, v216, v217\n"
"ds_write_b32 v238, v216 offset:8704\n"
"ds_read_b128 v[176:179], v234 offset:8704\n"
"ds_read_b128 v[180:183], v234 offset:8768\n"
"s_waitcnt lgkmcnt(1)\n"
"v_smfmac_f32_16x16x64_bf16 v[184:187], v[176:179], a[160:167], v225\n"
"v_smfmac_f32_16x16x64_bf16 v[188:191], v[176:179], a[168:175], v225\n"
"v_smfmac_f32_16x16x64_bf16 v[192:195], v[176:179], a[176:183], v225\n"
"v_smfmac_f32_16x16x64_bf16 v[196:199], v[176:179], a[184:191], v225\n"
"v_smfmac_f32_16x16x64_bf16 v[200:203], v[176:179], a[192:199], v225\n"
"s_waitcnt lgkmcnt(0)\n"
"v_smfmac_f32_16x16x64_bf16 v[184:187], v[180:183], a[200:207], v225\n"
"v_smfmac_f32_16x16x64_bf16 v[188:191], v[180:183], a[208:215], v225\n"
"v_smfmac_f32_16x16x64_bf16 v[192:195], v[180:183], a[216:223], v225\n"
"v_smfmac_f32_16x16x64_bf16 v[196:199], v[180:183], a[224:231], v225\n"
"v_smfmac_f32_16x16x64_bf16 v[200:203], v[180:183], a[232:239], v225\n"
"s_mov_b32 exec_lo, 0xffff0000\n"
"s_mov_b32 exec_hi, 0\n"
"s_nop 1\n"
"v_min_u32 v184, -2, v184\n"
"v_min_u32 v185, -2, v185\n"
"buffer_store_dwordx2 v[184:185], v239, s[52:55], s91 offen offset:0 sc1\n"
"v_min_u32 v188, -2, v188\n"
"v_min_u32 v189, -2, v189\n"
"buffer_store_dwordx2 v[188:189], v239, s[52:55], s91 offen offset:128 sc1\n"
"v_min_u32 v192, -2, v192\n"
"v_min_u32 v193, -2, v193\n"
"buffer_store_dwordx2 v[192:193], v239, s[52:55], s91 offen offset:256 sc1\n"
"v_min_u32 v196, -2, v196\n"
"v_min_u32 v197, -2, v197\n"
"buffer_store_dwordx2 v[196:197], v239, s[52:55], s91 offen offset:384 sc1\n"
"v_min_u32 v200, -2, v200\n"
"v_min_u32 v201, -2, v201\n"
"buffer_store_dwordx2 v[200:201], v239, s[52:55], s91 offen offset:512 sc1\n"
"s_mov_b64 exec, -1\n"
"s_branch .Lk_mj_200\n"
".Lk_ms_199:\n"
"s_sleep 24\n"
"s_nop 1\n"
".Lk_mj_200:\n"
"s_mul_i32 s88, s15, 640\n"
"v_and_b32 v170, 15, v224\n"
"v_lshrrev_b32 v171, 4, v224\n"
"v_lshl_add_u32 v170, v170, 4, s88\n"
"v_mul_u32_u24 v168, 2560, v171\n"
"v_add_u32 v168, v168, v170\n"
"v_lshrrev_b32 v169, 3, v224\n"
"v_mul_u32_u24 v169, 2560, v169\n"
"v_and_b32 v254, 7, v224\n"
"v_lshl_add_u32 v169, v254, 4, v169\n"
"v_add_u32 v169, s88, v169\n"
"v_and_b32 v254, 1, v171\n"
"v_lshrrev_b32 v171, 1, v171\n"
"v_lshl_add_u32 v170, v254, 9, v170\n"
"v_lshl_add_u32 v170, v171, 8, v170\n"
"v_lshrrev_b32 v171, 1, v170\n"
"v_mov_b32 v254, 0\n"
"v_mov_b32 v255, 0\n"
"s_mov_b32 s56, 765440\n"
"s_mov_b32 s57, 775680\n"
"s_mov_b32 s58, 785920\n"
"s_mov_b32 s59, 796160\n"
"s_mov_b32 s60, 806400\n"
"s_mov_b32 s61, 816640\n"
"s_mov_b32 s62, 826880\n"
"v_mov_b64 v[108:109], 0\n"
"v_mov_b64 v[110:111], 0\n"
"v_mov_b64 v[136:137], 0\n"
"v_mov_b64 v[138:139], 0\n"
"v_mov_b64 v[164:165], 0\n"
"v_mov_b64 v[166:167], 0\n"
"s_mov_b32 exec_lo, 0x00ffffff\n"
"s_mov_b32 exec_hi, 0xffff\n"
"ds_read_b128 v[250:253], v170 offset:2560\n"
"s_waitcnt lgkmcnt(0)\n"
"s_sleep 6\n"
"s_nop 1\n"
".Lk_g_201:\n"
"s_cmp_eq_u32 s96, 0\n"
"s_cbranch_scc1 .Lk_gok_202\n"
"s_sub_u32 s96, s96, 1\n"
"s_mov_b64 exec, -1\n"
"s_mov_b32 exec_lo, 0xffffff\n"
"s_mov_b32 exec_hi, 0x0\n"
"buffer_load_dwordx4 v[108:111], v169, s[52:55], s62 offen offset:512 sc1\n"
"s_mov_b32 exec_lo, 0xffffffff\n"
"s_mov_b32 exec_hi, 0xffff\n"
"buffer_load_dwordx4 v[136:139], v168, s[52:55], s62 offen offset:0 sc1\n"
"buffer_load_dwordx4 v[164:167], v168, s[52:55], s62 offen offset:256 sc1\n"
"s_mov_b64 exec, -1\n"
"buffer_load_dwordx4 v[96:99], v169, s[52:55], s56 offen offset:512 sc1\n"
"buffer_load_dwordx4 v[100:103], v169, s[52:55], s58 offen offset:512 sc1\n"
"buffer_load_dwordx4 v[104:107], v169, s[52:55], s60 offen offset:512 sc1\n"
"buffer_load_dwordx4 v[112:115], v168, s[52:55], s56 offen offset:0 sc1\n"
"buffer_load_dwordx4 v[140:143], v168, s[52:55], s56 offen offset:256 sc1\n"
"buffer_load_dwordx4 v[116:119], v168, s[52:55], s57 offen offset:0 sc1\n"
"buffer_load_dwordx4 v[144:147], v168, s[52:55], s57 offen offset:256 sc1\n"
"buffer_load_dwordx4 v[120:123], v168, s[52:55], s58 offen offset:0 sc1\n"
"buffer_load_dwordx4 v[148:151], v168, s[52:55], s58 offen offset:256 sc1\n"
"buffer_load_dwordx4 v[124:127], v168, s[52:55], s59 offen offset:0 sc1\n"
"buffer_load_dwordx4 v[152:155], v168, s[52:55], s59 offen offset:256 sc1\n"
"buffer_load_dwordx4 v[128:131], v168, s[52:55], s60 offen offset:0 sc1\n"
"buffer_load_dwordx4 v[156:159], v168, s[52:55], s60 offen offset:256 sc1\n"
"buffer_load_dwordx4 v[132:135], v168, s[52:55], s61 offen offset:0 sc1\n"
"buffer_load_dwordx4 v[160:163], v168, s[52:55], s61 offen offset:256 sc1\n"
"s_waitcnt vmcnt(14)\n"
"v_pk_add_f32 v[172:173], v[108:109], v[96:97]\n"
"v_pk_add_f32 v[174:175], v[110:111], v[98:99]\n"
"s_waitcnt vmcnt(13)\n"
"v_pk_add_f32 v[172:173], v[172:173], v[100:101]\n"
"v_pk_add_f32 v[174:175], v[174:175], v[102:103]\n"
"s_waitcnt vmcnt(12)\n"
"v_pk_add_f32 v[172:173], v[172:173], v[104:105]\n"
"v_pk_add_f32 v[174:175], v[174:175], v[106:107]\n"
"s_nop 0\n"
"v_add_f32_dpp v184, v172, v172 row_ror:8 row_mask:0xf bank_mask:0xf\n"
"v_add_f32_dpp v185, v173, v173 row_ror:8 row_mask:0xf bank_mask:0xf\n"
"v_add_f32_dpp v186, v174, v174 row_ror:8 row_mask:0xf bank_mask:0xf\n"
"v_add_f32_dpp v187, v175, v175 row_ror:8 row_mask:0xf bank_mask:0xf\n"
"v_permlane32_swap_b32 v184, v172\n"
"v_permlane32_swap_b32 v185, v173\n"
"v_permlane32_swap_b32 v186, v174\n"
"v_permlane32_swap_b32 v187, v175\n"
"v_pk_add_f32 v[172:173], v[184:185], v[172:173]\n"
"v_pk_add_f32 v[174:175], v[186:187], v[174:175]\n"
"s_waitcnt vmcnt(11)\n"
"v_pk_add_f32 v[176:177], v[136:137], v[112:113]\n"
"v_pk_add_f32 v[178:179], v[138:139], v[114:115]\n"
"s_waitcnt vmcnt(10)\n"
"v_pk_add_f32 v[180:181], v[164:165], v[140:141]\n"
"v_pk_add_f32 v[182:183], v[166:167], v[142:143]\n"
"s_waitcnt vmcnt(9)\n"
"v_pk_add_f32 v[176:177], v[176:177], v[116:117]\n"
"v_pk_add_f32 v[178:179], v[178:179], v[118:119]\n"
"s_waitcnt vmcnt(8)\n"
"v_pk_add_f32 v[180:181], v[180:181], v[144:145]\n"
"v_pk_add_f32 v[182:183], v[182:183], v[146:147]\n"
"s_waitcnt vmcnt(7)\n"
"v_pk_add_f32 v[176:177], v[176:177], v[120:121]\n"
"v_pk_add_f32 v[178:179], v[178:179], v[122:123]\n"
"s_waitcnt vmcnt(6)\n"
"v_pk_add_f32 v[180:181], v[180:181], v[148:149]\n"
"v_pk_add_f32 v[182:183], v[182:183], v[150:151]\n"
"s_waitcnt vmcnt(5)\n"
"v_pk_add_f32 v[176:177], v[176:177], v[124:125]\n"
"v_pk_add_f32 v[178:179], v[178:179], v[126:127]\n"
"s_waitcnt vmcnt(4)\n"
"v_pk_add_f32 v[180:181], v[180:181], v[152:153]\n"
"v_pk_add_f32 v[182:183], v[182:183], v[154:155]\n"
"s_waitcnt vmcnt(3)\n"
"v_pk_add_f32 v[176:177], v[176:177], v[128:129]\n"
"v_pk_add_f32 v[178:179], v[178:179], v[130:131]\n"
"s_waitcnt vmcnt(2)\n"
"v_pk_add_f32 v[180:181], v[180:181], v[156:157]\n"
"v_pk_add_f32 v[182:183], v[182:183], v[158:159]\n"
"s_waitcnt vmcnt(1)\n"
"v_pk_add_f32 v[176:177], v[176:177], v[132:133]\n"
"v_pk_add_f32 v[178:179], v[178:179], v[134:135]\n"
"s_waitcnt vmcnt(0)\n"
"v_pk_add_f32 v[180:181], v[180:181], v[160:161]\n"
"v_pk_add_f32 v[182:183], v[182:183], v[162:163]\n"
"s_nop 0\n"
"v_permlane32_swap_b32 v176, v180\n"
"v_permlane32_swap_b32 v177, v181\n"
"v_permlane32_swap_b32 v178, v182\n"
"v_permlane32_swap_b32 v179, v183\n"
"v_pk_add_f32 v[176:177], v[176:177], v[180:181]\n"
"v_pk_add_f32 v[178:179], v[178:179], v[182:183]\n"
"s_nop 0\n"
"v_permlane16_swap_b32 v176, v172\n"
"v_permlane16_swap_b32 v177, v173\n"
"v_permlane16_swap_b32 v178, v174\n"
"v_permlane16_swap_b32 v179, v175\n"
"s_mov_b32 exec_lo, 0x00ffffff\n"
"s_mov_b32 exec_hi, 0xffff\n"
"v_pk_add_f32 v[246:247], v[176:177], v[172:173]\n"
"v_pk_add_f32 v[248:249], v[178:179], v[174:175]\n"
"v_cmp_u_f32 vcc, v246, v247\n"
"v_cmp_u_f32 s[88:89], v248, v249\n"
"s_or_b64 s[88:89], s[88:89], vcc\n"
"s_cbranch_scc1 .Lk_g_201\n"
".Lk_gok_202:\n"
"v_cvt_pk_bf16_f32 v246, 0, v246\n"
"v_cvt_pk_bf16_f32 v247, 0, v247\n"
"v_cvt_pk_bf16_f32 v248, 0, v248\n"
"v_cvt_pk_bf16_f32 v249, 0, v249\n"
"v_pk_add_f32 v[250:251], v[250:251], v[246:247]\n"
"v_pk_add_f32 v[252:253], v[252:253], v[248:249]\n"
"v_cvt_pk_bf16_f32 v250, 0, v250\n"
"v_cvt_pk_bf16_f32 v251, 0, v251\n"
"v_cvt_pk_bf16_f32 v252, 0, v252\n"
"v_cvt_pk_bf16_f32 v253, 0, v253\n"
"v_add_f32 v254, v250, v251\n"
"v_mul_f32 v255, v250, v250\n"
"v_add_f32 v212, v252, v253\n"
"v_fmac_f32 v255, v251, v251\n"
"v_add_f32 v254, v254, v212\n"
"v_fmac_f32 v255, v252, v252\n"
"v_fmac_f32 v255, v253, v253\n"
"ds_write_b128 v170, v[250:253] offset:2560\n"
"s_mov_b64 exec, -1\n"
"v_add_f32_dpp v254, v254, v254 row_ror:8 row_mask:0xf bank_mask:0xf\n"
"v_add_f32_dpp v255, v255, v255 row_ror:8 row_mask:0xf bank_mask:0xf\n"
"s_nop 0\n"
"v_add_f32_dpp v254, v254, v254 row_ror:4 row_mask:0xf bank_mask:0xf\n"
"v_add_f32_dpp v255, v255, v255 row_ror:4 row_mask:0xf bank_mask:0xf\n"
"s_nop 0\n"
"v_add_f32_dpp v254, v254, v254 row_ror:2 row_mask:0xf bank_mask:0xf\n"
"v_add_f32_dpp v255, v255, v255 row_ror:2 row_mask:0xf bank_mask:0xf\n"
"s_nop 0\n"
"v_add_f32_dpp v254, v254, v254 row_ror:1 row_mask:0xf bank_mask:0xf\n"
"v_add_f32_dpp v255, v255, v255 row_ror:1 row_mask:0xf bank_mask:0xf\n"
"s_nop 0\n"
"v_add_f32_dpp v254, v254, v254 row_bcast:15 row_mask:0xa bank_mask:0xf\n"
"v_add_f32_dpp v255, v255, v255 row_bcast:15 row_mask:0xa bank_mask:0xf\n"
"s_nop 0\n"
"v_add_f32_dpp v254, v254, v254 row_bcast:31 row_mask:0xc bank_mask:0xf\n"
"v_add_f32_dpp v255, v255, v255 row_bcast:31 row_mask:0xc bank_mask:0xf\n"
"s_mov_b32 exec_lo, 0\n"
"s_mov_b32 exec_hi, 0x80000000\n"
"ds_write_b64 v244, v[254:255] offset:18464\n"
"s_mov_b32 exec_lo, 0x00ffffff\n"
"s_mov_b32 exec_hi, 0xffff\n"
"s_waitcnt lgkmcnt(0)\n"
"s_barrier\n"
"ds_read_b128 v[212:215], v243 offset:18464\n"
"ds_read_b128 v[216:219], v243 offset:18480\n"
"s_waitcnt lgkmcnt(1)\n"
"v_pk_add_f32 v[212:213], v[212:213], v[214:215]\n"
"s_waitcnt lgkmcnt(0)\n"
"v_pk_add_f32 v[216:217], v[216:217], v[218:219]\n"
"v_pk_add_f32 v[212:213], v[212:213], v[216:217]\n"
"v_mul_f32 v212, 0x3acccccd, v212\n"
"v_mul_f32 v213, 0x3acccccd, v213\n"
"v_cvt_pk_bf16_f32 v214, 0, v212\n"
"v_fma_f32 v213, -v212, v212, v213\n"
"v_sub_f32 v216, v250, v214\n"
"v_max_f32 v213, 0, v213\n"
"v_sub_f32 v217, v251, v214\n"
"v_cvt_pk_bf16_f32 v213, 0, v213\n"
"v_sub_f32 v218, v252, v214\n"
"v_add_f32 v213, 0x3727c5ac, v213\n"
"v_sub_f32 v219, v253, v214\n"
"v_cvt_pk_bf16_f32 v213, 0, v213\n"
"v_cvt_pk_bf16_f32 v216, 0, v216\n"
"v_sqrt_f32 v213, v213\n"
"v_cvt_pk_bf16_f32 v217, 0, v217\n"
"v_cvt_pk_bf16_f32 v213, 0, v213\n"
"v_cvt_pk_bf16_f32 v218, 0, v218\n"
"v_rcp_f32 v213, v213\n"
"v_cvt_pk_bf16_f32 v219, 0, v219\n"
"v_pk_mul_f32 v[216:217], v[216:217], v[212:213] op_sel:[0,1] op_sel_hi:[1,1]\n"
"v_pk_mul_f32 v[218:219], v[218:219], v[212:213] op_sel:[0,1] op_sel_hi:[1,1]\n"
"v_cvt_pk_bf16_f32 v216, v216, v217\n"
"v_cvt_pk_bf16_f32 v217, v218, v219\n"
"ds_write_b64 v171, v[216:217] offset:6400\n"
"s_mov_b64 exec, -1\n"
"ds_read_b128 v[104:107], v228 offset:5120\n"
"ds_read_b128 v[108:111], v228 offset:5184\n"
"ds_read_b128 v[112:115], v228 offset:5248\n"
"ds_read_b128 v[116:119], v228 offset:5312\n"
"ds_read_b128 v[120:123], v228 offset:5376\n"
"s_lshl_b32 s91, s14, 8\n"
"s_add_u32 s91, s91, 24576\n"
"v_mov_b32 v204, 0\n"
"v_mov_b32 v205, 0\n"
"v_mov_b32 v208, 0\n"
"v_mov_b32 v209, 0\n"
"v_mov_b32 v212, 0\n"
"v_mov_b32 v213, 0\n"
"v_mov_b32 v216, 0\n"
"v_mov_b32 v217, 0\n"
"s_waitcnt lgkmcnt(4)\n"
"v_smfmac_f32_16x16x64_bf16 v[204:207], v[104:107], a[0:7], v225\n"
"v_smfmac_f32_16x16x64_bf16 v[208:211], v[104:107], a[8:15], v225\n"
"v_smfmac_f32_16x16x64_bf16 v[212:215], v[104:107], a[16:23], v225\n"
"v_smfmac_f32_16x16x64_bf16 v[216:219], v[104:107], a[24:31], v225\n"
"s_waitcnt lgkmcnt(3)\n"
"v_smfmac_f32_16x16x64_bf16 v[204:207], v[108:111], a[32:39], v225\n"
"v_smfmac_f32_16x16x64_bf16 v[208:211], v[108:111], a[40:47], v225\n"
"v_smfmac_f32_16x16x64_bf16 v[212:215], v[108:111], a[48:55], v225\n"
"v_smfmac_f32_16x16x64_bf16 v[216:219], v[108:111], a[56:63], v225\n"
"s_waitcnt lgkmcnt(2)\n"
"v_smfmac_f32_16x16x64_bf16 v[204:207], v[112:115], a[64:71], v225\n"
"v_smfmac_f32_16x16x64_bf16 v[208:211], v[112:115], a[72:79], v225\n"
"v_smfmac_f32_16x16x64_bf16 v[212:215], v[112:115], a[80:87], v225\n"
"v_smfmac_f32_16x16x64_bf16 v[216:219], v[112:115], a[88:95], v225\n"
"s_waitcnt lgkmcnt(1)\n"
"v_smfmac_f32_16x16x64_bf16 v[204:207], v[116:119], a[96:103], v225\n"
"v_smfmac_f32_16x16x64_bf16 v[208:211], v[116:119], a[104:111], v225\n"
"v_smfmac_f32_16x16x64_bf16 v[212:215], v[116:119], a[112:119], v225\n"
"v_smfmac_f32_16x16x64_bf16 v[216:219], v[116:119], a[120:127], v225\n"
"s_waitcnt lgkmcnt(0)\n"
"v_smfmac_f32_16x16x64_bf16 v[204:207], v[120:123], a[128:135], v225\n"
"v_smfmac_f32_16x16x64_bf16 v[208:211], v[120:123], a[136:143], v225\n"
"v_smfmac_f32_16x16x64_bf16 v[212:215], v[120:123], a[144:151], v225\n"
"v_smfmac_f32_16x16x64_bf16 v[216:219], v[120:123], a[152:159], v225\n"
"s_mov_b32 exec_lo, -1\n"
"s_mov_b32 exec_hi, 0\n"
"s_nop 3\n"
"ds_write2_b64 v240, v[204:205], v[208:209] offset0:0 offset1:2\n"
"s_nop 0\n"
"ds_write2_b64 v240, v[212:213], v[216:217] offset0:1 offset1:3\n"
"s_mov_b64 exec, -1\n"
"s_waitcnt lgkmcnt(0)\n"
"s_barrier\n"
"s_cmp_lg_u32 s15, 0\n"
"s_cbranch_scc1 .Lk_qs_203\n"
"ds_read_b128 v[128:131], v226 offset:14336\n"
"ds_read_b128 v[132:135], v226 offset:15360\n"
"ds_read_b128 v[136:139], v226 offset:16384\n"
"ds_read_b128 v[140:143], v226 offset:17408\n"
"s_waitcnt lgkmcnt(2)\n"
"v_pk_add_f32 v[128:129], v[128:129], v[132:133]\n"
"v_pk_add_f32 v[130:131], v[130:131], v[134:135]\n"
"s_waitcnt lgkmcnt(0)\n"
"v_pk_add_f32 v[136:137], v[136:137], v[140:141]\n"
"v_pk_add_f32 v[138:139], v[138:139], v[142:143]\n"
"v_pk_add_f32 v[128:129], v[128:129], v[136:137]\n"
"v_pk_add_f32 v[130:131], v[130:131], v[138:139]\n"
"v_cvt_pk_bf16_f32 v128, v128, v129\n"
"v_cvt_pk_bf16_f32 v129, v130, v131\n"
"v_min_u32 v128, -2, v128\n"
"v_min_u32 v129, -2, v129\n"
"s_mov_b32 exec_lo, 0\n"
"s_mov_b32 exec_hi, -1\n"
"buffer_store_dwordx2 v[128:129], v241, s[52:55], s91 offen sc1\n"
"s_mov_b64 exec, -1\n"
"s_nop 1\n"
".Lk_qs_203:\n"
"ds_read_b128 v[96:99], v242 offset:0\n"
"ds_read_b128 v[100:103], v242 offset:1024\n"
"ds_read_b128 v[104:107], v242 offset:2048\n"
"ds_read_b128 v[108:111], v242 offset:3072\n"
"ds_read_b128 v[112:115], v242 offset:4096\n"
"ds_read_b128 v[116:119], v242 offset:5120\n"
"ds_read_b128 v[120:123], v242 offset:6144\n"
"ds_read_b128 v[124:127], v242 offset:7168\n"
"ds_read_b128 v[128:131], v242 offset:8192\n"
"ds_read_b128 v[132:135], v242 offset:9216\n"
"ds_read_b128 v[136:139], v242 offset:10240\n"
"ds_read_b128 v[140:143], v242 offset:11264\n"
"v_mov_b32 v188, 0\n"
"v_mov_b32 v189, 0\n"
"v_mov_b32 v192, 0\n"
"v_mov_b32 v193, 0\n"
"v_mov_b32 v196, 0\n"
"v_mov_b32 v197, 0\n"
"v_mov_b32 v200, 0\n"
"v_mov_b32 v201, 0\n"
"v_mov_b32 v204, 0\n"
"v_mov_b32 v205, 0\n"
"s_mov_b32 s90, 327680\n"
"s_mul_i32 s91, s13, 2560\n"
"s_mul_i32 s92, s34, 640\n"
"s_add_u32 s91, s91, s92\n"
"s_add_u32 s91, s91, 655360\n"
"s_waitcnt lgkmcnt(0)\n"
"s_nop 1\n"
"s_branch .Lk_o2_206\n"
".Lk_o_204:\n"
".Lk_o2_206:\n"
"s_cmp_eq_u32 s96, 0\n"
"s_cbranch_scc1 .Lk_ook_205\n"
"s_sub_u32 s96, s96, 1\n"
"buffer_load_dwordx2 v[160:161], v231, s[52:55], s90 offen sc1\n"
"buffer_load_dwordx4 v[144:147], v229, s[52:55], s90 offen offset:0 sc1\n"
"buffer_load_dwordx4 v[148:151], v229, s[52:55], s90 offen offset:1280 sc1\n"
"buffer_load_dwordx4 v[152:155], v229, s[52:55], s90 offen offset:2560 sc1\n"
"buffer_load_dwordx4 v[156:159], v230, s[52:55], s90 offen offset:3840 sc1\n"
"s_waitcnt vmcnt(2)\n"
"v_max3_u32 v246, v160, v144, v148\n"
"s_waitcnt vmcnt(0)\n"
"v_max3_u32 v246, v246, v152, v156\n"
"v_cmp_eq_u32 vcc, -1, v246\n"
"s_cbranch_vccnz .Lk_o_204\n"
"v_max_f32_dpp v246, v160, v160 quad_perm:[1,0,3,2] row_mask:0xf bank_mask:0xf\n"
"s_nop 1\n"
"v_max_f32_dpp v246, v246, v246 quad_perm:[2,3,0,1] row_mask:0xf bank_mask:0xf\n"
"s_nop 1\n"
"v_max_f32_dpp v246, v246, v246 row_half_mirror row_mask:0xf bank_mask:0xf\n"
"v_sub_f32 v246, v160, v246\n"
"v_exp_f32 v246, v246\n"
"s_nop 0\n"
"v_cndmask_b32 v246, v246, 0, s[76:77]\n"
"v_mul_f32 v247, v246, v161\n"
"s_nop 1\n"
"v_add_f32_dpp v247, v247, v247 quad_perm:[1,0,3,2] row_mask:0xf bank_mask:0xf\n"
"s_nop 1\n"
"v_add_f32_dpp v247, v247, v247 quad_perm:[2,3,0,1] row_mask:0xf bank_mask:0xf\n"
"s_nop 1\n"
"v_add_f32_dpp v247, v247, v247 row_half_mirror row_mask:0xf bank_mask:0xf\n"
"v_rcp_f32 v247, v247\n"
"s_nop 0\n"
"v_mul_f32 v246, v246, v247\n"
"s_nop 1\n"
"v_mov_b32_dpp v162, v246 row_newbcast:0 row_mask:0xf bank_mask:0xf\n"
"v_mov_b32_dpp v163, v246 row_newbcast:2 row_mask:0xf bank_mask:0xf\n"
"v_mov_b32_dpp v164, v246 row_newbcast:4 row_mask:0xf bank_mask:0xf\n"
"v_mov_b32_dpp v165, v246 row_newbcast:6 row_mask:0xf bank_mask:0xf\n"
"v_pk_mul_f32 v[166:167], v[144:145], v[162:163] op_sel:[0,0] op_sel_hi:[1,0]\n"
"v_pk_mul_f32 v[168:169], v[146:147], v[162:163] op_sel:[0,0] op_sel_hi:[1,0]\n"
"v_pk_fma_f32 v[166:167], v[148:149], v[162:163], v[166:167] op_sel:[0,1,0] op_sel_hi:[1,1,1]\n"
"v_pk_fma_f32 v[168:169], v[150:151], v[162:163], v[168:169] op_sel:[0,1,0] op_sel_hi:[1,1,1]\n"
"v_pk_fma_f32 v[166:167], v[152:153], v[164:165], v[166:167] op_sel:[0,0,0] op_sel_hi:[1,0,1]\n"
"v_pk_fma_f32 v[168:169], v[154:155], v[164:165], v[168:169] op_sel:[0,0,0] op_sel_hi:[1,0,1]\n"
"v_pk_fma_f32 v[166:167], v[156:157], v[164:165], v[166:167] op_sel:[0,1,0] op_sel_hi:[1,1,1]\n"
"v_pk_fma_f32 v[168:169], v[158:159], v[164:165], v[168:169] op_sel:[0,1,0] op_sel_hi:[1,1,1]\n"
"s_nop 1\n"
"v_permlane32_swap_b32 v166, v168\n"
"v_permlane32_swap_b32 v167, v169\n"
"v_pk_add_f32 v[166:167], v[166:167], v[168:169]\n"
"v_cmp_u_f32 vcc, v166, v167\n"
"v_cvt_pk_bf16_f32 v166, v166, v167\n"
"s_cbranch_vccnz .Lk_o_204\n"
".Lk_ook_205:\n"
"ds_write_b32 v232, v166 offset:7680\n"
"ds_read_b128 v[172:175], v233 offset:7680\n"
"ds_read_b128 v[176:179], v233 offset:7744\n"
"ds_read_b128 v[180:183], v233 offset:7808\n"
"ds_read_b128 v[184:187], v233 offset:7872\n"
"s_waitcnt lgkmcnt(3)\n"
"v_smfmac_f32_16x16x64_bf16 v[188:191], v[172:175], a[240:247], v225\n"
"v_smfmac_f32_16x16x64_bf16 v[192:195], v[172:175], a[248:255], v225\n"
"v_smfmac_f32_16x16x64_bf16 v[196:199], v[172:175], v[0:7], v225\n"
"v_smfmac_f32_16x16x64_bf16 v[200:203], v[172:175], v[8:15], v225\n"
"v_smfmac_f32_16x16x64_bf16 v[204:207], v[172:175], v[16:23], v225\n"
"s_waitcnt lgkmcnt(2)\n"
"v_smfmac_f32_16x16x64_bf16 v[188:191], v[176:179], v[24:31], v225\n"
"v_smfmac_f32_16x16x64_bf16 v[192:195], v[176:179], v[32:39], v225\n"
"v_smfmac_f32_16x16x64_bf16 v[196:199], v[176:179], v[40:47], v225\n"
"v_smfmac_f32_16x16x64_bf16 v[200:203], v[176:179], v[48:55], v225\n"
"v_smfmac_f32_16x16x64_bf16 v[204:207], v[176:179], v[56:63], v225\n"
"s_waitcnt lgkmcnt(1)\n"
"v_smfmac_f32_16x16x64_bf16 v[188:191], v[180:183], v[64:71], v225\n"
"v_smfmac_f32_16x16x64_bf16 v[192:195], v[180:183], v[72:79], v225\n"
"v_smfmac_f32_16x16x64_bf16 v[196:199], v[180:183], v[80:87], v225\n"
"v_smfmac_f32_16x16x64_bf16 v[200:203], v[180:183], v[88:95], v225\n"
"v_smfmac_f32_16x16x64_bf16 v[204:207], v[180:183], v[96:103], v225\n"
"s_waitcnt lgkmcnt(0)\n"
"v_smfmac_f32_16x16x64_bf16 v[188:191], v[184:187], v[104:111], v225\n"
"v_smfmac_f32_16x16x64_bf16 v[192:195], v[184:187], v[112:119], v225\n"
"v_smfmac_f32_16x16x64_bf16 v[196:199], v[184:187], v[120:127], v225\n"
"v_smfmac_f32_16x16x64_bf16 v[200:203], v[184:187], v[128:135], v225\n"
"v_smfmac_f32_16x16x64_bf16 v[204:207], v[184:187], v[136:143], v225\n"
"s_mov_b64 exec, 0xffff\n"
"s_nop 3\n"
"ds_write2_b64 v235, v[188:189], v[192:193] offset0:0 offset1:16\n"
"s_nop 0\n"
"ds_write2_b64 v235, v[196:197], v[200:201] offset0:32 offset1:48\n"
"ds_write_b64 v235, v[204:205] offset:512\n"
"s_mov_b32 exec_lo, -1\n"
"s_mov_b32 exec_hi, 0xff\n"
"s_waitcnt lgkmcnt(0)\n"
"s_barrier\n"
"s_cmp_lg_u32 s15, 0\n"
"s_cbranch_scc1 .Lk_os_207\n"
"ds_read_b128 v[144:147], v226 offset:9728\n"
"ds_read_b128 v[148:151], v226 offset:10368\n"
"ds_read_b128 v[152:155], v226 offset:11008\n"
"ds_read_b128 v[156:159], v226 offset:11648\n"
"s_waitcnt lgkmcnt(2)\n"
"v_pk_add_f32 v[144:145], v[144:145], v[148:149]\n"
"v_pk_add_f32 v[146:147], v[146:147], v[150:151]\n"
"s_waitcnt lgkmcnt(0)\n"
"v_pk_add_f32 v[152:153], v[152:153], v[156:157]\n"
"v_pk_add_f32 v[154:155], v[154:155], v[158:159]\n"
"v_pk_add_f32 v[144:145], v[144:145], v[152:153]\n"
"v_pk_add_f32 v[146:147], v[146:147], v[154:155]\n"
"v_min_u32 v144, -2, v144\n"
"v_min_u32 v145, -2, v145\n"
"v_min_u32 v146, -2, v146\n"
"v_min_u32 v147, -2, v147\n"
"buffer_store_dwordx4 v[144:147], v226, s[52:55], s91 offen sc1\n"
"s_branch .Lk_oj_208\n"
".Lk_os_207:\n"
"s_sleep 12\n"
"s_nop 1\n"
".Lk_oj_208:\n"
"s_mov_b64 exec, -1\n"
"v_mov_b32 v254, 0\n"
"v_mov_b32 v255, 0\n"
"s_mov_b32 exec_lo, -1\n"
"s_mov_b32 exec_hi, 0xff\n"
"ds_read_b128 v[250:253], v226 offset:0\n"
"s_mov_b32 s90, 655360\n"
"s_add_u32 s56, s90, 0\n"
"s_add_u32 s57, s90, 5120\n"
"s_add_u32 s58, s90, 10240\n"
"s_add_u32 s59, s90, 15360\n"
"s_nop 1\n"
".Lk_g_209:\n"
"s_cmp_eq_u32 s96, 0\n"
"s_cbranch_scc1 .Lk_gok_210\n"
"s_sub_u32 s96, s96, 1\n"
"buffer_load_dwordx4 v[176:179], v226, s[52:55], s56 offen offset:0 sc1\n"
"buffer_load_dwordx4 v[180:183], v226, s[52:55], s56 offen offset:2560 sc1\n"
"buffer_load_dwordx4 v[184:187], v226, s[52:55], s57 offen offset:0 sc1\n"
"buffer_load_dwordx4 v[188:191], v226, s[52:55], s57 offen offset:2560 sc1\n"
"buffer_load_dwordx4 v[192:195], v226, s[52:55], s58 offen offset:0 sc1\n"
"buffer_load_dwordx4 v[196:199], v226, s[52:55], s58 offen offset:2560 sc1\n"
"buffer_load_dwordx4 v[200:203], v226, s[52:55], s59 offen offset:0 sc1\n"
"buffer_load_dwordx4 v[204:207], v226, s[52:55], s59 offen offset:2560 sc1\n"
"s_waitcnt vmcnt(6)\n"
"v_pk_add_f32 v[246:247], v[176:177], v[180:181]\n"
"v_pk_add_f32 v[248:249], v[178:179], v[182:183]\n"
"s_waitcnt vmcnt(5)\n"
"v_pk_add_f32 v[246:247], v[246:247], v[184:185]\n"
"v_pk_add_f32 v[248:249], v[248:249], v[186:187]\n"
"s_waitcnt vmcnt(4)\n"
"v_pk_add_f32 v[246:247], v[246:247], v[188:189]\n"
"v_pk_add_f32 v[248:249], v[248:249], v[190:191]\n"
"s_waitcnt vmcnt(3)\n"
"v_pk_add_f32 v[246:247], v[246:247], v[192:193]\n"
"v_pk_add_f32 v[248:249], v[248:249], v[194:195]\n"
"s_waitcnt vmcnt(2)\n"
"v_pk_add_f32 v[246:247], v[246:247], v[196:197]\n"
"v_pk_add_f32 v[248:249], v[248:249], v[198:199]\n"
"s_waitcnt vmcnt(1)\n"
"v_pk_add_f32 v[246:247], v[246:247], v[200:201]\n"
"v_pk_add_f32 v[248:249], v[248:249], v[202:203]\n"
"s_waitcnt vmcnt(0)\n"
"v_pk_add_f32 v[246:247], v[246:247], v[204:205]\n"
"v_pk_add_f32 v[248:249], v[248:249], v[206:207]\n"
"v_cmp_u_f32 vcc, v246, v247\n"
"v_cmp_u_f32 s[88:89], v248, v249\n"
"s_or_b64 s[88:89], s[88:89], vcc\n"
"s_cbranch_scc1 .Lk_g_209\n"
".Lk_gok_210:\n"
"v_cvt_pk_bf16_f32 v246, 0, v246\n"
"v_cvt_pk_bf16_f32 v247, 0, v247\n"
"v_cvt_pk_bf16_f32 v248, 0, v248\n"
"v_cvt_pk_bf16_f32 v249, 0, v249\n"
"s_waitcnt lgkmcnt(0)\n"
"v_pk_add_f32 v[250:251], v[250:251], v[246:247]\n"
"v_pk_add_f32 v[252:253], v[252:253], v[248:249]\n"
"v_cvt_pk_bf16_f32 v250, 0, v250\n"
"v_cvt_pk_bf16_f32 v251, 0, v251\n"
"v_cvt_pk_bf16_f32 v252, 0, v252\n"
"v_cvt_pk_bf16_f32 v253, 0, v253\n"
"s_sleep 2\n"
"s_mov_b32 s90, 471040\n"
"s_mov_b64 exec, -1\n"
"buffer_load_dwordx2 v[160:161], v231, s[52:55], s90 offen sc1\n"
"buffer_load_dwordx4 v[144:147], v229, s[52:55], s90 offen offset:0 sc1\n"
"buffer_load_dwordx4 v[148:151], v229, s[52:55], s90 offen offset:1280 sc1\n"
"buffer_load_dwordx4 v[152:155], v229, s[52:55], s90 offen offset:2560 sc1\n"
"buffer_load_dwordx4 v[156:159], v230, s[52:55], s90 offen offset:3840 sc1\n"
"s_mov_b32 exec_lo, -1\n"
"s_mov_b32 exec_hi, 0xff\n"
"v_add_f32 v254, v250, v251\n"
"v_mul_f32 v255, v250, v250\n"
"v_add_f32 v216, v252, v253\n"
"v_fmac_f32 v255, v251, v251\n"
"v_add_f32 v254, v254, v216\n"
"v_fmac_f32 v255, v252, v252\n"
"v_fmac_f32 v255, v253, v253\n"
"ds_write_b128 v226, v[250:253] offset:0\n"
"s_mov_b64 exec, -1\n"
"v_add_f32_dpp v254, v254, v254 row_ror:8 row_mask:0xf bank_mask:0xf\n"
"v_add_f32_dpp v255, v255, v255 row_ror:8 row_mask:0xf bank_mask:0xf\n"
"s_nop 0\n"
"v_add_f32_dpp v254, v254, v254 row_ror:4 row_mask:0xf bank_mask:0xf\n"
"v_add_f32_dpp v255, v255, v255 row_ror:4 row_mask:0xf bank_mask:0xf\n"
"s_nop 0\n"
"v_add_f32_dpp v254, v254, v254 row_ror:2 row_mask:0xf bank_mask:0xf\n"
"v_add_f32_dpp v255, v255, v255 row_ror:2 row_mask:0xf bank_mask:0xf\n"
"s_nop 0\n"
"v_add_f32_dpp v254, v254, v254 row_ror:1 row_mask:0xf bank_mask:0xf\n"
"v_add_f32_dpp v255, v255, v255 row_ror:1 row_mask:0xf bank_mask:0xf\n"
"s_nop 0\n"
"v_add_f32_dpp v254, v254, v254 row_bcast:15 row_mask:0xa bank_mask:0xf\n"
"v_add_f32_dpp v255, v255, v255 row_bcast:15 row_mask:0xa bank_mask:0xf\n"
"s_nop 0\n"
"v_add_f32_dpp v254, v254, v254 row_bcast:31 row_mask:0xc bank_mask:0xf\n"
"v_add_f32_dpp v255, v255, v255 row_bcast:31 row_mask:0xc bank_mask:0xf\n"
"s_mov_b32 exec_lo, 0\n"
"s_mov_b32 exec_hi, 0x80000000\n"
"ds_write_b64 v244, v[254:255] offset:18432\n"
"s_mov_b32 exec_lo, -1\n"
"s_mov_b32 exec_hi, 0xff\n"
"s_waitcnt lgkmcnt(0)\n"
"s_barrier\n"
"ds_read_b128 v[216:219], v243 offset:18432\n"
"ds_read_b128 v[220:223], v243 offset:18448\n"
"s_waitcnt lgkmcnt(1)\n"
"v_pk_add_f32 v[216:217], v[216:217], v[218:219]\n"
"s_waitcnt lgkmcnt(0)\n"
"v_pk_add_f32 v[220:221], v[220:221], v[222:223]\n"
"v_pk_add_f32 v[216:217], v[216:217], v[220:221]\n"
"v_mul_f32 v216, 0x3acccccd, v216\n"
"v_mul_f32 v217, 0x3acccccd, v217\n"
"v_cvt_pk_bf16_f32 v218, 0, v216\n"
"v_fma_f32 v217, -v216, v216, v217\n"
"v_sub_f32 v220, v250, v218\n"
"v_max_f32 v217, 0, v217\n"
"v_sub_f32 v221, v251, v218\n"
"v_cvt_pk_bf16_f32 v217, 0, v217\n"
"v_sub_f32 v222, v252, v218\n"
"v_add_f32 v217, 0x3727c5ac, v217\n"
"v_sub_f32 v223, v253, v218\n"
"v_cvt_pk_bf16_f32 v217, 0, v217\n"
"v_cvt_pk_bf16_f32 v220, 0, v220\n"
"v_sqrt_f32 v217, v217\n"
"v_cvt_pk_bf16_f32 v221, 0, v221\n"
"v_cvt_pk_bf16_f32 v217, 0, v217\n"
"v_cvt_pk_bf16_f32 v222, 0, v222\n"
"v_rcp_f32 v217, v217\n"
"v_cvt_pk_bf16_f32 v223, 0, v223\n"
"v_pk_mul_f32 v[220:221], v[220:221], v[216:217] op_sel:[0,1] op_sel_hi:[1,1]\n"
"v_pk_mul_f32 v[222:223], v[222:223], v[216:217] op_sel:[0,1] op_sel_hi:[1,1]\n"
"v_cvt_pk_bf16_f32 v220, v220, v221\n"
"v_cvt_pk_bf16_f32 v221, v222, v223\n"
"ds_write_b64 v227, v[220:221] offset:5120\n"
"s_mov_b64 exec, -1\n"
"v_mov_b32 v188, 0\n"
"v_mov_b32 v189, 0\n"
"v_mov_b32 v192, 0\n"
"v_mov_b32 v193, 0\n"
"v_mov_b32 v196, 0\n"
"v_mov_b32 v197, 0\n"
"v_mov_b32 v200, 0\n"
"v_mov_b32 v201, 0\n"
"v_mov_b32 v204, 0\n"
"v_mov_b32 v205, 0\n"
"s_mov_b32 s90, 471040\n"
"s_mul_i32 s91, s13, 2560\n"
"s_mul_i32 s92, s34, 640\n"
"s_add_u32 s91, s91, s92\n"
"s_add_u32 s91, s91, 675840\n"
"s_waitcnt vmcnt(2)\n"
"v_max3_u32 v246, v160, v144, v148\n"
"s_waitcnt vmcnt(0)\n"
"v_max3_u32 v246, v246, v152, v156\n"
"v_cmp_eq_u32 vcc, -1, v246\n"
"s_cbranch_vccz .Lk_oc_213\n"
".Lk_o_211:\n"
"s_cmp_eq_u32 s96, 0\n"
"s_cbranch_scc1 .Lk_ook_212\n"
"s_sub_u32 s96, s96, 1\n"
"buffer_load_dwordx2 v[160:161], v231, s[52:55], s90 offen sc1\n"
"buffer_load_dwordx4 v[144:147], v229, s[52:55], s90 offen offset:0 sc1\n"
"buffer_load_dwordx4 v[148:151], v229, s[52:55], s90 offen offset:1280 sc1\n"
"buffer_load_dwordx4 v[152:155], v229, s[52:55], s90 offen offset:2560 sc1\n"
"buffer_load_dwordx4 v[156:159], v230, s[52:55], s90 offen offset:3840 sc1\n"
"s_waitcnt vmcnt(2)\n"
"v_max3_u32 v246, v160, v144, v148\n"
"s_waitcnt vmcnt(0)\n"
"v_max3_u32 v246, v246, v152, v156\n"
"v_cmp_eq_u32 vcc, -1, v246\n"
"s_cbranch_vccnz .Lk_o_211\n"
".Lk_oc_213:\n"
"v_max_f32_dpp v246, v160, v160 quad_perm:[1,0,3,2] row_mask:0xf bank_mask:0xf\n"
"s_nop 1\n"
"v_max_f32_dpp v246, v246, v246 quad_perm:[2,3,0,1] row_mask:0xf bank_mask:0xf\n"
"s_nop 1\n"
"v_max_f32_dpp v246, v246, v246 row_half_mirror row_mask:0xf bank_mask:0xf\n"
"v_sub_f32 v246, v160, v246\n"
"v_exp_f32 v246, v246\n"
"s_nop 0\n"
"v_cndmask_b32 v246, v246, 0, s[76:77]\n"
"v_mul_f32 v247, v246, v161\n"
"s_nop 1\n"
"v_add_f32_dpp v247, v247, v247 quad_perm:[1,0,3,2] row_mask:0xf bank_mask:0xf\n"
"s_nop 1\n"
"v_add_f32_dpp v247, v247, v247 quad_perm:[2,3,0,1] row_mask:0xf bank_mask:0xf\n"
"s_nop 1\n"
"v_add_f32_dpp v247, v247, v247 row_half_mirror row_mask:0xf bank_mask:0xf\n"
"v_rcp_f32 v247, v247\n"
"s_nop 0\n"
"v_mul_f32 v246, v246, v247\n"
"s_nop 1\n"
"v_mov_b32_dpp v162, v246 row_newbcast:0 row_mask:0xf bank_mask:0xf\n"
"v_mov_b32_dpp v163, v246 row_newbcast:2 row_mask:0xf bank_mask:0xf\n"
"v_mov_b32_dpp v164, v246 row_newbcast:4 row_mask:0xf bank_mask:0xf\n"
"v_mov_b32_dpp v165, v246 row_newbcast:6 row_mask:0xf bank_mask:0xf\n"
"v_pk_mul_f32 v[166:167], v[144:145], v[162:163] op_sel:[0,0] op_sel_hi:[1,0]\n"
"v_pk_mul_f32 v[168:169], v[146:147], v[162:163] op_sel:[0,0] op_sel_hi:[1,0]\n"
"v_pk_fma_f32 v[166:167], v[148:149], v[162:163], v[166:167] op_sel:[0,1,0] op_sel_hi:[1,1,1]\n"
"v_pk_fma_f32 v[168:169], v[150:151], v[162:163], v[168:169] op_sel:[0,1,0] op_sel_hi:[1,1,1]\n"
"v_pk_fma_f32 v[166:167], v[152:153], v[164:165], v[166:167] op_sel:[0,0,0] op_sel_hi:[1,0,1]\n"
"v_pk_fma_f32 v[168:169], v[154:155], v[164:165], v[168:169] op_sel:[0,0,0] op_sel_hi:[1,0,1]\n"
"v_pk_fma_f32 v[166:167], v[156:157], v[164:165], v[166:167] op_sel:[0,1,0] op_sel_hi:[1,1,1]\n"
"v_pk_fma_f32 v[168:169], v[158:159], v[164:165], v[168:169] op_sel:[0,1,0] op_sel_hi:[1,1,1]\n"
"s_nop 1\n"
"v_permlane32_swap_b32 v166, v168\n"
"v_permlane32_swap_b32 v167, v169\n"
"v_pk_add_f32 v[166:167], v[166:167], v[168:169]\n"
"v_cmp_u_f32 vcc, v166, v167\n"
"v_cvt_pk_bf16_f32 v166, v166, v167\n"
"s_cbranch_vccnz .Lk_o_211\n"
".Lk_ook_212:\n"
"ds_write_b32 v232, v166 offset:7680\n"
"ds_read_b128 v[172:175], v233 offset:7680\n"
"ds_read_b128 v[176:179], v233 offset:7744\n"
"ds_read_b128 v[180:183], v233 offset:7808\n"
"ds_read_b128 v[184:187], v233 offset:7872\n"
"s_waitcnt lgkmcnt(3)\n"
"v_smfmac_f32_16x16x64_bf16 v[188:191], v[172:175], a[240:247], v225\n"
"v_smfmac_f32_16x16x64_bf16 v[192:195], v[172:175], a[248:255], v225\n"
"v_smfmac_f32_16x16x64_bf16 v[196:199], v[172:175], v[0:7], v225\n"
"v_smfmac_f32_16x16x64_bf16 v[200:203], v[172:175], v[8:15], v225\n"
"v_smfmac_f32_16x16x64_bf16 v[204:207], v[172:175], v[16:23], v225\n"
"s_waitcnt lgkmcnt(2)\n"
"v_smfmac_f32_16x16x64_bf16 v[188:191], v[176:179], v[24:31], v225\n"
"v_smfmac_f32_16x16x64_bf16 v[192:195], v[176:179], v[32:39], v225\n"
"v_smfmac_f32_16x16x64_bf16 v[196:199], v[176:179], v[40:47], v225\n"
"v_smfmac_f32_16x16x64_bf16 v[200:203], v[176:179], v[48:55], v225\n"
"v_smfmac_f32_16x16x64_bf16 v[204:207], v[176:179], v[56:63], v225\n"
"s_waitcnt lgkmcnt(1)\n"
"v_smfmac_f32_16x16x64_bf16 v[188:191], v[180:183], v[64:71], v225\n"
"v_smfmac_f32_16x16x64_bf16 v[192:195], v[180:183], v[72:79], v225\n"
"v_smfmac_f32_16x16x64_bf16 v[196:199], v[180:183], v[80:87], v225\n"
"v_smfmac_f32_16x16x64_bf16 v[200:203], v[180:183], v[88:95], v225\n"
"v_smfmac_f32_16x16x64_bf16 v[204:207], v[180:183], v[96:103], v225\n"
"s_waitcnt lgkmcnt(0)\n"
"v_smfmac_f32_16x16x64_bf16 v[188:191], v[184:187], v[104:111], v225\n"
"v_smfmac_f32_16x16x64_bf16 v[192:195], v[184:187], v[112:119], v225\n"
"v_smfmac_f32_16x16x64_bf16 v[196:199], v[184:187], v[120:127], v225\n"
"v_smfmac_f32_16x16x64_bf16 v[200:203], v[184:187], v[128:135], v225\n"
"v_smfmac_f32_16x16x64_bf16 v[204:207], v[184:187], v[136:143], v225\n"
"s_mov_b64 exec, 0xffff\n"
"s_nop 3\n"
"ds_write2_b64 v235, v[188:189], v[192:193] offset0:0 offset1:16\n"
"s_nop 0\n"
"ds_write2_b64 v235, v[196:197], v[200:201] offset0:32 offset1:48\n"
"ds_write_b64 v235, v[204:205] offset:512\n"
"s_mov_b32 exec_lo, -1\n"
"s_mov_b32 exec_hi, 0xff\n"
"s_waitcnt lgkmcnt(0)\n"
"s_barrier\n"
"s_cmp_lg_u32 s15, 0\n"
"s_cbranch_scc1 .Lk_os_214\n"
"ds_read_b128 v[144:147], v226 offset:9728\n"
"ds_read_b128 v[148:151], v226 offset:10368\n"
"ds_read_b128 v[152:155], v226 offset:11008\n"
"ds_read_b128 v[156:159], v226 offset:11648\n"
"s_waitcnt lgkmcnt(2)\n"
"v_pk_add_f32 v[144:145], v[144:145], v[148:149]\n"
"v_pk_add_f32 v[146:147], v[146:147], v[150:151]\n"
"s_waitcnt lgkmcnt(0)\n"
"v_pk_add_f32 v[152:153], v[152:153], v[156:157]\n"
"v_pk_add_f32 v[154:155], v[154:155], v[158:159]\n"
"v_pk_add_f32 v[144:145], v[144:145], v[152:153]\n"
"v_pk_add_f32 v[146:147], v[146:147], v[154:155]\n"
"v_min_u32 v144, -2, v144\n"
"v_min_u32 v145, -2, v145\n"
"v_min_u32 v146, -2, v146\n"
"v_min_u32 v147, -2, v147\n"
"buffer_store_dwordx4 v[144:147], v226, s[52:55], s91 offen sc1\n"
"s_branch .Lk_oj_215\n"
".Lk_os_214:\n"
"s_sleep 12\n"
"s_nop 1\n"
".Lk_oj_215:\n"
"s_mov_b64 exec, -1\n"
"ds_read_b128 v[96:99], v242 offset:12288\n"
"ds_read_b128 v[100:103], v242 offset:13312\n"
"ds_read_b128 v[104:107], v242 offset:14336\n"
"ds_read_b128 v[108:111], v242 offset:15360\n"
"ds_read_b128 v[112:115], v242 offset:16384\n"
"ds_read_b128 v[116:119], v242 offset:17408\n"
"ds_read_b128 v[120:123], v242 offset:18432\n"
"ds_read_b128 v[124:127], v242 offset:19456\n"
"ds_read_b128 v[128:131], v242 offset:20480\n"
"ds_read_b128 v[132:135], v242 offset:21504\n"
"ds_read_b128 v[136:139], v242 offset:22528\n"
"ds_read_b128 v[140:143], v242 offset:23552\n"
"ds_read_b128 v[144:147], v242 offset:24576\n"
"ds_read_b128 v[148:151], v242 offset:25600\n"
"ds_read_b128 v[152:155], v242 offset:26624\n"
"s_waitcnt lgkmcnt(12)\n"
"ds_read_b128 v[156:159], v242 offset:27648\n"
"ds_read_b128 v[160:163], v242 offset:28672\n"
"ds_read_b128 v[164:167], v242 offset:29696\n"
"s_waitcnt lgkmcnt(12)\n"
"ds_read_b128 v[168:171], v242 offset:30720\n"
"ds_read_b128 v[172:175], v242 offset:31744\n"
"v_mov_b32 v208, 0\n"
"v_mov_b32 v209, 0\n"
"v_mov_b32 v212, 0\n"
"v_mov_b32 v213, 0\n"
"v_mov_b32 v254, 0\n"
"v_mov_b32 v255, 0\n"
"s_mov_b32 exec_lo, -1\n"
"s_mov_b32 exec_hi, 0xff\n"
"ds_read_b128 v[250:253], v226 offset:2560\n"
"s_mov_b32 s90, 675840\n"
"s_add_u32 s56, s90, 0\n"
"s_add_u32 s57, s90, 5120\n"
"s_add_u32 s58, s90, 10240\n"
"s_add_u32 s59, s90, 15360\n"
"s_nop 1\n"
".Lk_g_216:\n"
"s_cmp_eq_u32 s96, 0\n"
"s_cbranch_scc1 .Lk_gok_217\n"
"s_sub_u32 s96, s96, 1\n"
"buffer_load_dwordx4 v[176:179], v226, s[52:55], s56 offen offset:0 sc1\n"
"buffer_load_dwordx4 v[180:183], v226, s[52:55], s56 offen offset:2560 sc1\n"
"buffer_load_dwordx4 v[184:187], v226, s[52:55], s57 offen offset:0 sc1\n"
"buffer_load_dwordx4 v[188:191], v226, s[52:55], s57 offen offset:2560 sc1\n"
"buffer_load_dwordx4 v[192:195], v226, s[52:55], s58 offen offset:0 sc1\n"
"buffer_load_dwordx4 v[196:199], v226, s[52:55], s58 offen offset:2560 sc1\n"
"buffer_load_dwordx4 v[200:203], v226, s[52:55], s59 offen offset:0 sc1\n"
"buffer_load_dwordx4 v[204:207], v226, s[52:55], s59 offen offset:2560 sc1\n"
"s_waitcnt vmcnt(6)\n"
"v_pk_add_f32 v[246:247], v[176:177], v[180:181]\n"
"v_pk_add_f32 v[248:249], v[178:179], v[182:183]\n"
"s_waitcnt vmcnt(5)\n"
"v_pk_add_f32 v[246:247], v[246:247], v[184:185]\n"
"v_pk_add_f32 v[248:249], v[248:249], v[186:187]\n"
"s_waitcnt vmcnt(4)\n"
"v_pk_add_f32 v[246:247], v[246:247], v[188:189]\n"
"v_pk_add_f32 v[248:249], v[248:249], v[190:191]\n"
"s_waitcnt vmcnt(3)\n"
"v_pk_add_f32 v[246:247], v[246:247], v[192:193]\n"
"v_pk_add_f32 v[248:249], v[248:249], v[194:195]\n"
"s_waitcnt vmcnt(2)\n"
"v_pk_add_f32 v[246:247], v[246:247], v[196:197]\n"
"v_pk_add_f32 v[248:249], v[248:249], v[198:199]\n"
"s_waitcnt vmcnt(1)\n"
"v_pk_add_f32 v[246:247], v[246:247], v[200:201]\n"
"v_pk_add_f32 v[248:249], v[248:249], v[202:203]\n"
"s_waitcnt vmcnt(0)\n"
"v_pk_add_f32 v[246:247], v[246:247], v[204:205]\n"
"v_pk_add_f32 v[248:249], v[248:249], v[206:207]\n"
"v_cmp_u_f32 vcc, v246, v247\n"
"v_cmp_u_f32 s[88:89], v248, v249\n"
"s_or_b64 s[88:89], s[88:89], vcc\n"
"s_cbranch_scc1 .Lk_g_216\n"
".Lk_gok_217:\n"
"v_cvt_pk_bf16_f32 v246, 0, v246\n"
"v_cvt_pk_bf16_f32 v247, 0, v247\n"
"v_cvt_pk_bf16_f32 v248, 0, v248\n"
"v_cvt_pk_bf16_f32 v249, 0, v249\n"
"s_waitcnt lgkmcnt(0)\n"
"v_pk_add_f32 v[250:251], v[250:251], v[246:247]\n"
"v_pk_add_f32 v[252:253], v[252:253], v[248:249]\n"
"v_cvt_pk_bf16_f32 v250, 0, v250\n"
"v_cvt_pk_bf16_f32 v251, 0, v251\n"
"v_cvt_pk_bf16_f32 v252, 0, v252\n"
"v_cvt_pk_bf16_f32 v253, 0, v253\n"
"v_add_f32 v254, v250, v251\n"
"v_mul_f32 v255, v250, v250\n"
"v_add_f32 v216, v252, v253\n"
"v_fmac_f32 v255, v251, v251\n"
"v_add_f32 v254, v254, v216\n"
"v_fmac_f32 v255, v252, v252\n"
"v_fmac_f32 v255, v253, v253\n"
"ds_write_b128 v226, v[250:253] offset:2560\n"
"s_mov_b64 exec, -1\n"
"v_add_f32_dpp v254, v254, v254 row_ror:8 row_mask:0xf bank_mask:0xf\n"
"v_add_f32_dpp v255, v255, v255 row_ror:8 row_mask:0xf bank_mask:0xf\n"
"s_nop 0\n"
"v_add_f32_dpp v254, v254, v254 row_ror:4 row_mask:0xf bank_mask:0xf\n"
"v_add_f32_dpp v255, v255, v255 row_ror:4 row_mask:0xf bank_mask:0xf\n"
"s_nop 0\n"
"v_add_f32_dpp v254, v254, v254 row_ror:2 row_mask:0xf bank_mask:0xf\n"
"v_add_f32_dpp v255, v255, v255 row_ror:2 row_mask:0xf bank_mask:0xf\n"
"s_nop 0\n"
"v_add_f32_dpp v254, v254, v254 row_ror:1 row_mask:0xf bank_mask:0xf\n"
"v_add_f32_dpp v255, v255, v255 row_ror:1 row_mask:0xf bank_mask:0xf\n"
"s_nop 0\n"
"v_add_f32_dpp v254, v254, v254 row_bcast:15 row_mask:0xa bank_mask:0xf\n"
"v_add_f32_dpp v255, v255, v255 row_bcast:15 row_mask:0xa bank_mask:0xf\n"
"s_nop 0\n"
"v_add_f32_dpp v254, v254, v254 row_bcast:31 row_mask:0xc bank_mask:0xf\n"
"v_add_f32_dpp v255, v255, v255 row_bcast:31 row_mask:0xc bank_mask:0xf\n"
"s_mov_b32 exec_lo, 0\n"
"s_mov_b32 exec_hi, 0x80000000\n"
"ds_write_b64 v244, v[254:255] offset:18464\n"
"s_mov_b32 exec_lo, -1\n"
"s_mov_b32 exec_hi, 0xff\n"
"s_waitcnt lgkmcnt(0)\n"
"s_barrier\n"
"ds_read_b128 v[216:219], v243 offset:18464\n"
"ds_read_b128 v[220:223], v243 offset:18480\n"
"s_waitcnt lgkmcnt(1)\n"
"v_pk_add_f32 v[216:217], v[216:217], v[218:219]\n"
"s_waitcnt lgkmcnt(0)\n"
"v_pk_add_f32 v[220:221], v[220:221], v[222:223]\n"
"v_pk_add_f32 v[216:217], v[216:217], v[220:221]\n"
"v_mul_f32 v216, 0x3acccccd, v216\n"
"v_mul_f32 v217, 0x3acccccd, v217\n"
"v_cvt_pk_bf16_f32 v218, 0, v216\n"
"v_fma_f32 v217, -v216, v216, v217\n"
"v_sub_f32 v220, v250, v218\n"
"v_max_f32 v217, 0, v217\n"
"v_sub_f32 v221, v251, v218\n"
"v_cvt_pk_bf16_f32 v217, 0, v217\n"
"v_sub_f32 v222, v252, v218\n"
"v_add_f32 v217, 0x3727c5ac, v217\n"
"v_sub_f32 v223, v253, v218\n"
"v_cvt_pk_bf16_f32 v217, 0, v217\n"
"v_cvt_pk_bf16_f32 v220, 0, v220\n"
"v_sqrt_f32 v217, v217\n"
"v_cvt_pk_bf16_f32 v221, 0, v221\n"
"v_cvt_pk_bf16_f32 v217, 0, v217\n"
"v_cvt_pk_bf16_f32 v222, 0, v222\n"
"v_rcp_f32 v217, v217\n"
"v_cvt_pk_bf16_f32 v223, 0, v223\n"
"v_pk_mul_f32 v[220:221], v[220:221], v[216:217] op_sel:[0,1] op_sel_hi:[1,1]\n"
"v_pk_mul_f32 v[222:223], v[222:223], v[216:217] op_sel:[0,1] op_sel_hi:[1,1]\n"
"v_cvt_pk_bf16_f32 v220, v220, v221\n"
"v_cvt_pk_bf16_f32 v221, v222, v223\n"
"ds_write_b64 v227, v[220:221] offset:6400\n"
"s_mov_b64 exec, -1\n"
"s_cmp_ge_u32 s14, 27\n"
"s_cbranch_scc1 .Lk_ms_218\n"
"ds_read_b128 v[184:187], v228 offset:5120\n"
"ds_read_b128 v[188:191], v228 offset:5184\n"
"ds_read_b128 v[192:195], v228 offset:5248\n"
"ds_read_b128 v[196:199], v228 offset:5312\n"
"ds_read_b128 v[200:203], v228 offset:5376\n"
"s_mul_i32 s91, s14, 2560\n"
"s_add_u32 s91, s91, 834560\n"
"s_waitcnt lgkmcnt(4)\n"
"v_smfmac_f32_16x16x64_bf16 v[208:211], v[184:187], v[96:103], v225\n"
"v_smfmac_f32_16x16x64_bf16 v[212:215], v[184:187], v[104:111], v225\n"
"s_waitcnt lgkmcnt(3)\n"
"v_smfmac_f32_16x16x64_bf16 v[208:211], v[188:191], v[112:119], v225\n"
"v_smfmac_f32_16x16x64_bf16 v[212:215], v[188:191], v[120:127], v225\n"
"s_waitcnt lgkmcnt(2)\n"
"v_smfmac_f32_16x16x64_bf16 v[208:211], v[192:195], v[128:135], v225\n"
"v_smfmac_f32_16x16x64_bf16 v[212:215], v[192:195], v[136:143], v225\n"
"s_waitcnt lgkmcnt(1)\n"
"v_smfmac_f32_16x16x64_bf16 v[208:211], v[196:199], v[144:151], v225\n"
"v_smfmac_f32_16x16x64_bf16 v[212:215], v[196:199], v[152:159], v225\n"
"s_waitcnt lgkmcnt(0)\n"
"v_smfmac_f32_16x16x64_bf16 v[208:211], v[200:203], v[160:167], v225\n"
"v_smfmac_f32_16x16x64_bf16 v[212:215], v[200:203], v[168:175], v225\n"
"v_mov_b32 v184, 0\n"
"v_mov_b32 v185, 0\n"
"v_mov_b32 v188, 0\n"
"v_mov_b32 v189, 0\n"
"v_mov_b32 v192, 0\n"
"v_mov_b32 v193, 0\n"
"v_mov_b32 v196, 0\n"
"v_mov_b32 v197, 0\n"
"v_mov_b32 v200, 0\n"
"v_mov_b32 v201, 0\n"
"s_mov_b32 exec_lo, -1\n"
"s_mov_b32 exec_hi, 0\n"
"ds_write2_b64 v236, v[208:209], v[212:213] offset0:0 offset1:16\n"
"s_mov_b64 exec, -1\n"
"s_waitcnt lgkmcnt(0)\n"
"s_barrier\n"
"ds_read2st64_b64 v[216:219], v237 offset0:24 offset1:25\n"
"ds_read2st64_b64 v[220:223], v237 offset0:26 offset1:27\n"
"s_waitcnt lgkmcnt(1)\n"
"v_pk_add_f32 v[216:217], v[216:217], v[218:219]\n"
"s_waitcnt lgkmcnt(0)\n"
"v_pk_add_f32 v[220:221], v[220:221], v[222:223]\n"
"v_pk_add_f32 v[216:217], v[216:217], v[220:221]\n"
"v_cvt_pk_bf16_f32 v216, 0, v216\n"
"v_cvt_pk_bf16_f32 v217, 0, v217\n"
"v_mul_f32 v218, 0xbfb8aa3b, v216\n"
"v_mul_f32 v219, 0xbfb8aa3b, v217\n"
"v_exp_f32 v218, v218\n"
"v_exp_f32 v219, v219\n"
"v_add_f32 v218, 1.0, v218\n"
"v_add_f32 v219, 1.0, v219\n"
"v_rcp_f32 v218, v218\n"
"v_rcp_f32 v219, v219\n"
"s_nop 0\n"
"v_pk_mul_f32 v[216:217], v[216:217], v[218:219]\n"
"v_cvt_pk_bf16_f32 v216, v216, v217\n"
"ds_write_b32 v238, v216 offset:8704\n"
"ds_read_b128 v[176:179], v234 offset:8704\n"
"ds_read_b128 v[180:183], v234 offset:8768\n"
"s_waitcnt lgkmcnt(1)\n"
"v_smfmac_f32_16x16x64_bf16 v[184:187], v[176:179], a[160:167], v225\n"
"v_smfmac_f32_16x16x64_bf16 v[188:191], v[176:179], a[168:175], v225\n"
"v_smfmac_f32_16x16x64_bf16 v[192:195], v[176:179], a[176:183], v225\n"
"v_smfmac_f32_16x16x64_bf16 v[196:199], v[176:179], a[184:191], v225\n"
"v_smfmac_f32_16x16x64_bf16 v[200:203], v[176:179], a[192:199], v225\n"
"s_waitcnt lgkmcnt(0)\n"
"v_smfmac_f32_16x16x64_bf16 v[184:187], v[180:183], a[200:207], v225\n"
"v_smfmac_f32_16x16x64_bf16 v[188:191], v[180:183], a[208:215], v225\n"
"v_smfmac_f32_16x16x64_bf16 v[192:195], v[180:183], a[216:223], v225\n"
"v_smfmac_f32_16x16x64_bf16 v[196:199], v[180:183], a[224:231], v225\n"
"v_smfmac_f32_16x16x64_bf16 v[200:203], v[180:183], a[232:239], v225\n"
"s_mov_b32 exec_lo, 0xffffffff\n"
"s_mov_b32 exec_hi, 0\n"
"s_nop 1\n"
"v_min_u32 v184, -2, v184\n"
"v_min_u32 v185, -2, v185\n"
"buffer_store_dwordx2 v[184:185], v239, s[52:55], s91 offen offset:0 sc1\n"
"v_min_u32 v188, -2, v188\n"
"v_min_u32 v189, -2, v189\n"
"buffer_store_dwordx2 v[188:189], v239, s[52:55], s91 offen offset:128 sc1\n"
"v_min_u32 v192, -2, v192\n"
"v_min_u32 v193, -2, v193\n"
"buffer_store_dwordx2 v[192:193], v239, s[52:55], s91 offen offset:256 sc1\n"
"v_min_u32 v196, -2, v196\n"
"v_min_u32 v197, -2, v197\n"
"buffer_store_dwordx2 v[196:197], v239, s[52:55], s91 offen offset:384 sc1\n"
"v_min_u32 v200, -2, v200\n"
"v_min_u32 v201, -2, v201\n"
"buffer_store_dwordx2 v[200:201], v239, s[52:55], s91 offen offset:512 sc1\n"
"s_mov_b64 exec, -1\n"
"s_branch .Lk_mj_219\n"
".Lk_ms_218:\n"
"s_sleep 24\n"
"s_nop 1\n"
".Lk_mj_219:\n"
"s_bfe_u32 s88, s14, 0x20002\n"
"s_cmp_lg_u32 s88, s15\n"
"s_cbranch_scc1 .Lk_done_220\n"
"s_lshr_b32 s92, s14, 4\n"
"s_mul_i32 s88, s92, 2560\n"
"v_add_u32 v254, s88, v226\n"
"s_mul_i32 s90, s92, 69120\n"
"s_add_u32 s90, s90, 834560\n"
"s_and_b32 s88, s14, 3\n"
"s_mul_i32 s88, s88, 10\n"
"v_and_b32 v208, 15, v224\n"
"v_add_u32 v208, s88, v208\n"
"v_lshrrev_b32 v209, 4, v224\n"
"s_and_b32 s88, s15, 1\n"
"s_mul_i32 s89, s15, 320\n"
"s_lshl_b32 s93, s88, 6\n"
"s_add_u32 s93, s93, s89\n"
"s_xor_b32 s88, s88, 1\n"
"s_lshl_b32 s88, s88, 8\n"
"s_add_u32 s88, s88, s89\n"
"v_and_b32 v206, 7, v208\n"
"v_lshrrev_b32 v207, 3, v208\n"
"v_lshlrev_b32 v207, 2, v207\n"
"v_lshl_add_u32 v207, v206, 5, v207\n"
"v_lshlrev_b32 v206, 3, v206\n"
"v_add_u32 v207, s93, v207\n"
"v_add_u32 v206, s88, v206\n"
"v_cmp_gt_u32 vcc, 32, v208\n"
"s_nop 1\n"
"v_cndmask_b32 v206, v206, v207, vcc\n"
"v_cndmask_b32 v207, 4, 16, vcc\n"
"v_add_u32 v207, v206, v207\n"
"v_cmp_gt_u32 vcc, 32, v224\n"
"s_nop 1\n"
"v_cndmask_b32 v206, v207, v206, vcc\n"
"v_and_b32 v207, 1, v209\n"
"v_lshl_add_u32 v206, v207, 1, v206\n"
"s_mul_i32 s89, s15, 640\n"
"v_lshl_add_u32 v208, v208, 4, s89\n"
"s_mul_i32 s89, s92, 2560\n"
"v_lshl_add_u32 v207, v209, 2, v208\n"
"v_add_u32 v207, s89, v207\n"
"v_mul_u32_u24 v209, 2560, v209\n"
"v_add_u32 v208, v208, v209\n"
"v_mov_b64 v[120:121], 0\n"
"v_mov_b64 v[122:123], 0\n"
"s_mul_i32 s92, s92, 1280\n"
"s_mov_b32 s40, s30\n"
"s_and_b32 s41, s31, 0xffff\n"
"s_mov_b32 s42, 2560\n"
"s_mov_b32 s43, 0x20000\n"
"s_mov_b32 exec_lo, 0x03ff03ff\n"
"s_mov_b32 exec_hi, 0x03ff03ff\n"
"ds_read_b32 v250, v207 offset:0\n"
"s_add_u32 s56, s90, 0\n"
"s_add_u32 s57, s90, 10240\n"
"s_add_u32 s58, s90, 20480\n"
"s_add_u32 s59, s90, 30720\n"
"s_add_u32 s60, s90, 40960\n"
"s_add_u32 s61, s90, 51200\n"
"s_add_u32 s62, s90, 61440\n"
"s_waitcnt lgkmcnt(0)\n"
"s_sleep 1\n"
"s_nop 1\n"
".Lk_f_221:\n"
"s_cmp_eq_u32 s96, 0\n"
"s_cbranch_scc1 .Lk_fok_222\n"
"s_sub_u32 s96, s96, 1\n"
"s_mov_b32 exec_hi, 0x03ff\n"
"buffer_load_dwordx4 v[120:123], v208, s[52:55], s62 offen sc1\n"
"s_mov_b32 exec_hi, 0x03ff03ff\n"
"buffer_load_dwordx4 v[96:99], v208, s[52:55], s56 offen sc1\n"
"buffer_load_dwordx4 v[100:103], v208, s[52:55], s57 offen sc1\n"
"buffer_load_dwordx4 v[104:107], v208, s[52:55], s58 offen sc1\n"
"buffer_load_dwordx4 v[108:111], v208, s[52:55], s59 offen sc1\n"
"buffer_load_dwordx4 v[112:115], v208, s[52:55], s60 offen sc1\n"
"buffer_load_dwordx4 v[116:119], v208, s[52:55], s61 offen sc1\n"
"s_waitcnt vmcnt(5)\n"
"v_pk_add_f32 v[246:247], v[120:121], v[96:97]\n"
"v_pk_add_f32 v[248:249], v[122:123], v[98:99]\n"
"s_waitcnt vmcnt(4)\n"
"v_pk_add_f32 v[246:247], v[246:247], v[100:101]\n"
"v_pk_add_f32 v[248:249], v[248:249], v[102:103]\n"
"s_waitcnt vmcnt(3)\n"
"v_pk_add_f32 v[246:247], v[246:247], v[104:105]\n"
"v_pk_add_f32 v[248:249], v[248:249], v[106:107]\n"
"s_waitcnt vmcnt(2)\n"
"v_pk_add_f32 v[246:247], v[246:247], v[108:109]\n"
"v_pk_add_f32 v[248:249], v[248:249], v[110:111]\n"
"s_waitcnt vmcnt(1)\n"
"v_pk_add_f32 v[246:247], v[246:247], v[112:113]\n"
"v_pk_add_f32 v[248:249], v[248:249], v[114:115]\n"
"s_waitcnt vmcnt(0)\n"
"v_pk_add_f32 v[246:247], v[246:247], v[116:117]\n"
"v_pk_add_f32 v[248:249], v[248:249], v[118:119]\n"
"s_nop 1\n"
"v_permlane32_swap_b32 v246, v248\n"
"v_permlane32_swap_b32 v247, v249\n"
"v_pk_add_f32 v[246:247], v[246:247], v[248:249]\n"
"s_nop 1\n"
"v_permlane16_swap_b32 v246, v247\n"
"v_add_f32 v246, v246, v247\n"
"v_cmp_u_f32 vcc, v246, v246\n"
"s_cbranch_vccnz .Lk_f_221\n"
".Lk_fok_222:\n"
"v_cvt_pk_bf16_f32 v246, 0, v246\n"
"v_add_f32 v250, v250, v246\n"
"v_cvt_pk_bf16_f32 v250, v250, v250\n"
"buffer_store_short v250, v206, s[40:43], s92 offen\n"
"s_cmp_lg_u32 s14, 0\n"
"s_cbranch_scc1 .Lk_done_220\n"
"v_mov_b32 v246, s86\n"
"v_mov_b32 v247, s87\n"
"s_mov_b64 exec, 1\n"
"buffer_store_dwordx2 v[246:247], off, s[52:55], 0 sc1\n"
".Lk_done_220:\n"
".Lk_end:\n"
  :: "s"(__builtin_amdgcn_kernarg_segment_ptr()), "s"(blockIdx.x), "s"(wave), "s"((unsigned)(size_t)lds)
  : "v0", "v1", "v2", "v3", "v4", "v5", "v6", "v7", "v8", "v9", "v10", "v11", "v12", "v13", "v14", "v15", "v16", "v17", "v18", "v19", "v20", "v21", "v22", "v23", "v24", "v25", "v26", "v27", "v28", "v29", "v30", "v31", "v32", "v33", "v34", "v35", "v36", "v37", "v38", "v39", "v40", "v41", "v42", "v43", "v44", "v45", "v46", "v47", "v48", "v49", "v50", "v51", "v52", "v53", "v54", "v55", "v56", "v57", "v58", "v59", "v60", "v61", "v62", "v63", "v64", "v65", "v66", "v67", "v68", "v69", "v70", "v71", "v72", "v73", "v74", "v75", "v76", "v77", "v78", "v79", "v80", "v81", "v82", "v83", "v84", "v85", "v86", "v87", "v88", "v89", "v90", "v91", "v92", "v93", "v94", "v95", "v96", "v97", "v98", "v99", "v100", "v101", "v102", "v103", "v104", "v105", "v106", "v107", "v108", "v109", "v110", "v111", "v112", "v113", "v114", "v115", "v116", "v117", "v118", "v119", "v120", "v121", "v122", "v123", "v124", "v125", "v126", "v127", "v128", "v129", "v130", "v131", "v132", "v133", "v134", "v135", "v136", "v137", "v138", "v139", "v140", "v141", "v142", "v143", "v144", "v145", "v146", "v147", "v148", "v149", "v150", "v151", "v152", "v153", "v154", "v155", "v156", "v157", "v158", "v159", "v160", "v161", "v162", "v163", "v164", "v165", "v166", "v167", "v168", "v169", "v170", "v171", "v172", "v173", "v174", "v175", "v176", "v177", "v178", "v179", "v180", "v181", "v182", "v183", "v184", "v185", "v186", "v187", "v188", "v189", "v190", "v191", "v192", "v193", "v194", "v195", "v196", "v197", "v198", "v199", "v200", "v201", "v202", "v203", "v204", "v205", "v206", "v207", "v208", "v209", "v210", "v211", "v212", "v213", "v214", "v215", "v216", "v217", "v218", "v219", "v220", "v221", "v222", "v223", "v224", "v225", "v226", "v227", "v228", "v229", "v230", "v231", "v232", "v233", "v234", "v235", "v236", "v237", "v238", "v239", "v240", "v241", "v242", "v243", "v244", "v245", "v246", "v247", "v248", "v249", "v250", "v251", "v252", "v253", "v254", "v255", "a0", "a1", "a2", "a3", "a4", "a5", "a6", "a7", "a8", "a9", "a10", "a11", "a12", "a13", "a14", "a15", "a16", "a17", "a18", "a19", "a20", "a21", "a22", "a23", "a24", "a25", "a26", "a27", "a28", "a29", "a30", "a31", "a32", "a33", "a34", "a35", "a36", "a37", "a38", "a39", "a40", "a41", "a42", "a43", "a44", "a45", "a46", "a47", "a48", "a49", "a50", "a51", "a52", "a53", "a54", "a55", "a56", "a57", "a58", "a59", "a60", "a61", "a62", "a63", "a64", "a65", "a66", "a67", "a68", "a69", "a70", "a71", "a72", "a73", "a74", "a75", "a76", "a77", "a78", "a79", "a80", "a81", "a82", "a83", "a84", "a85", "a86", "a87", "a88", "a89", "a90", "a91", "a92", "a93", "a94", "a95", "a96", "a97", "a98", "a99", "a100", "a101", "a102", "a103", "a104", "a105", "a106", "a107", "a108", "a109", "a110", "a111", "a112", "a113", "a114", "a115", "a116", "a117", "a118", "a119", "a120", "a121", "a122", "a123", "a124", "a125", "a126", "a127", "a128", "a129", "a130", "a131", "a132", "a133", "a134", "a135", "a136", "a137", "a138", "a139", "a140", "a141", "a142", "a143", "a144", "a145", "a146", "a147", "a148", "a149", "a150", "a151", "a152", "a153", "a154", "a155", "a156", "a157", "a158", "a159", "a160", "a161", "a162", "a163", "a164", "a165", "a166", "a167", "a168", "a169", "a170", "a171", "a172", "a173", "a174", "a175", "a176", "a177", "a178", "a179", "a180", "a181", "a182", "a183", "a184", "a185", "a186", "a187", "a188", "a189", "a190", "a191", "a192", "a193", "a194", "a195", "a196", "a197", "a198", "a199", "a200", "a201", "a202", "a203", "a204", "a205", "a206", "a207", "a208", "a209", "a210", "a211", "a212", "a213", "a214", "a215", "a216", "a217", "a218", "a219", "a220", "a221", "a222", "a223", "a224", "a225", "a226", "a227", "a228", "a229", "a230", "a231", "a232", "a233", "a234", "a235", "a236", "a237", "a238", "a239", "a240", "a241", "a242", "a243", "a244", "a245", "a246", "a247", "a248", "a249", "a250", "a251", "a252", "a253", "a254", "a255", "s8", "s9", "s10", "s11", "s12", "s13", "s14", "s15", "s16", "s17", "s18", "s19", "s20", "s21", "s22", "s23", "s24", "s25", "s26", "s27", "s28", "s29", "s30", "s31", "s33", "s34", "s35", "s36", "s37", "s38", "s39", "s40", "s41", "s42", "s43", "s44", "s45", "s46", "s47", "s48", "s49", "s50", "s51", "s52", "s53", "s54", "s55", "s56", "s57", "s58", "s59", "s60", "s61", "s62", "s63", "s64", "s65", "s66", "s67", "s68", "s69", "s70", "s71", "s72", "s73", "s74", "s75", "s76", "s77", "s78", "s79", "s80", "s81", "s82", "s83", "s84", "s85", "s86", "s87", "s88", "s89", "s90", "s91", "s92", "s93", "s94", "s95", "s96", "s97", "s98", "s99", "vcc", "scc", "memory");
}

extern "C" void kernel_launch(void* const* d_in, const int* in_sizes, int n_in, void* d_out, int out_size, void* d_ws, size_t ws_size,
                              hipStream_t stream) {
  fused_decode_kernel<<<256, 256, 0, stream>>>(d_in[0], d_in[1], d_in[2], d_in[3], d_in[4], d_in[6], d_in[7], d_out, d_ws);
}
```
